```python
import torch

def _softmax(x, axis=-1):
    return torch.softmax(x, dim=axis)
def _gelu(x):
    return torch.nn.functional.gelu(x, approximate="tanh")
def _silu(x):
    return torch.nn.functional.silu(x)
def _sigmoid(x):
    return torch.sigmoid(x)
def _layernorm(x, axis=-1, eps=1e-5):
    m = x.mean(dim=axis, keepdim=True)
    v = x.var(dim=axis, keepdim=True, unbiased=False)
    return (x - m) / torch.sqrt(v + eps)
def _scatter_add(tgt, idx, src, axis=0):
    return tgt.clone().index_add_(axis, idx, src)
def _segment_sum(src, seg, n):
    out = torch.zeros((n,) + src.shape[1:], dtype=src.dtype)
    return out.index_add_(0, seg, src)
def _pad(x, pw):
    flat = tuple(v for pair in reversed(pw) for v in pair)
    return torch.nn.functional.pad(x, flat)

kernel_name = 'concat_heads__mha_block_cached__mha_block_cached__near_ridge'

def setup_inputs():

    _w = (lambda a: (lambda t: t.to(torch.bfloat16) if t.is_floating_point() else t)(torch.as_tensor(a)))
    def _A_setup():
        B, Sq, D = 16, 160, 512
        H, Sk, Dh = 32, 512, 128
        return {
            "x": _w((lambda *s: torch.randn(*s, dtype=torch.bfloat16) * 0.8)(B, Sq, D)),
            "k_cache": _w((lambda *s: torch.randn(*s, dtype=torch.bfloat16) * 0.8)(B, H, Sk, Dh)),
            "v_cache": _w((lambda *s: torch.randn(*s, dtype=torch.bfloat16) * 0.05)(B, H, Sk, Dh)),
            "wq": _w((lambda *s: torch.randn(*s, dtype=torch.bfloat16) * 0.8)(D, H * Dh) * (D ** -0.5)),
            "wo": _w((lambda *s: torch.randn(*s, dtype=torch.bfloat16) * 0.05)(H * Dh, D) * ((H * Dh) ** -0.5)),
        }
    _A_in = _A_setup()

    def _B_setup():
        B, Sq, D = 16, 160, 512
        H, Sk, Dh = 32, 512, 128
        return {
            "x": _w((lambda *s: torch.randn(*s, dtype=torch.bfloat16) * 0.8)(B, Sq, D)),
            "k_cache": _w((lambda *s: torch.randn(*s, dtype=torch.bfloat16) * 0.8)(B, H, Sk, Dh)),
            "v_cache": _w((lambda *s: torch.randn(*s, dtype=torch.bfloat16) * 0.05)(B, H, Sk, Dh)),
            "wq": _w((lambda *s: torch.randn(*s, dtype=torch.bfloat16) * 0.8)(D, H * Dh) * (D ** -0.5)),
            "wo": _w((lambda *s: torch.randn(*s, dtype=torch.bfloat16) * 0.05)(H * Dh, D) * ((H * Dh) ** -0.5)),
        }
    _B_in = _B_setup()

    return {
        "A_x": _A_in["x"],
        "A_k_cache": _A_in["k_cache"],
        "A_v_cache": _A_in["v_cache"],
        "A_wq": _A_in["wq"],
        "A_wo": _A_in["wo"],
        "B_x": _B_in["x"],
        "B_k_cache": _B_in["k_cache"],
        "B_v_cache": _B_in["v_cache"],
        "B_wq": _B_in["wq"],
        "B_wo": _B_in["wo"],
    }

def reference(A_x, A_k_cache, A_v_cache, A_wq, A_wo, B_x, B_k_cache, B_v_cache, B_wq, B_wo):
    def _A_ref(x, k_cache, v_cache, wq, wo):
        B, Sq, D = x.shape
        H, Dh = 32, 128
        q = (lambda x, *d: x.permute(*d))((x @ wq).reshape(B, Sq, H, Dh), 0, 2, 1, 3)
        scores = torch.einsum("bhqd,bhkd->bhqk", q, k_cache) * (Dh ** -0.5)
        p = _softmax(scores, axis=-1)
        o = torch.einsum("bhqk,bhkd->bhqd", p, v_cache)
        return (lambda x, *d: x.permute(*d))(o, 0, 2, 1, 3).reshape(B, Sq, H * Dh) @ wo

    def _B_ref(x, k_cache, v_cache, wq, wo):
        B, Sq, D = x.shape
        H, Dh = 32, 128
        q = (lambda x, *d: x.permute(*d))((x @ wq).reshape(B, Sq, H, Dh), 0, 2, 1, 3)
        scores = torch.einsum("bhqd,bhkd->bhqk", q, k_cache) * (Dh ** -0.5)
        p = _softmax(scores, axis=-1)
        o = torch.einsum("bhqk,bhkd->bhqd", p, v_cache)
        return (lambda x, *d: x.permute(*d))(o, 0, 2, 1, 3).reshape(B, Sq, H * Dh) @ wo

    a = _A_ref(A_x, A_k_cache, A_v_cache, A_wq, A_wo)
    b = _B_ref(B_x, B_k_cache, B_v_cache, B_wq, B_wo)
    return torch.concatenate([a, b], axis=-1)
```

```cpp
#include <hip/hip_runtime.h>
#include <hip/hip_bf16.h>
typedef unsigned short u16;
__device__ __forceinline__ float b2f(u16 x){ return __uint_as_float(((unsigned)x)<<16); }
__device__ __forceinline__ u16 f2b(float f){ __hip_bfloat16 h = __float2bfloat16(f); return *(u16*)&h; }
__global__ __launch_bounds__(256) void gemm(const u16* A, const u16* B, u16* C, int M, int N, int K, int ldc){
  __shared__ float As[16][65], Bs[16][65];
  int tx=threadIdx.x&15, ty=threadIdx.x>>4; int m0=blockIdx.y*64, n0=blockIdx.x*64;
  float acc[4][4]={};
  for(int k0=0;k0<K;k0+=16){
    for(int i=threadIdx.x;i<1024;i+=256){ int r=i>>4,c=i&15; As[c][r]=b2f(A[(long)(m0+r)*K+k0+c]); }
    for(int i=threadIdx.x;i<1024;i+=256){ int r=i>>6,c=i&63; Bs[r][c]=b2f(B[(long)(k0+r)*N+n0+c]); }
    __syncthreads();
    for(int k=0;k<16;k++){ float a[4],b[4];
      for(int i=0;i<4;i++){a[i]=As[k][ty*4+i]; b[i]=Bs[k][tx*4+i];}
      for(int i=0;i<4;i++)for(int j=0;j<4;j++)acc[i][j]+=a[i]*b[j]; }
    __syncthreads();
  }
  for(int i=0;i<4;i++)for(int j=0;j<4;j++) C[(long)(m0+ty*4+i)*ldc+n0+tx*4+j]=f2b(acc[i][j]);
}
__global__ __launch_bounds__(256) void attn(const u16* Q, const u16* Kc, const u16* Vc, u16* O){
  int qb=blockIdx.x, h=blockIdx.y, b=blockIdx.z;
  __shared__ float q[8][128]; __shared__ float s[8][512]; __shared__ float red[8];
  for(int i=threadIdx.x;i<8*128;i+=256){ int r=i>>7,c=i&127; q[r][c]=b2f(Q[(long)(b*160+qb*8+r)*4096+h*128+c]); }
  __syncthreads();
  const u16* Kp=Kc+(long)(b*32+h)*512*128; const u16* Vp=Vc+(long)(b*32+h)*512*128;
  for(int kk=threadIdx.x;kk<512;kk+=256){ float a[8]={};
    for(int d=0;d<128;d++){ float kv=b2f(Kp[kk*128+d]); for(int r=0;r<8;r++) a[r]+=q[r][d]*kv; }
    for(int r=0;r<8;r++) s[r][kk]=a[r]*0.08838834764831845f; }
  __syncthreads();
  if(threadIdx.x<8){ int r=threadIdx.x; float m=-1e30f; for(int k=0;k<512;k++) m=fmaxf(m,s[r][k]); float l=0; for(int k=0;k<512;k++){ float p=__expf(s[r][k]-m); s[r][k]=p; l+=p;} red[r]=1.f/l; }
  __syncthreads();
  int d=threadIdx.x&127, rh=threadIdx.x>>7; float a[4]={};
  for(int k=0;k<512;k++){ float v=b2f(Vp[k*128+d]); for(int r=0;r<4;r++) a[r]+=s[rh*4+r][k]*v; }
  for(int r=0;r<4;r++) O[(long)(b*160+qb*8+rh*4+r)*4096+h*128+d]=f2b(a[r]*red[rh*4+r]);
}
extern "C" void kernel_launch(void* const* d_in, const int* in_sizes, int n_in, void* d_out, int out_size, void* d_ws, size_t ws_size, hipStream_t stream){
  u16* ws=(u16*)d_ws; u16* out=(u16*)d_out;
  for(int br=0;br<2;br++){
    const u16* x=(const u16*)d_in[br*5+0]; const u16* k=(const u16*)d_in[br*5+1]; const u16* v=(const u16*)d_in[br*5+2];
    const u16* wq=(const u16*)d_in[br*5+3]; const u16* wo=(const u16*)d_in[br*5+4];
    u16* Q=ws+(long)br*2*2560*4096; u16* O=Q+(long)2560*4096;
    gemm<<<dim3(64,40),256,0,stream>>>(x,wq,Q,2560,4096,512,4096);
    attn<<<dim3(20,32,16),256,0,stream>>>(Q,k,v,O);
    gemm<<<dim3(8,40),256,0,stream>>>(O,wo,out+br*512,2560,512,4096,1024);
  }
}
```

```cpp
#include <hip/hip_runtime.h>
struct MhaArgs { const void* p[12]; };
extern "C" __global__ __launch_bounds__(256, 1) void mha_fused(MhaArgs args) {
  __shared__ __attribute__((aligned(16))) char lds[163840];
  asm volatile("" :: "v"(lds));
  asm volatile(
R"(.macro M d,a,b,c
v_mfma_f32_16x16x32_bf16 \d,\a,\b,\c
.endm
.macro T d,a,o
ds_read_b64_tr_b16 \d,\a offset:\o
.endm
.macro D d,a,o
ds_read_b128 \d,\a offset:\o
.endm
.macro C d,a,b
v_cvt_pk_bf16_f32 \d,\a,\b
.endm
.macro R d,a
v_accvgpr_read_b32 \d,\a
.endm
.macro E d,a
v_exp_f32 \d,\a
.endm
L_code0:
s_getpc_b64 s[8:9]
s_mov_b64 s[20:21], %0
s_mov_b32 s22, %2
s_and_b32 s31, s22, 7
s_lshr_b32 s29, s31, 2
s_mul_i32 s23, s29, 40
s_add_u32 s96, s20, s23
s_addc_u32 s97, s21, 0
s_load_dwordx8 s[64:71], s[96:97], 0
s_load_dwordx2 s[72:73], s[96:97], 32
s_load_dwordx4 s[76:79], s[20:21], 80
s_and_b32 s27, s31, 3
s_lshl_b32 s27, s27, 1
s_lshr_b32 s31, s22, 3
s_lshr_b32 s28, s31, 1
s_and_b32 s31, s31, 1
s_add_u32 s27, s27, s31
s_lshl_b32 s30, s29, 4
s_add_u32 s30, s30, s28
s_mov_b32 s10, L_code_end-L_code0-4
s_mov_b32 s11, 131072
v_lshrrev_b32 v15, 6, v0
v_and_b32 v0, 63, v0
v_readfirstlane_b32 s24, v15
v_lshlrev_b32 v13, 14, v0
s_lshr_b32 s31, s22, 3
s_lshl_b32 s31, s31, 2
s_add_u32 s31, s31, s24
s_lshl_b32 s31, s31, 7
v_add_u32 v13, s31, v13
s_mov_b32 s23, L_code0-L_code0
buffer_load_dword v14, v13, s[8:11], s23 offen
v_bfe_u32 v16, v0, 2, 3
v_lshrrev_b32 v17, 5, v0
v_and_b32 v18, 3, v0
v_lshlrev_b32 v9, 13, v16
v_lshl_add_u32 v9, v17, 6, v9
v_lshl_add_u32 v9, v18, 4, v9
v_lshrrev_b32 v10, 3, v0
v_and_b32 v15, 6, v10
v_and_b32 v20, 7, v0
v_xor_b32 v15, v15, v20
v_lshlrev_b32 v15, 4, v15
v_lshl_add_u32 v19, v10, 8, v15
v_lshl_add_u32 v10, v10, 10, v15
s_lshr_b32 s25, s24, 1
s_and_b32 s26, s24, 1
s_lshl_b32 s23, s25, 3
s_add_u32 s23, s23, s27
s_waitcnt lgkmcnt(0)
s_mov_b32 s40, s70
s_mov_b32 s41, s71
s_mov_b32 s42, 4194304
s_mov_b32 s43, 131072
s_mul_i32 s31, s28, 163840
s_add_u32 s36, s64, s31
s_addc_u32 s37, s65, 0
s_mov_b32 s38, 163840
s_mov_b32 s39, 131072
s_mov_b64 s[84:85], s[72:73]
s_mov_b64 s[86:87], s[76:77]
s_mov_b64 s[88:89], s[78:79]
s_mov_b64 s[12:13], s[66:67]
s_mov_b64 s[14:15], s[68:69]
s_lshl_b32 s78, s23, 8
s_lshl_b32 s31, s26, 18
s_add_u32 s78, s78, s31
s_mul_i32 s79, s24, 40960
s_mul_i32 s74, s24, 8448
s_mul_i32 s80, s24, 5120
s_add_u32 s80, s80, 101376
s_mov_b32 s75, s80
s_add_u32 s95, s80, 61440
s_mov_b32 s64, 67584
s_bitcmp1_b32 s22, 4
s_cselect_b32 s64, 33792, s64
s_mov_b32 s66, -61440
s_bitcmp1_b32 s22, 4
s_cselect_b32 s66, 458752, s66
s_mov_b32 s65, s64
s_mov_b32 m0, s75
s_nop 0
buffer_load_dwordx4 v10, s[36:39], s79 offen lds
s_add_u32 s71, s79, 7168
buffer_load_dwordx4 v10, s[36:39], s71 offen offset:1024 lds
s_add_u32 s72, s79, 14336
buffer_load_dwordx4 v10, s[36:39], s72 offen offset:2048 lds
s_add_u32 s71, s79, 21504
buffer_load_dwordx4 v10, s[36:39], s71 offen offset:3072 lds
s_add_u32 s72, s79, 32768
s_add_u32 m0, s75, 4096
s_nop 0
buffer_load_dwordx4 v10, s[36:39], s72 offen lds
s_add_u32 s79, s79, 128
s_add_u32 s75, s75, 20480
s_cmp_eq_u32 s75, s95
s_cselect_b32 s75, s80, s75
s_add_u32 s70, s74, s64
s_mov_b32 m0, s70
s_nop 0
buffer_load_dwordx4 v9, s[40:43], s78 offen lds
s_add_u32 s71, s78, 129024
buffer_load_dwordx4 v9, s[40:43], s71 offen offset:2048 lds
s_add_u32 m0, s70, 896
s_nop 0
buffer_load_dwordx4 v9, s[40:43], s78 offen offset:128 lds
buffer_load_dwordx4 v9, s[40:43], s71 offen offset:2176 lds
s_add_u32 s78, s78, 65536
s_add_u32 m0, s70, 4104
s_nop 0
buffer_load_dwordx4 v9, s[40:43], s78 offen lds
s_add_u32 s71, s78, 129024
buffer_load_dwordx4 v9, s[40:43], s71 offen offset:2048 lds
s_add_u32 m0, s70, 5000
s_nop 0
buffer_load_dwordx4 v9, s[40:43], s78 offen offset:128 lds
s_add_u32 s78, s78, s66
buffer_load_dwordx4 v9, s[40:43], s71 offen offset:2176 lds
s_mov_b32 s64, 0
s_bitcmp1_b32 s22, 4
s_cselect_b32 s64, 67584, s64
s_mov_b32 s66, 454656
s_bitcmp1_b32 s22, 4
s_cselect_b32 s66, 458752, s66
s_mov_b32 m0, s75
s_nop 0
buffer_load_dwordx4 v10, s[36:39], s79 offen lds
s_add_u32 s71, s79, 7168
buffer_load_dwordx4 v10, s[36:39], s71 offen offset:1024 lds
s_add_u32 s72, s79, 14336
buffer_load_dwordx4 v10, s[36:39], s72 offen offset:2048 lds
s_add_u32 s71, s79, 21504
buffer_load_dwordx4 v10, s[36:39], s71 offen offset:3072 lds
s_add_u32 s72, s79, 32768
s_add_u32 m0, s75, 4096
s_nop 0
buffer_load_dwordx4 v10, s[36:39], s72 offen lds
s_add_u32 s79, s79, 128
s_add_u32 s75, s75, 20480
s_cmp_eq_u32 s75, s95
s_cselect_b32 s75, s80, s75
s_add_u32 s70, s74, s64
s_mov_b32 m0, s70
s_nop 0
buffer_load_dwordx4 v9, s[40:43], s78 offen lds
s_add_u32 s71, s78, 129024
buffer_load_dwordx4 v9, s[40:43], s71 offen offset:2048 lds
s_add_u32 m0, s70, 896
s_nop 0
buffer_load_dwordx4 v9, s[40:43], s78 offen offset:128 lds
buffer_load_dwordx4 v9, s[40:43], s71 offen offset:2176 lds
s_add_u32 s78, s78, 65536
s_add_u32 m0, s70, 4104
s_nop 0
buffer_load_dwordx4 v9, s[40:43], s78 offen lds
s_add_u32 s71, s78, 129024
buffer_load_dwordx4 v9, s[40:43], s71 offen offset:2048 lds
s_add_u32 m0, s70, 5000
s_nop 0
buffer_load_dwordx4 v9, s[40:43], s78 offen offset:128 lds
s_add_u32 s78, s78, s66
buffer_load_dwordx4 v9, s[40:43], s71 offen offset:2176 lds
s_lshl_b32 s23, s25, 3
s_add_u32 s23, s23, s27
s_mov_b32 s48, s84
s_mov_b32 s49, s85
s_mov_b32 s50, 4194304
s_mov_b32 s51, 131072
s_lshl_b32 s31, s28, 5
s_add_u32 s31, s31, s23
s_lshl_b32 s31, s31, 17
s_add_u32 s12, s12, s31
s_addc_u32 s13, s13, 0
s_add_u32 s14, s14, s31
s_addc_u32 s15, s15, 0
s_cmp_eq_u32 s26, 0
s_cselect_b32 s44, s12, s14
s_cselect_b32 s45, s13, s15
s_mov_b32 s46, 2228224
s_mov_b32 s47, 131072
s_mul_i32 s31, s28, 327680
s_lshl_b32 s96, s29, 10
s_lshl_b32 s97, s27, 7
s_add_u32 s96, s96, s97
s_lshl_b32 s97, s25, 6
s_add_u32 s96, s96, s97
s_add_u32 s31, s31, s96
s_add_u32 s56, s86, s31
s_addc_u32 s57, s87, 0
s_mov_b32 s58, 0
s_mov_b32 s59, 131072
s_sub_u32 s58, 327680, s96
s_mov_b32 s60, s88
s_mov_b32 s61, s89
s_mov_b32 s62, 65536
s_mov_b32 s63, 131072
s_mov_b32 s31, 65536
s_add_u32 s52, s88, s31
s_addc_u32 s53, s89, 0
s_mov_b32 s54, 41943040
s_mov_b32 s55, 131072
s_lshl_b32 s93, s23, 17
s_lshl_b32 s31, s26, 16
s_add_u32 s93, s93, s31
s_lshl_b32 s73, s24, 13
s_lshl_b32 s77, s27, 1
s_add_u32 s77, s77, s25
s_xor_b32 s77, s77, s28
s_and_b32 s77, s77, 15
s_lshl_b32 s77, s77, 13
s_mov_b32 s4, s77
s_mov_b32 s6, s77
s_mov_b32 s5, 0
s_mov_b32 s92, 0x3e0293ee
s_add_u32 s91, s27, 1
s_and_b32 s91, s91, 7
v_lshrrev_b32 v15, 2, v16
v_lshlrev_b32 v15, 1, v15
v_xor_b32 v15, v15, v18
v_lshlrev_b32 v15, 4, v15
v_lshl_add_u32 v15, v17, 6, v15
v_lshl_add_u32 v8, v16, 8, v15
s_cmp_eq_u32 s26, 0
s_cselect_b64 vcc, -1, 0
s_nop 1
v_cndmask_b32 v8, v8, v19, vcc
v_add_u32 v15, s91, v0
v_and_b32 v15, 7, v15
v_lshlrev_b32 v15, 2, v15
s_lshl_b32 s31, s30, 7
s_add_u32 s31, s31, 32768
buffer_load_dword v12, v15, s[60:63], s31 offen sc1
v_and_b32 v21, 15, v0
v_lshrrev_b32 v22, 4, v0
v_bfe_u32 v23, v0, 2, 2
v_and_b32 v24, 3, v0
v_lshlrev_b32 v1, 4, v0
s_lshl_b32 s31, s25, 14
v_and_b32 v16, 6, v21
v_xor_b32 v2, v22, v16
v_lshlrev_b32 v2, 4, v2
v_and_b32 v16, 7, v21
v_lshl_add_u32 v2, v16, 7, v2
v_lshrrev_b32 v16, 3, v21
v_lshl_add_u32 v7, v16, 10, v2
v_lshl_add_u32 v2, v16, 11, v2
v_add_u32 v2, s31, v2
v_xor_b32 v3, 64, v2
s_add_u32 s31, s31, 8192
v_and_b32 v16, 1, v22
v_mul_u32_u24 v4, 288, v16
v_lshl_add_u32 v4, v23, 6, v4
v_lshl_add_u32 v4, v24, 3, v4
v_lshrrev_b32 v17, 1, v22
v_lshl_add_u32 v4, v17, 11, v4
v_add_u32 v4, s31, v4
v_xor_b32 v5, 32, v4
s_mul_i32 s31, s25, 16896
s_add_u32 s31, s31, s65
v_mul_u32_u24 v6, 4104, v16
v_lshl_add_u32 v6, v17, 11, v6
v_lshl_add_u32 v6, v23, 6, v6
v_lshl_add_u32 v6, v24, 4, v6
v_add_u32 v6, s31, v6
s_mul_i32 s31, s26, 10240
s_add_u32 s31, s31, 101376
v_add_u32 v7, s31, v7
v_xor_b32 v11, 64, v7
v_mov_b32 v16, 0x3f803f80
v_accvgpr_write_b32 a224, v16
v_accvgpr_write_b32 a225, v16
v_accvgpr_write_b32 a226, v16
v_accvgpr_write_b32 a227, v16
s_bitcmp1_b32 s22, 4
s_cbranch_scc1 L_entryY
L_ph_qp0X:
s_mov_b32 s65, 67584
s_mov_b32 s76, 33792
s_mov_b32 s90, -67584
s_mov_b32 s7, 0
s_mov_b32 s68, 20480
s_waitcnt vmcnt(14) lgkmcnt(0)
s_barrier
T a[144:145],v6,0
T a[146:147],v6,256
T a[148:149],v6,8448
T a[150:151],v6,8704
D a[176:179],v7,0
D a[196:199],v11,0
D a[180:183],v7,2048
D a[200:203],v11,2048
D a[184:187],v7,4096
D a[204:207],v11,4096
D a[188:191],v7,6144
D a[208:211],v11,6144
D a[192:195],v7,8192
D a[212:215],v11,8192
T a[152:153],v6,8
T a[154:155],v6,264
s_waitcnt lgkmcnt(11)
M v[176:179],a[144:147],a[176:179],0
s_waitcnt lgkmcnt(10)
T a[160:161],v6,512
M v[176:179],a[148:151],a[196:199],v[176:179]
s_waitcnt lgkmcnt(9)
s_add_u32 s70, s74, 33792
T a[156:157],v6,8456
M v[192:195],a[148:151],a[200:203],0
s_mov_b32 m0, s70
T a[162:163],v6,768
M v[192:195],a[144:147],a[180:183],v[192:195]
s_waitcnt lgkmcnt(10)
T a[158:159],v6,8712
buffer_load_dwordx4 v9, s[40:43], s78 offen lds
M v[208:211],a[144:147],a[184:187],0
s_waitcnt lgkmcnt(10)
T a[164:165],v6,8960
M v[208:211],a[148:151],a[204:207],v[208:211]
s_waitcnt lgkmcnt(9)
M v[224:227],a[148:151],a[208:211],0
T a[166:167],v6,9216
M v[224:227],a[144:147],a[188:191],v[224:227]
s_waitcnt lgkmcnt(9)
s_add_u32 s71, s78, 129024
buffer_load_dwordx4 v9, s[40:43], s71 offen offset:2048 lds
M v[240:243],a[144:147],a[192:195],0
s_waitcnt lgkmcnt(8)
M v[240:243],a[148:151],a[212:215],v[240:243]
s_waitcnt lgkmcnt(6)
M v[180:183],a[152:155],a[176:179],0
s_waitcnt lgkmcnt(2)
s_add_u32 m0, s70, 896
T a[168:169],v6,520
M v[180:183],a[156:159],a[196:199],v[180:183]
buffer_load_dwordx4 v9, s[40:43], s78 offen offset:128 lds
M v[196:199],a[156:159],a[200:203],0
T a[170:171],v6,776
M v[196:199],a[152:155],a[180:183],v[196:199]
M v[212:215],a[152:155],a[184:187],0
T a[172:173],v6,8968
M v[212:215],a[156:159],a[204:207],v[212:215]
buffer_load_dwordx4 v9, s[40:43], s71 offen offset:2176 lds
M v[228:231],a[156:159],a[208:211],0
T a[174:175],v6,9224
M v[228:231],a[152:155],a[188:191],v[228:231]
s_add_u32 s78, s78, 65536
M v[244:247],a[152:155],a[192:195],0
s_add_u32 m0, s70, 4104
M v[244:247],a[156:159],a[212:215],v[244:247]
buffer_load_dwordx4 v9, s[40:43], s78 offen lds
M v[184:187],a[160:163],a[176:179],0
s_waitcnt lgkmcnt(4)
T a[144:145],v6,1024
M v[184:187],a[164:167],a[196:199],v[184:187]
M v[200:203],a[164:167],a[200:203],0
T a[146:147],v6,1280
M v[200:203],a[160:163],a[180:183],v[200:203]
s_add_u32 s71, s78, 129024
buffer_load_dwordx4 v9, s[40:43], s71 offen offset:2048 lds
M v[216:219],a[160:163],a[184:187],0
T a[148:149],v6,9472
M v[216:219],a[164:167],a[204:207],v[216:219]
M v[232:235],a[164:167],a[208:211],0
s_add_u32 m0, s70, 5000
T a[150:151],v6,9728
M v[232:235],a[160:163],a[188:191],v[232:235]
buffer_load_dwordx4 v9, s[40:43], s78 offen offset:128 lds
M v[248:251],a[160:163],a[192:195],0
M v[248:251],a[164:167],a[212:215],v[248:251]
s_waitcnt lgkmcnt(6)
M v[188:191],a[168:171],a[176:179],0
s_waitcnt lgkmcnt(4)
T a[152:153],v6,1032
M v[188:191],a[172:175],a[196:199],v[188:191]
s_add_u32 s78, s78, -61440
buffer_load_dwordx4 v9, s[40:43], s71 offen offset:2176 lds
M v[204:207],a[172:175],a[200:203],0
T a[154:155],v6,1288
M v[204:207],a[168:171],a[180:183],v[204:207]
M v[220:223],a[168:171],a[184:187],0
T a[156:157],v6,9480
M v[220:223],a[172:175],a[204:207],v[220:223]
M v[236:239],a[172:175],a[208:211],0
T a[158:159],v6,9736
M v[236:239],a[168:171],a[188:191],v[236:239]
M v[252:255],a[168:171],a[192:195],0
M v[252:255],a[172:175],a[212:215],v[252:255]
s_waitcnt lgkmcnt(6)
M a[0:3],a[144:147],a[176:179],0
s_waitcnt lgkmcnt(4)
T a[160:161],v6,1536
M a[0:3],a[148:151],a[196:199],a[0:3]
M a[16:19],a[148:151],a[200:203],0
T a[162:163],v6,1792
M a[16:19],a[144:147],a[180:183],a[16:19]
M a[32:35],a[144:147],a[184:187],0
T a[164:165],v6,9984
M a[32:35],a[148:151],a[204:207],a[32:35]
M a[48:51],a[148:151],a[208:211],0
T a[166:167],v6,10240
M a[48:51],a[144:147],a[188:191],a[48:51]
M a[64:67],a[144:147],a[192:195],0
M a[64:67],a[148:151],a[212:215],a[64:67]
s_waitcnt lgkmcnt(6)
M a[4:7],a[152:155],a[176:179],0
s_waitcnt lgkmcnt(4)
T a[168:169],v6,1544
M a[4:7],a[156:159],a[196:199],a[4:7]
M a[20:23],a[156:159],a[200:203],0
T a[170:171],v6,1800
M a[20:23],a[152:155],a[180:183],a[20:23]
M a[36:39],a[152:155],a[184:187],0
T a[172:173],v6,9992
M a[36:39],a[156:159],a[204:207],a[36:39]
M a[52:55],a[156:159],a[208:211],0
T a[174:175],v6,10248
M a[52:55],a[152:155],a[188:191],a[52:55]
s_mov_b32 s64, s65
M a[68:71],a[152:155],a[192:195],0
s_cmp_eq_u32 s65, 67584
s_cselect_b32 s66, s90, s76
M a[68:71],a[156:159],a[212:215],a[68:71]
s_waitcnt lgkmcnt(6)
s_add_u32 s65, s65, s66
M a[8:11],a[160:163],a[176:179],0
s_waitcnt lgkmcnt(4)
M a[8:11],a[164:167],a[196:199],a[8:11]
s_waitcnt vmcnt(9) lgkmcnt(0)
v_add_u32 v6, s66, v6
M a[24:27],a[164:167],a[200:203],0
s_barrier
M a[24:27],a[160:163],a[180:183],a[24:27]
s_add_u32 s70, s74, s64
T a[144:145],v6,0
M a[40:43],a[160:163],a[184:187],0
s_mov_b32 m0, s70
T a[146:147],v6,256
M a[40:43],a[164:167],a[204:207],a[40:43]
T a[148:149],v6,8448
buffer_load_dwordx4 v9, s[40:43], s78 offen lds
M a[56:59],a[164:167],a[208:211],0
T a[150:151],v6,8704
M a[56:59],a[160:163],a[188:191],a[56:59]
T a[152:153],v6,8
M a[72:75],a[160:163],a[192:195],0
T a[154:155],v6,264
M a[72:75],a[164:167],a[212:215],a[72:75]
M a[12:15],a[168:171],a[176:179],0
s_add_u32 s71, s78, 129024
buffer_load_dwordx4 v9, s[40:43], s71 offen offset:2048 lds
M a[12:15],a[172:175],a[196:199],a[12:15]
M a[28:31],a[172:175],a[200:203],0
M a[28:31],a[168:171],a[180:183],a[28:31]
M a[44:47],a[168:171],a[184:187],0
s_add_u32 m0, s70, 896
M a[44:47],a[172:175],a[204:207],a[44:47]
buffer_load_dwordx4 v9, s[40:43], s78 offen offset:128 lds
M a[60:63],a[172:175],a[208:211],0
M a[60:63],a[168:171],a[188:191],a[60:63]
M a[76:79],a[168:171],a[192:195],0
M a[76:79],a[172:175],a[212:215],a[76:79]
s_waitcnt vmcnt(11)
v_add_u32 v12, 1, v12
s_nop 0
v_readlane_b32 s82, v12, 0
v_readlane_b32 s83, v12, 1
v_readlane_b32 s84, v12, 2
v_readlane_b32 s85, v12, 3
v_readlane_b32 s86, v12, 4
v_readlane_b32 s87, v12, 5
v_readlane_b32 s88, v12, 6
v_readlane_b32 s89, v12, 7
s_waitcnt lgkmcnt(4)
M v[16:19],a[144:147],a[176:179],0
s_waitcnt lgkmcnt(2)
T a[160:161],v6,512
buffer_load_dwordx4 v9, s[40:43], s71 offen offset:2176 lds
M v[16:19],a[148:151],a[196:199],v[16:19]
T a[156:157],v6,8456
M v[48:51],a[148:151],a[200:203],0
T a[162:163],v6,768
)"
R"(M v[48:51],a[144:147],a[180:183],v[48:51]
s_add_u32 s78, s78, 65536
T a[158:159],v6,8712
M v[80:83],a[144:147],a[184:187],0
s_add_u32 m0, s70, 4104
T a[164:165],v6,8960
M v[80:83],a[148:151],a[204:207],v[80:83]
buffer_load_dwordx4 v9, s[40:43], s78 offen lds
M v[112:115],a[148:151],a[208:211],0
T a[166:167],v6,9216
M v[112:115],a[144:147],a[188:191],v[112:115]
M v[144:147],a[144:147],a[192:195],0
M v[144:147],a[148:151],a[212:215],v[144:147]
s_waitcnt lgkmcnt(6)
M v[20:23],a[152:155],a[176:179],0
s_waitcnt lgkmcnt(2)
s_add_u32 s71, s78, 129024
T a[168:169],v6,520
buffer_load_dwordx4 v9, s[40:43], s71 offen offset:2048 lds
M v[20:23],a[156:159],a[196:199],v[20:23]
M v[52:55],a[156:159],a[200:203],0
T a[170:171],v6,776
M v[52:55],a[152:155],a[180:183],v[52:55]
M v[84:87],a[152:155],a[184:187],0
s_add_u32 m0, s70, 5000
T a[172:173],v6,8968
M v[84:87],a[156:159],a[204:207],v[84:87]
buffer_load_dwordx4 v9, s[40:43], s78 offen offset:128 lds
M v[116:119],a[156:159],a[208:211],0
T a[174:175],v6,9224
M v[116:119],a[152:155],a[188:191],v[116:119]
M v[148:151],a[152:155],a[192:195],0
M v[148:151],a[156:159],a[212:215],v[148:151]
M v[24:27],a[160:163],a[176:179],0
s_waitcnt lgkmcnt(4)
s_add_u32 s78, s78, 454656
T a[144:145],v6,1024
buffer_load_dwordx4 v9, s[40:43], s71 offen offset:2176 lds
M v[24:27],a[164:167],a[196:199],v[24:27]
s_cmp_eq_u32 s7, 40960
s_cselect_b32 s67, -40960, s68
M v[56:59],a[164:167],a[200:203],0
T a[146:147],v6,1280
M v[56:59],a[160:163],a[180:183],v[56:59]
s_add_u32 s7, s7, s67
M v[88:91],a[160:163],a[184:187],0
s_mov_b32 m0, s75
T a[148:149],v6,9472
M v[88:91],a[164:167],a[204:207],v[88:91]
buffer_load_dwordx4 v10, s[36:39], s79 offen lds
v_add_u32 v7, s67, v7
M v[120:123],a[164:167],a[208:211],0
T a[150:151],v6,9728
M v[120:123],a[160:163],a[188:191],v[120:123]
v_add_u32 v11, s67, v11
M v[152:155],a[160:163],a[192:195],0
M v[152:155],a[164:167],a[212:215],v[152:155]
s_waitcnt lgkmcnt(6)
M v[28:31],a[168:171],a[176:179],0
s_waitcnt lgkmcnt(4)
s_add_u32 s71, s79, 7168
T a[152:153],v6,1032
buffer_load_dwordx4 v10, s[36:39], s71 offen offset:1024 lds
M v[28:31],a[172:175],a[196:199],v[28:31]
M v[60:63],a[172:175],a[200:203],0
T a[154:155],v6,1288
M v[60:63],a[168:171],a[180:183],v[60:63]
M v[92:95],a[168:171],a[184:187],0
T a[156:157],v6,9480
M v[92:95],a[172:175],a[204:207],v[92:95]
s_add_u32 s72, s79, 14336
buffer_load_dwordx4 v10, s[36:39], s72 offen offset:2048 lds
M v[124:127],a[172:175],a[208:211],0
T a[158:159],v6,9736
M v[124:127],a[168:171],a[188:191],v[124:127]
M v[156:159],a[168:171],a[192:195],0
M v[156:159],a[172:175],a[212:215],v[156:159]
s_waitcnt lgkmcnt(6)
M v[32:35],a[144:147],a[176:179],0
s_waitcnt lgkmcnt(4)
s_add_u32 s71, s79, 21504
T a[160:161],v6,1536
buffer_load_dwordx4 v10, s[36:39], s71 offen offset:3072 lds
M v[32:35],a[148:151],a[196:199],v[32:35]
M v[64:67],a[148:151],a[200:203],0
T a[162:163],v6,1792
M v[64:67],a[144:147],a[180:183],v[64:67]
s_add_u32 s72, s79, 32768
M v[96:99],a[144:147],a[184:187],0
s_add_u32 m0, s75, 4096
T a[164:165],v6,9984
M v[96:99],a[148:151],a[204:207],v[96:99]
buffer_load_dwordx4 v10, s[36:39], s72 offen lds
M v[128:131],a[148:151],a[208:211],0
s_add_u32 s79, s79, 128
T a[166:167],v6,10240
M v[128:131],a[144:147],a[188:191],v[128:131]
s_add_u32 s75, s75, 20480
M v[160:163],a[144:147],a[192:195],0
s_cmp_eq_u32 s75, s95
s_cselect_b32 s75, s80, s75
M v[160:163],a[148:151],a[212:215],v[160:163]
s_waitcnt lgkmcnt(6)
M v[36:39],a[152:155],a[176:179],0
s_waitcnt lgkmcnt(4)
T a[168:169],v6,1544
M v[36:39],a[156:159],a[196:199],v[36:39]
M v[68:71],a[156:159],a[200:203],0
T a[170:171],v6,1800
M v[68:71],a[152:155],a[180:183],v[68:71]
M v[100:103],a[152:155],a[184:187],0
T a[172:173],v6,9992
M v[100:103],a[156:159],a[204:207],v[100:103]
M v[132:135],a[156:159],a[208:211],0
T a[174:175],v6,10248
M v[132:135],a[152:155],a[188:191],v[132:135]
s_mov_b32 s64, s65
M v[164:167],a[152:155],a[192:195],0
s_cmp_eq_u32 s65, 67584
s_cselect_b32 s66, s90, s76
M v[164:167],a[156:159],a[212:215],v[164:167]
s_waitcnt lgkmcnt(6)
s_add_u32 s65, s65, s66
M v[40:43],a[160:163],a[176:179],0
s_waitcnt lgkmcnt(4)
M v[40:43],a[164:167],a[196:199],v[40:43]
s_waitcnt vmcnt(13) lgkmcnt(0)
v_add_u32 v6, s66, v6
M v[72:75],a[164:167],a[200:203],0
s_barrier
M v[72:75],a[160:163],a[180:183],v[72:75]
s_add_u32 s70, s74, s64
T a[144:145],v6,0
M v[104:107],a[160:163],a[184:187],0
s_mov_b32 m0, s70
T a[146:147],v6,256
M v[104:107],a[164:167],a[204:207],v[104:107]
T a[148:149],v6,8448
buffer_load_dwordx4 v9, s[40:43], s78 offen lds
M v[136:139],a[164:167],a[208:211],0
T a[150:151],v6,8704
M v[136:139],a[160:163],a[188:191],v[136:139]
T a[152:153],v6,8
M v[168:171],a[160:163],a[192:195],0
T a[154:155],v6,264
M v[168:171],a[164:167],a[212:215],v[168:171]
M v[44:47],a[168:171],a[176:179],0
D a[176:179],v7,0
M v[44:47],a[172:175],a[196:199],v[44:47]
s_add_u32 s71, s78, 129024
D a[196:199],v11,0
buffer_load_dwordx4 v9, s[40:43], s71 offen offset:2048 lds
M v[76:79],a[172:175],a[200:203],0
D a[200:203],v11,2048
M v[76:79],a[168:171],a[180:183],v[76:79]
D a[180:183],v7,2048
M v[108:111],a[168:171],a[184:187],0
D a[184:187],v7,4096
M v[108:111],a[172:175],a[204:207],v[108:111]
D a[204:207],v11,4096
M v[140:143],a[172:175],a[208:211],0
s_add_u32 m0, s70, 896
D a[208:211],v11,6144
M v[140:143],a[168:171],a[188:191],v[140:143]
D a[188:191],v7,6144
buffer_load_dwordx4 v9, s[40:43], s78 offen offset:128 lds
M v[172:175],a[168:171],a[192:195],0
D a[192:195],v7,8192
M v[172:175],a[172:175],a[212:215],v[172:175]
D a[212:215],v11,8192
s_mov_b32 s69, 5
.p2align 3
L_qpX_1:
s_waitcnt lgkmcnt(9)
M v[176:179],a[144:147],a[176:179],v[176:179]
s_waitcnt lgkmcnt(8)
T a[160:161],v6,512
M v[176:179],a[148:151],a[196:199],v[176:179]
s_waitcnt lgkmcnt(8)
T a[156:157],v6,8456
M v[192:195],a[148:151],a[200:203],v[192:195]
s_waitcnt lgkmcnt(8)
T a[162:163],v6,768
M v[192:195],a[144:147],a[180:183],v[192:195]
s_waitcnt lgkmcnt(8)
T a[158:159],v6,8712
buffer_load_dwordx4 v9, s[40:43], s71 offen offset:2176 lds
M v[208:211],a[144:147],a[184:187],v[208:211]
s_waitcnt lgkmcnt(8)
T a[164:165],v6,8960
M v[208:211],a[148:151],a[204:207],v[208:211]
s_waitcnt lgkmcnt(8)
M v[224:227],a[148:151],a[208:211],v[224:227]
s_waitcnt lgkmcnt(7)
T a[166:167],v6,9216
M v[224:227],a[144:147],a[188:191],v[224:227]
s_waitcnt lgkmcnt(7)
s_add_u32 s78, s78, 65536
M v[240:243],a[144:147],a[192:195],v[240:243]
s_waitcnt lgkmcnt(6)
s_add_u32 m0, s70, 4104
M v[240:243],a[148:151],a[212:215],v[240:243]
buffer_load_dwordx4 v9, s[40:43], s78 offen lds
M v[180:183],a[152:155],a[176:179],v[180:183]
s_waitcnt lgkmcnt(2)
T a[168:169],v6,520
M v[180:183],a[156:159],a[196:199],v[180:183]
M v[196:199],a[156:159],a[200:203],v[196:199]
T a[170:171],v6,776
M v[196:199],a[152:155],a[180:183],v[196:199]
M v[212:215],a[152:155],a[184:187],v[212:215]
T a[172:173],v6,8968
M v[212:215],a[156:159],a[204:207],v[212:215]
s_add_u32 s71, s78, 129024
buffer_load_dwordx4 v9, s[40:43], s71 offen offset:2048 lds
M v[228:231],a[156:159],a[208:211],v[228:231]
T a[174:175],v6,9224
M v[228:231],a[152:155],a[188:191],v[228:231]
M v[244:247],a[152:155],a[192:195],v[244:247]
M v[244:247],a[156:159],a[212:215],v[244:247]
M v[184:187],a[160:163],a[176:179],v[184:187]
s_waitcnt lgkmcnt(4)
s_add_u32 m0, s70, 5000
T a[144:145],v6,1024
M v[184:187],a[164:167],a[196:199],v[184:187]
buffer_load_dwordx4 v9, s[40:43], s78 offen offset:128 lds
M v[200:203],a[164:167],a[200:203],v[200:203]
T a[146:147],v6,1280
M v[200:203],a[160:163],a[180:183],v[200:203]
M v[216:219],a[160:163],a[184:187],v[216:219]
T a[148:149],v6,9472
M v[216:219],a[164:167],a[204:207],v[216:219]
M v[232:235],a[164:167],a[208:211],v[232:235]
T a[150:151],v6,9728
M v[232:235],a[160:163],a[188:191],v[232:235]
s_add_u32 s78, s78, -61440
buffer_load_dwordx4 v9, s[40:43], s71 offen offset:2176 lds
M v[248:251],a[160:163],a[192:195],v[248:251]
M v[248:251],a[164:167],a[212:215],v[248:251]
s_waitcnt lgkmcnt(6)
M v[188:191],a[168:171],a[176:179],v[188:191]
s_waitcnt lgkmcnt(4)
T a[152:153],v6,1032
M v[188:191],a[172:175],a[196:199],v[188:191]
M v[204:207],a[172:175],a[200:203],v[204:207]
T a[154:155],v6,1288
M v[204:207],a[168:171],a[180:183],v[204:207]
M v[220:223],a[168:171],a[184:187],v[220:223]
T a[156:157],v6,9480
M v[220:223],a[172:175],a[204:207],v[220:223]
M v[236:239],a[172:175],a[208:211],v[236:239]
T a[158:159],v6,9736
M v[236:239],a[168:171],a[188:191],v[236:239]
M v[252:255],a[168:171],a[192:195],v[252:255]
M v[252:255],a[172:175],a[212:215],v[252:255]
s_waitcnt lgkmcnt(6)
M a[0:3],a[144:147],a[176:179],a[0:3]
s_waitcnt lgkmcnt(4)
T a[160:161],v6,1536
M a[0:3],a[148:151],a[196:199],a[0:3]
M a[16:19],a[148:151],a[200:203],a[16:19]
T a[162:163],v6,1792
M a[16:19],a[144:147],a[180:183],a[16:19]
M a[32:35],a[144:147],a[184:187],a[32:35]
T a[164:165],v6,9984
M a[32:35],a[148:151],a[204:207],a[32:35]
M a[48:51],a[148:151],a[208:211],a[48:51]
T a[166:167],v6,10240
M a[48:51],a[144:147],a[188:191],a[48:51]
M a[64:67],a[144:147],a[192:195],a[64:67]
M a[64:67],a[148:151],a[212:215],a[64:67]
s_waitcnt lgkmcnt(6)
M a[4:7],a[152:155],a[176:179],a[4:7]
s_waitcnt lgkmcnt(4)
T a[168:169],v6,1544
M a[4:7],a[156:159],a[196:199],a[4:7]
M a[20:23],a[156:159],a[200:203],a[20:23]
T a[170:171],v6,1800
M a[20:23],a[152:155],a[180:183],a[20:23]
M a[36:39],a[152:155],a[184:187],a[36:39]
T a[172:173],v6,9992
M a[36:39],a[156:159],a[204:207],a[36:39]
M a[52:55],a[156:159],a[208:211],a[52:55]
T a[174:175],v6,10248
M a[52:55],a[152:155],a[188:191],a[52:55]
s_mov_b32 s64, s65
M a[68:71],a[152:155],a[192:195],a[68:71]
s_cmp_eq_u32 s65, 67584
s_cselect_b32 s66, s90, s76
M a[68:71],a[156:159],a[212:215],a[68:71]
s_waitcnt lgkmcnt(6)
s_add_u32 s65, s65, s66
M a[8:11],a[160:163],a[176:179],a[8:11]
s_waitcnt lgkmcnt(4)
M a[8:11],a[164:167],a[196:199],a[8:11]
s_waitcnt vmcnt(13) lgkmcnt(0)
v_add_u32 v6, s66, v6
M a[24:27],a[164:167],a[200:203],a[24:27]
s_barrier
M a[24:27],a[160:163],a[180:183],a[24:27]
s_add_u32 s70, s74, s64
T a[144:145],v6,0
M a[40:43],a[160:163],a[184:187],a[40:43]
s_mov_b32 m0, s70
T a[146:147],v6,256
M a[40:43],a[164:167],a[204:207],a[40:43]
T a[148:149],v6,8448
buffer_load_dwordx4 v9, s[40:43], s78 offen lds
M a[56:59],a[164:167],a[208:211],a[56:59]
T a[150:151],v6,8704
M a[56:59],a[160:163],a[188:191],a[56:59]
T a[152:153],v6,8
M a[72:75],a[160:163],a[192:195],a[72:75]
T a[154:155],v6,264
M a[72:75],a[164:167],a[212:215],a[72:75]
M a[12:15],a[168:171],a[176:179],a[12:15]
s_add_u32 s71, s78, 129024
buffer_load_dwordx4 v9, s[40:43], s71 offen offset:2048 lds
M a[12:15],a[172:175],a[196:199],a[12:15]
M a[28:31],a[172:175],a[200:203],a[28:31]
M a[28:31],a[168:171],a[180:183],a[28:31]
M a[44:47],a[168:171],a[184:187],a[44:47]
s_add_u32 m0, s70, 896
M a[44:47],a[172:175],a[204:207],a[44:47]
buffer_load_dwordx4 v9, s[40:43], s78 offen offset:128 lds
M a[60:63],a[172:175],a[208:211],a[60:63]
M a[60:63],a[168:171],a[188:191],a[60:63]
M a[76:79],a[168:171],a[192:195],a[76:79]
M a[76:79],a[172:175],a[212:215],a[76:79]
s_waitcnt lgkmcnt(4)
M v[16:19],a[144:147],a[176:179],v[16:19]
s_waitcnt lgkmcnt(2)
T a[160:161],v6,512
buffer_load_dwordx4 v9, s[40:43], s71 offen offset:2176 lds
M v[16:19],a[148:151],a[196:199],v[16:19]
T a[156:157],v6,8456
M v[48:51],a[148:151],a[200:203],v[48:51]
T a[162:163],v6,768
M v[48:51],a[144:147],a[180:183],v[48:51]
s_add_u32 s78, s78, 65536
T a[158:159],v6,8712
M v[80:83],a[144:147],a[184:187],v[80:83]
s_add_u32 m0, s70, 4104
T a[164:165],v6,8960
M v[80:83],a[148:151],a[204:207],v[80:83]
buffer_load_dwordx4 v9, s[40:43], s78 offen lds
M v[112:115],a[148:151],a[208:211],v[112:115]
T a[166:167],v6,9216
M v[112:115],a[144:147],a[188:191],v[112:115]
M v[144:147],a[144:147],a[192:195],v[144:147]
M v[144:147],a[148:151],a[212:215],v[144:147]
s_waitcnt lgkmcnt(6)
M v[20:23],a[152:155],a[176:179],v[20:23]
s_waitcnt lgkmcnt(2)
s_add_u32 s71, s78, 129024
T a[168:169],v6,520
buffer_load_dwordx4 v9, s[40:43], s71 offen offset:2048 lds
M v[20:23],a[156:159],a[196:199],v[20:23]
M v[52:55],a[156:159],a[200:203],v[52:55]
T a[170:171],v6,776
M v[52:55],a[152:155],a[180:183],v[52:55]
M v[84:87],a[152:155],a[184:187],v[84:87]
s_add_u32 m0, s70, 5000
T a[172:173],v6,8968
M v[84:87],a[156:159],a[204:207],v[84:87]
buffer_load_dwordx4 v9, s[40:43], s78 offen offset:128 lds
M v[116:119],a[156:159],a[208:211],v[116:119]
T a[174:175],v6,9224
M v[116:119],a[152:155],a[188:191],v[116:119]
M v[148:151],a[152:155],a[192:195],v[148:151]
M v[148:151],a[156:159],a[212:215],v[148:151]
M v[24:27],a[160:163],a[176:179],v[24:27]
s_waitcnt lgkmcnt(4)
s_add_u32 s78, s78, 454656
T a[144:145],v6,1024
buffer_load_dwordx4 v9, s[40:43], s71 offen offset:2176 lds
M v[24:27],a[164:167],a[196:199],v[24:27]
s_cmp_eq_u32 s7, 40960
s_cselect_b32 s67, -40960, s68
M v[56:59],a[164:167],a[200:203],v[56:59]
T a[146:147],v6,1280
M v[56:59],a[160:163],a[180:183],v[56:59]
s_add_u32 s7, s7, s67
M v[88:91],a[160:163],a[184:187],v[88:91]
s_mov_b32 m0, s75
T a[148:149],v6,9472
M v[88:91],a[164:167],a[204:207],v[88:91]
buffer_load_dwordx4 v10, s[36:39], s79 offen lds
v_add_u32 v7, s67, v7
M v[120:123],a[164:167],a[208:211],v[120:123]
T a[150:151],v6,9728
M v[120:123],a[160:163],a[188:191],v[120:123]
v_add_u32 v11, s67, v11
M v[152:155],a[160:163],a[192:195],v[152:155]
M v[152:155],a[164:167],a[212:215],v[152:155]
s_waitcnt lgkmcnt(6)
M v[28:31],a[168:171],a[176:179],v[28:31]
s_waitcnt lgkmcnt(4)
s_add_u32 s71, s79, 7168
T a[152:153],v6,1032
buffer_load_dwordx4 v10, s[36:39], s71 offen offset:1024 lds
M v[28:31],a[172:175],a[196:199],v[28:31]
M v[60:63],a[172:175],a[200:203],v[60:63]
T a[154:155],v6,1288
M v[60:63],a[168:171],a[180:183],v[60:63]
M v[92:95],a[168:171],a[184:187],v[92:95]
T a[156:157],v6,9480
M v[92:95],a[172:175],a[204:207],v[92:95]
s_add_u32 s72, s79, 14336
buffer_load_dwordx4 v10, s[36:39], s72 offen offset:2048 lds
M v[124:127],a[172:175],a[208:211],v[124:127]
T a[158:159],v6,9736
M v[124:127],a[168:171],a[188:191],v[124:127]
M v[156:159],a[168:171],a[192:195],v[156:159]
M v[156:159],a[172:175],a[212:215],v[156:159]
s_waitcnt lgkmcnt(6)
M v[32:35],a[144:147],a[176:179],v[32:35]
s_waitcnt lgkmcnt(4)
s_add_u32 s71, s79, 21504
T a[160:161],v6,1536
buffer_load_dwordx4 v10, s[36:39], s71 offen offset:3072 lds
M v[32:35],a[148:151],a[196:199],v[32:35]
M v[64:67],a[148:151],a[200:203],v[64:67]
T a[162:163],v6,1792
M v[64:67],a[144:147],a[180:183],v[64:67]
s_add_u32 s72, s79, 32768
M v[96:99],a[144:147],a[184:187],v[96:99]
s_add_u32 m0, s75, 4096
T a[164:165],v6,9984
M v[96:99],a[148:151],a[204:207],v[96:99]
buffer_load_dwordx4 v10, s[36:39], s72 offen lds
M v[128:131],a[148:151],a[208:211],v[128:131]
s_add_u32 s79, s79, 128
T a[166:167],v6,10240
M v[128:131],a[144:147],a[188:191],v[128:131]
s_add_u32 s75, s75, 20480
M v[160:163],a[144:147],a[192:195],v[160:163]
s_cmp_eq_u32 s75, s95
s_cselect_b32 s75, s80, s75
M v[160:163],a[148:151],a[212:215],v[160:163]
s_waitcnt lgkmcnt(6)
M v[36:39],a[152:155],a[176:179],v[36:39]
s_waitcnt lgkmcnt(4)
T a[168:169],v6,1544
M v[36:39],a[156:159],a[196:199],v[36:39]
M v[68:71],a[156:159],a[200:203],v[68:71]
T a[170:171],v6,1800
M v[68:71],a[152:155],a[180:183],v[68:71]
M v[100:103],a[152:155],a[184:187],v[100:103]
T a[172:173],v6,9992
M v[100:103],a[156:159],a[204:207],v[100:103]
M v[132:135],a[156:159],a[208:211],v[132:135]
T a[174:175],v6,10248
M v[132:135],a[152:155],a[188:191],v[132:135]
s_mov_b32 s64, s65
M v[164:167],a[152:155],a[192:195],v[164:167]
s_cmp_eq_u32 s65, 67584
s_cselect_b32 s66, s90, s76
M v[164:167],a[156:159],a[212:215],v[164:167]
s_waitcnt lgkmcnt(6)
s_add_u32 s65, s65, s66
M v[40:43],a[160:163],a[176:179],v[40:43]
s_waitcnt lgkmcnt(4)
M v[40:43],a[164:167],a[196:199],v[40:43]
s_waitcnt vmcnt(13) lgkmcnt(0)
v_add_u32 v6, s66, v6
M v[72:75],a[164:167],a[200:203],v[72:75]
s_barrier
M v[72:75],a[160:163],a[180:183],v[72:75]
s_add_u32 s70, s74, s64
T a[144:145],v6,0
M v[104:107],a[160:163],a[184:187],v[104:107]
s_mov_b32 m0, s70
T a[146:147],v6,256
M v[104:107],a[164:167],a[204:207],v[104:107]
T a[148:149],v6,8448
buffer_load_dwordx4 v9, s[40:43], s78 offen lds
M v[136:139],a[164:167],a[208:211],v[136:139]
T a[150:151],v6,8704
M v[136:139],a[160:163],a[188:191],v[136:139]
T a[152:153],v6,8
M v[168:171],a[160:163],a[192:195],v[168:171]
T a[154:155],v6,264
M v[168:171],a[164:167],a[212:215],v[168:171]
M v[44:47],a[168:171],a[176:179],v[44:47]
D a[176:179],v7,0
M v[44:47],a[172:175],a[196:199],v[44:47]
s_add_u32 s71, s78, 129024
D a[196:199],v11,0
buffer_load_dwordx4 v9, s[40:43], s71 offen offset:2048 lds
M v[76:79],a[172:175],a[200:203],v[76:79]
D a[200:203],v11,2048
M v[76:79],a[168:171],a[180:183],v[76:79]
D a[180:183],v7,2048
M v[108:111],a[168:171],a[184:187],v[108:111]
D a[184:187],v7,4096
M v[108:111],a[172:175],a[204:207],v[108:111]
D a[204:207],v11,4096
M v[140:143],a[172:175],a[208:211],v[140:143]
s_add_u32 m0, s70, 896
)"
R"(D a[208:211],v11,6144
M v[140:143],a[168:171],a[188:191],v[140:143]
D a[188:191],v7,6144
buffer_load_dwordx4 v9, s[40:43], s78 offen offset:128 lds
M v[172:175],a[168:171],a[192:195],v[172:175]
s_sub_u32 s69, s69, 1
D a[192:195],v7,8192
M v[172:175],a[172:175],a[212:215],v[172:175]
s_cmp_lg_u32 s69, 0
D a[212:215],v11,8192
s_cbranch_scc1 L_qpX_1
s_waitcnt lgkmcnt(9)
M v[176:179],a[144:147],a[176:179],v[176:179]
s_waitcnt lgkmcnt(8)
T a[160:161],v6,512
M v[176:179],a[148:151],a[196:199],v[176:179]
s_waitcnt lgkmcnt(8)
T a[156:157],v6,8456
M v[192:195],a[148:151],a[200:203],v[192:195]
s_waitcnt lgkmcnt(8)
T a[162:163],v6,768
M v[192:195],a[144:147],a[180:183],v[192:195]
s_waitcnt lgkmcnt(8)
T a[158:159],v6,8712
buffer_load_dwordx4 v9, s[40:43], s71 offen offset:2176 lds
M v[208:211],a[144:147],a[184:187],v[208:211]
s_waitcnt lgkmcnt(8)
T a[164:165],v6,8960
M v[208:211],a[148:151],a[204:207],v[208:211]
s_waitcnt lgkmcnt(8)
M v[224:227],a[148:151],a[208:211],v[224:227]
s_waitcnt lgkmcnt(7)
T a[166:167],v6,9216
M v[224:227],a[144:147],a[188:191],v[224:227]
s_waitcnt lgkmcnt(7)
s_add_u32 s78, s78, 65536
M v[240:243],a[144:147],a[192:195],v[240:243]
s_waitcnt lgkmcnt(6)
s_add_u32 m0, s70, 4104
M v[240:243],a[148:151],a[212:215],v[240:243]
buffer_load_dwordx4 v9, s[40:43], s78 offen lds
M v[180:183],a[152:155],a[176:179],v[180:183]
s_waitcnt lgkmcnt(2)
T a[168:169],v6,520
M v[180:183],a[156:159],a[196:199],v[180:183]
M v[196:199],a[156:159],a[200:203],v[196:199]
T a[170:171],v6,776
M v[196:199],a[152:155],a[180:183],v[196:199]
M v[212:215],a[152:155],a[184:187],v[212:215]
T a[172:173],v6,8968
M v[212:215],a[156:159],a[204:207],v[212:215]
s_add_u32 s71, s78, 129024
buffer_load_dwordx4 v9, s[40:43], s71 offen offset:2048 lds
M v[228:231],a[156:159],a[208:211],v[228:231]
T a[174:175],v6,9224
M v[228:231],a[152:155],a[188:191],v[228:231]
M v[244:247],a[152:155],a[192:195],v[244:247]
M v[244:247],a[156:159],a[212:215],v[244:247]
M v[184:187],a[160:163],a[176:179],v[184:187]
s_waitcnt lgkmcnt(4)
s_add_u32 m0, s70, 5000
T a[144:145],v6,1024
M v[184:187],a[164:167],a[196:199],v[184:187]
buffer_load_dwordx4 v9, s[40:43], s78 offen offset:128 lds
M v[200:203],a[164:167],a[200:203],v[200:203]
T a[146:147],v6,1280
M v[200:203],a[160:163],a[180:183],v[200:203]
M v[216:219],a[160:163],a[184:187],v[216:219]
T a[148:149],v6,9472
M v[216:219],a[164:167],a[204:207],v[216:219]
M v[232:235],a[164:167],a[208:211],v[232:235]
T a[150:151],v6,9728
M v[232:235],a[160:163],a[188:191],v[232:235]
s_add_u32 s78, s78, -61440
buffer_load_dwordx4 v9, s[40:43], s71 offen offset:2176 lds
M v[248:251],a[160:163],a[192:195],v[248:251]
M v[248:251],a[164:167],a[212:215],v[248:251]
s_waitcnt lgkmcnt(6)
M v[188:191],a[168:171],a[176:179],v[188:191]
s_waitcnt lgkmcnt(4)
T a[152:153],v6,1032
M v[188:191],a[172:175],a[196:199],v[188:191]
M v[204:207],a[172:175],a[200:203],v[204:207]
T a[154:155],v6,1288
M v[204:207],a[168:171],a[180:183],v[204:207]
M v[220:223],a[168:171],a[184:187],v[220:223]
T a[156:157],v6,9480
M v[220:223],a[172:175],a[204:207],v[220:223]
M v[236:239],a[172:175],a[208:211],v[236:239]
T a[158:159],v6,9736
M v[236:239],a[168:171],a[188:191],v[236:239]
M v[252:255],a[168:171],a[192:195],v[252:255]
M v[252:255],a[172:175],a[212:215],v[252:255]
s_waitcnt lgkmcnt(6)
M a[0:3],a[144:147],a[176:179],a[0:3]
s_waitcnt lgkmcnt(4)
T a[160:161],v6,1536
M a[0:3],a[148:151],a[196:199],a[0:3]
M a[16:19],a[148:151],a[200:203],a[16:19]
T a[162:163],v6,1792
M a[16:19],a[144:147],a[180:183],a[16:19]
M a[32:35],a[144:147],a[184:187],a[32:35]
T a[164:165],v6,9984
M a[32:35],a[148:151],a[204:207],a[32:35]
M a[48:51],a[148:151],a[208:211],a[48:51]
T a[166:167],v6,10240
M a[48:51],a[144:147],a[188:191],a[48:51]
M a[64:67],a[144:147],a[192:195],a[64:67]
M a[64:67],a[148:151],a[212:215],a[64:67]
s_waitcnt lgkmcnt(6)
M a[4:7],a[152:155],a[176:179],a[4:7]
s_waitcnt lgkmcnt(4)
T a[168:169],v6,1544
M a[4:7],a[156:159],a[196:199],a[4:7]
M a[20:23],a[156:159],a[200:203],a[20:23]
T a[170:171],v6,1800
M a[20:23],a[152:155],a[180:183],a[20:23]
M a[36:39],a[152:155],a[184:187],a[36:39]
T a[172:173],v6,9992
M a[36:39],a[156:159],a[204:207],a[36:39]
M a[52:55],a[156:159],a[208:211],a[52:55]
T a[174:175],v6,10248
M a[52:55],a[152:155],a[188:191],a[52:55]
s_mov_b32 s64, s65
M a[68:71],a[152:155],a[192:195],a[68:71]
s_cmp_eq_u32 s65, 67584
s_cselect_b32 s66, s90, s76
M a[68:71],a[156:159],a[212:215],a[68:71]
s_waitcnt lgkmcnt(6)
s_add_u32 s65, s65, s66
M a[8:11],a[160:163],a[176:179],a[8:11]
s_waitcnt lgkmcnt(4)
M a[8:11],a[164:167],a[196:199],a[8:11]
s_waitcnt vmcnt(13) lgkmcnt(0)
v_add_u32 v6, s66, v6
M a[24:27],a[164:167],a[200:203],a[24:27]
s_barrier
M a[24:27],a[160:163],a[180:183],a[24:27]
s_add_u32 s70, s74, s64
T a[144:145],v6,0
M a[40:43],a[160:163],a[184:187],a[40:43]
s_mov_b32 m0, s70
T a[146:147],v6,256
M a[40:43],a[164:167],a[204:207],a[40:43]
T a[148:149],v6,8448
buffer_load_dwordx4 v9, s[40:43], s78 offen lds
M a[56:59],a[164:167],a[208:211],a[56:59]
T a[150:151],v6,8704
M a[56:59],a[160:163],a[188:191],a[56:59]
T a[152:153],v6,8
M a[72:75],a[160:163],a[192:195],a[72:75]
T a[154:155],v6,264
M a[72:75],a[164:167],a[212:215],a[72:75]
M a[12:15],a[168:171],a[176:179],a[12:15]
M a[12:15],a[172:175],a[196:199],a[12:15]
s_add_u32 s71, s78, 129024
buffer_load_dwordx4 v9, s[40:43], s71 offen offset:2048 lds
M a[28:31],a[172:175],a[200:203],a[28:31]
M a[28:31],a[168:171],a[180:183],a[28:31]
M a[44:47],a[168:171],a[184:187],a[44:47]
M a[44:47],a[172:175],a[204:207],a[44:47]
M a[60:63],a[172:175],a[208:211],a[60:63]
s_add_u32 m0, s70, 896
M a[60:63],a[168:171],a[188:191],a[60:63]
buffer_load_dwordx4 v9, s[40:43], s78 offen offset:128 lds
M a[76:79],a[168:171],a[192:195],a[76:79]
M a[76:79],a[172:175],a[212:215],a[76:79]
s_waitcnt lgkmcnt(4)
M v[16:19],a[144:147],a[176:179],v[16:19]
s_waitcnt lgkmcnt(2)
T a[160:161],v6,512
M v[16:19],a[148:151],a[196:199],v[16:19]
T a[156:157],v6,8456
M v[48:51],a[148:151],a[200:203],v[48:51]
T a[162:163],v6,768
M v[48:51],a[144:147],a[180:183],v[48:51]
T a[158:159],v6,8712
buffer_load_dwordx4 v9, s[40:43], s71 offen offset:2176 lds
M v[80:83],a[144:147],a[184:187],v[80:83]
T a[164:165],v6,8960
M v[80:83],a[148:151],a[204:207],v[80:83]
M v[112:115],a[148:151],a[208:211],v[112:115]
T a[166:167],v6,9216
M v[112:115],a[144:147],a[188:191],v[112:115]
s_add_u32 s78, s78, 65536
M v[144:147],a[144:147],a[192:195],v[144:147]
s_add_u32 m0, s70, 4104
M v[144:147],a[148:151],a[212:215],v[144:147]
s_waitcnt lgkmcnt(6)
buffer_load_dwordx4 v9, s[40:43], s78 offen lds
M v[20:23],a[152:155],a[176:179],v[20:23]
s_waitcnt lgkmcnt(2)
T a[168:169],v6,520
M v[20:23],a[156:159],a[196:199],v[20:23]
M v[52:55],a[156:159],a[200:203],v[52:55]
T a[170:171],v6,776
M v[52:55],a[152:155],a[180:183],v[52:55]
M v[84:87],a[152:155],a[184:187],v[84:87]
T a[172:173],v6,8968
M v[84:87],a[156:159],a[204:207],v[84:87]
s_add_u32 s71, s78, 129024
buffer_load_dwordx4 v9, s[40:43], s71 offen offset:2048 lds
M v[116:119],a[156:159],a[208:211],v[116:119]
T a[174:175],v6,9224
M v[116:119],a[152:155],a[188:191],v[116:119]
M v[148:151],a[152:155],a[192:195],v[148:151]
M v[148:151],a[156:159],a[212:215],v[148:151]
M v[24:27],a[160:163],a[176:179],v[24:27]
s_waitcnt lgkmcnt(4)
s_add_u32 m0, s70, 5000
T a[144:145],v6,1024
M v[24:27],a[164:167],a[196:199],v[24:27]
s_cmp_eq_u32 s7, 40960
s_cselect_b32 s67, -40960, s68
buffer_load_dwordx4 v9, s[40:43], s78 offen offset:128 lds
M v[56:59],a[164:167],a[200:203],v[56:59]
T a[146:147],v6,1280
M v[56:59],a[160:163],a[180:183],v[56:59]
s_add_u32 s7, s7, s67
M v[88:91],a[160:163],a[184:187],v[88:91]
T a[148:149],v6,9472
M v[88:91],a[164:167],a[204:207],v[88:91]
v_add_u32 v7, s67, v7
M v[120:123],a[164:167],a[208:211],v[120:123]
T a[150:151],v6,9728
M v[120:123],a[160:163],a[188:191],v[120:123]
s_add_u32 s78, s78, 454656
buffer_load_dwordx4 v9, s[40:43], s71 offen offset:2176 lds
v_add_u32 v11, s67, v11
M v[152:155],a[160:163],a[192:195],v[152:155]
M v[152:155],a[164:167],a[212:215],v[152:155]
s_waitcnt lgkmcnt(6)
M v[28:31],a[168:171],a[176:179],v[28:31]
s_waitcnt lgkmcnt(4)
T a[152:153],v6,1032
M v[28:31],a[172:175],a[196:199],v[28:31]
M v[60:63],a[172:175],a[200:203],v[60:63]
T a[154:155],v6,1288
M v[60:63],a[168:171],a[180:183],v[60:63]
M v[92:95],a[168:171],a[184:187],v[92:95]
T a[156:157],v6,9480
M v[92:95],a[172:175],a[204:207],v[92:95]
M v[124:127],a[172:175],a[208:211],v[124:127]
T a[158:159],v6,9736
M v[124:127],a[168:171],a[188:191],v[124:127]
M v[156:159],a[168:171],a[192:195],v[156:159]
M v[156:159],a[172:175],a[212:215],v[156:159]
s_waitcnt lgkmcnt(6)
M v[32:35],a[144:147],a[176:179],v[32:35]
s_waitcnt lgkmcnt(4)
T a[160:161],v6,1536
M v[32:35],a[148:151],a[196:199],v[32:35]
M v[64:67],a[148:151],a[200:203],v[64:67]
T a[162:163],v6,1792
M v[64:67],a[144:147],a[180:183],v[64:67]
M v[96:99],a[144:147],a[184:187],v[96:99]
T a[164:165],v6,9984
M v[96:99],a[148:151],a[204:207],v[96:99]
M v[128:131],a[148:151],a[208:211],v[128:131]
T a[166:167],v6,10240
M v[128:131],a[144:147],a[188:191],v[128:131]
M v[160:163],a[144:147],a[192:195],v[160:163]
M v[160:163],a[148:151],a[212:215],v[160:163]
s_waitcnt lgkmcnt(6)
M v[36:39],a[152:155],a[176:179],v[36:39]
s_waitcnt lgkmcnt(4)
T a[168:169],v6,1544
M v[36:39],a[156:159],a[196:199],v[36:39]
M v[68:71],a[156:159],a[200:203],v[68:71]
T a[170:171],v6,1800
M v[68:71],a[152:155],a[180:183],v[68:71]
M v[100:103],a[152:155],a[184:187],v[100:103]
T a[172:173],v6,9992
M v[100:103],a[156:159],a[204:207],v[100:103]
M v[132:135],a[156:159],a[208:211],v[132:135]
T a[174:175],v6,10248
M v[132:135],a[152:155],a[188:191],v[132:135]
s_mov_b32 s64, s65
M v[164:167],a[152:155],a[192:195],v[164:167]
s_cmp_eq_u32 s65, 67584
s_cselect_b32 s66, s90, s76
M v[164:167],a[156:159],a[212:215],v[164:167]
s_waitcnt lgkmcnt(6)
s_add_u32 s65, s65, s66
M v[40:43],a[160:163],a[176:179],v[40:43]
s_waitcnt lgkmcnt(4)
M v[40:43],a[164:167],a[196:199],v[40:43]
s_waitcnt vmcnt(8) lgkmcnt(0)
v_add_u32 v6, s66, v6
M v[72:75],a[164:167],a[200:203],v[72:75]
s_barrier
M v[72:75],a[160:163],a[180:183],v[72:75]
s_add_u32 s70, s73, 0
T a[144:145],v6,0
M v[104:107],a[160:163],a[184:187],v[104:107]
s_mov_b32 m0, s70
T a[146:147],v6,256
M v[104:107],a[164:167],a[204:207],v[104:107]
T a[148:149],v6,8448
buffer_load_dwordx4 v8, s[44:47], s6 offen nt lds
M v[136:139],a[164:167],a[208:211],v[136:139]
T a[150:151],v6,8704
M v[136:139],a[160:163],a[188:191],v[136:139]
T a[152:153],v6,8
M v[168:171],a[160:163],a[192:195],v[168:171]
T a[154:155],v6,264
M v[168:171],a[164:167],a[212:215],v[168:171]
M v[44:47],a[168:171],a[176:179],v[44:47]
D a[176:179],v7,0
M v[44:47],a[172:175],a[196:199],v[44:47]
s_add_u32 s77, s77, 8192
D a[196:199],v11,0
buffer_load_dwordx4 v8, s[44:47], s6 offen offset:2048 nt lds
M v[76:79],a[172:175],a[200:203],v[76:79]
D a[200:203],v11,2048
M v[76:79],a[168:171],a[180:183],v[76:79]
D a[180:183],v7,2048
M v[108:111],a[168:171],a[184:187],v[108:111]
D a[184:187],v7,4096
M v[108:111],a[172:175],a[204:207],v[108:111]
D a[204:207],v11,4096
M v[140:143],a[172:175],a[208:211],v[140:143]
s_add_u32 m0, s70, 896
D a[208:211],v11,6144
M v[140:143],a[168:171],a[188:191],v[140:143]
D a[188:191],v7,6144
buffer_load_dwordx4 v8, s[44:47], s6 offen offset:128 nt lds
M v[172:175],a[168:171],a[192:195],v[172:175]
D a[192:195],v7,8192
M v[172:175],a[172:175],a[212:215],v[172:175]
D a[212:215],v11,8192
s_waitcnt lgkmcnt(9)
M v[176:179],a[144:147],a[176:179],v[176:179]
s_waitcnt lgkmcnt(8)
T a[160:161],v6,512
M v[176:179],a[148:151],a[196:199],v[176:179]
s_waitcnt lgkmcnt(8)
T a[156:157],v6,8456
M v[192:195],a[148:151],a[200:203],v[192:195]
s_waitcnt lgkmcnt(8)
T a[162:163],v6,768
M v[192:195],a[144:147],a[180:183],v[192:195]
s_waitcnt lgkmcnt(8)
s_add_u32 s71, s6, 4096
T a[158:159],v6,8712
buffer_load_dwordx4 v8, s[44:47], s6 offen offset:2176 nt lds
M v[208:211],a[144:147],a[184:187],v[208:211]
s_waitcnt lgkmcnt(8)
T a[164:165],v6,8960
M v[208:211],a[148:151],a[204:207],v[208:211]
s_waitcnt lgkmcnt(8)
M v[224:227],a[148:151],a[208:211],v[224:227]
s_waitcnt lgkmcnt(7)
T a[166:167],v6,9216
M v[224:227],a[144:147],a[188:191],v[224:227]
s_waitcnt lgkmcnt(7)
M v[240:243],a[144:147],a[192:195],v[240:243]
s_waitcnt lgkmcnt(6)
s_add_u32 m0, s70, 4096
M v[240:243],a[148:151],a[212:215],v[240:243]
buffer_load_dwordx4 v8, s[44:47], s71 offen nt lds
M v[180:183],a[152:155],a[176:179],v[180:183]
s_waitcnt lgkmcnt(2)
T a[168:169],v6,520
M v[180:183],a[156:159],a[196:199],v[180:183]
M v[196:199],a[156:159],a[200:203],v[196:199]
T a[170:171],v6,776
M v[196:199],a[152:155],a[180:183],v[196:199]
M v[212:215],a[152:155],a[184:187],v[212:215]
T a[172:173],v6,8968
M v[212:215],a[156:159],a[204:207],v[212:215]
s_and_b32 s77, s77, 131071
buffer_load_dwordx4 v8, s[44:47], s71 offen offset:2048 nt lds
M v[228:231],a[156:159],a[208:211],v[228:231]
T a[174:175],v6,9224
M v[228:231],a[152:155],a[188:191],v[228:231]
M v[244:247],a[152:155],a[192:195],v[244:247]
M v[244:247],a[156:159],a[212:215],v[244:247]
M v[184:187],a[160:163],a[176:179],v[184:187]
s_waitcnt lgkmcnt(4)
s_add_u32 m0, s70, 4992
T a[144:145],v6,1024
M v[184:187],a[164:167],a[196:199],v[184:187]
buffer_load_dwordx4 v8, s[44:47], s71 offen offset:128 nt lds
M v[200:203],a[164:167],a[200:203],v[200:203]
T a[146:147],v6,1280
M v[200:203],a[160:163],a[180:183],v[200:203]
M v[216:219],a[160:163],a[184:187],v[216:219]
T a[148:149],v6,9472
M v[216:219],a[164:167],a[204:207],v[216:219]
M v[232:235],a[164:167],a[208:211],v[232:235]
T a[150:151],v6,9728
M v[232:235],a[160:163],a[188:191],v[232:235]
s_cmp_eq_u32 s77, s4
s_cselect_b32 s5, 2097152, s5
buffer_load_dwordx4 v8, s[44:47], s71 offen offset:2176 nt lds
M v[248:251],a[160:163],a[192:195],v[248:251]
s_add_u32 s6, s77, s5
M v[248:251],a[164:167],a[212:215],v[248:251]
s_waitcnt lgkmcnt(6)
M v[188:191],a[168:171],a[176:179],v[188:191]
s_waitcnt lgkmcnt(4)
T a[152:153],v6,1032
M v[188:191],a[172:175],a[196:199],v[188:191]
M v[204:207],a[172:175],a[200:203],v[204:207]
T a[154:155],v6,1288
M v[204:207],a[168:171],a[180:183],v[204:207]
M v[220:223],a[168:171],a[184:187],v[220:223]
T a[156:157],v6,9480
M v[220:223],a[172:175],a[204:207],v[220:223]
M v[236:239],a[172:175],a[208:211],v[236:239]
T a[158:159],v6,9736
M v[236:239],a[168:171],a[188:191],v[236:239]
M v[252:255],a[168:171],a[192:195],v[252:255]
M v[252:255],a[172:175],a[212:215],v[252:255]
s_waitcnt lgkmcnt(6)
M a[0:3],a[144:147],a[176:179],a[0:3]
s_waitcnt lgkmcnt(4)
T a[160:161],v6,1536
M a[0:3],a[148:151],a[196:199],a[0:3]
M a[16:19],a[148:151],a[200:203],a[16:19]
T a[162:163],v6,1792
M a[16:19],a[144:147],a[180:183],a[16:19]
M a[32:35],a[144:147],a[184:187],a[32:35]
T a[164:165],v6,9984
M a[32:35],a[148:151],a[204:207],a[32:35]
M a[48:51],a[148:151],a[208:211],a[48:51]
T a[166:167],v6,10240
M a[48:51],a[144:147],a[188:191],a[48:51]
M a[64:67],a[144:147],a[192:195],a[64:67]
M a[64:67],a[148:151],a[212:215],a[64:67]
s_waitcnt lgkmcnt(6)
M a[4:7],a[152:155],a[176:179],a[4:7]
s_waitcnt lgkmcnt(4)
T a[168:169],v6,1544
M a[4:7],a[156:159],a[196:199],a[4:7]
M a[20:23],a[156:159],a[200:203],a[20:23]
T a[170:171],v6,1800
M a[20:23],a[152:155],a[180:183],a[20:23]
M a[36:39],a[152:155],a[184:187],a[36:39]
T a[172:173],v6,9992
M a[36:39],a[156:159],a[204:207],a[36:39]
M a[52:55],a[156:159],a[208:211],a[52:55]
T a[174:175],v6,10248
M a[52:55],a[152:155],a[188:191],a[52:55]
s_mov_b32 s64, s65
M a[68:71],a[152:155],a[192:195],a[68:71]
s_cmp_eq_u32 s65, 67584
s_cselect_b32 s66, s90, s76
M a[68:71],a[156:159],a[212:215],a[68:71]
s_waitcnt lgkmcnt(6)
s_add_u32 s65, s65, s66
M a[8:11],a[160:163],a[176:179],a[8:11]
s_waitcnt lgkmcnt(4)
M a[8:11],a[164:167],a[196:199],a[8:11]
s_waitcnt vmcnt(8) lgkmcnt(0)
v_add_u32 v6, s66, v6
M a[24:27],a[164:167],a[200:203],a[24:27]
s_barrier
M a[24:27],a[160:163],a[180:183],a[24:27]
s_add_u32 s70, s73, 32768
T a[144:145],v6,0
M a[40:43],a[160:163],a[184:187],a[40:43]
s_mov_b32 m0, s70
T a[146:147],v6,256
M a[40:43],a[164:167],a[204:207],a[40:43]
T a[148:149],v6,8448
buffer_load_dwordx4 v8, s[44:47], s6 offen nt lds
M a[56:59],a[164:167],a[208:211],a[56:59]
T a[150:151],v6,8704
M a[56:59],a[160:163],a[188:191],a[56:59]
T a[152:153],v6,8
M a[72:75],a[160:163],a[192:195],a[72:75]
T a[154:155],v6,264
M a[72:75],a[164:167],a[212:215],a[72:75]
M a[12:15],a[168:171],a[176:179],a[12:15]
M a[12:15],a[172:175],a[196:199],a[12:15]
s_add_u32 s77, s77, 8192
buffer_load_dwordx4 v8, s[44:47], s6 offen offset:2048 nt lds
M a[28:31],a[172:175],a[200:203],a[28:31]
M a[28:31],a[168:171],a[180:183],a[28:31]
M a[44:47],a[168:171],a[184:187],a[44:47]
M a[44:47],a[172:175],a[204:207],a[44:47]
M a[60:63],a[172:175],a[208:211],a[60:63]
s_add_u32 m0, s70, 896
M a[60:63],a[168:171],a[188:191],a[60:63]
buffer_load_dwordx4 v8, s[44:47], s6 offen offset:128 nt lds
M a[76:79],a[168:171],a[192:195],a[76:79]
M a[76:79],a[172:175],a[212:215],a[76:79]
s_waitcnt lgkmcnt(4)
M v[16:19],a[144:147],a[176:179],v[16:19]
s_waitcnt lgkmcnt(2)
T a[160:161],v6,512
M v[16:19],a[148:151],a[196:199],v[16:19]
T a[156:157],v6,8456
M v[48:51],a[148:151],a[200:203],v[48:51]
T a[162:163],v6,768
M v[48:51],a[144:147],a[180:183],v[48:51]
s_add_u32 s71, s6, 4096
T a[158:159],v6,8712
)"
R"(buffer_load_dwordx4 v8, s[44:47], s6 offen offset:2176 nt lds
M v[80:83],a[144:147],a[184:187],v[80:83]
T a[164:165],v6,8960
v_mul_f32 v176, s92, v176
M v[80:83],a[148:151],a[204:207],v[80:83]
v_mul_f32 v177, s92, v177
v_mul_f32 v178, s92, v178
M v[112:115],a[148:151],a[208:211],v[112:115]
T a[166:167],v6,9216
v_mul_f32 v179, s92, v179
M v[112:115],a[144:147],a[188:191],v[112:115]
C v176,v176,v177
C v177,v178,v179
M v[144:147],a[144:147],a[192:195],v[144:147]
s_add_u32 m0, s70, 4096
v_mul_f32 v180, s92, v180
v_mul_f32 v181, s92, v181
M v[144:147],a[148:151],a[212:215],v[144:147]
s_waitcnt lgkmcnt(6)
buffer_load_dwordx4 v8, s[44:47], s71 offen nt lds
v_mul_f32 v182, s92, v182
M v[20:23],a[152:155],a[176:179],v[20:23]
s_waitcnt lgkmcnt(2)
T a[168:169],v6,520
v_mul_f32 v183, s92, v183
M v[20:23],a[156:159],a[196:199],v[20:23]
C v178,v180,v181
C v179,v182,v183
M v[52:55],a[156:159],a[200:203],v[52:55]
T a[170:171],v6,776
v_mul_f32 v184, s92, v184
M v[52:55],a[152:155],a[180:183],v[52:55]
v_mul_f32 v185, s92, v185
v_mul_f32 v186, s92, v186
M v[84:87],a[152:155],a[184:187],v[84:87]
T a[172:173],v6,8968
v_mul_f32 v187, s92, v187
M v[84:87],a[156:159],a[204:207],v[84:87]
s_and_b32 s77, s77, 131071
buffer_load_dwordx4 v8, s[44:47], s71 offen offset:2048 nt lds
C v180,v184,v185
M v[116:119],a[156:159],a[208:211],v[116:119]
T a[174:175],v6,9224
C v181,v186,v187
M v[116:119],a[152:155],a[188:191],v[116:119]
v_mul_f32 v188, s92, v188
v_mul_f32 v189, s92, v189
M v[148:151],a[152:155],a[192:195],v[148:151]
v_mul_f32 v190, s92, v190
v_mul_f32 v191, s92, v191
M v[148:151],a[156:159],a[212:215],v[148:151]
C v182,v188,v189
C v183,v190,v191
M v[24:27],a[160:163],a[176:179],v[24:27]
s_waitcnt lgkmcnt(4)
s_add_u32 m0, s70, 4992
T a[144:145],v6,1024
v_mul_f32 v192, s92, v192
M v[24:27],a[164:167],a[196:199],v[24:27]
buffer_load_dwordx4 v8, s[44:47], s71 offen offset:128 nt lds
v_mul_f32 v193, s92, v193
M v[56:59],a[164:167],a[200:203],v[56:59]
T a[146:147],v6,1280
v_mul_f32 v194, s92, v194
M v[56:59],a[160:163],a[180:183],v[56:59]
v_mul_f32 v195, s92, v195
C v192,v192,v193
M v[88:91],a[160:163],a[184:187],v[88:91]
T a[148:149],v6,9472
C v193,v194,v195
M v[88:91],a[164:167],a[204:207],v[88:91]
v_mul_f32 v196, s92, v196
v_mul_f32 v197, s92, v197
M v[120:123],a[164:167],a[208:211],v[120:123]
T a[150:151],v6,9728
v_mul_f32 v198, s92, v198
M v[120:123],a[160:163],a[188:191],v[120:123]
s_cmp_eq_u32 s77, s4
s_cselect_b32 s5, 2097152, s5
buffer_load_dwordx4 v8, s[44:47], s71 offen offset:2176 nt lds
v_mul_f32 v199, s92, v199
M v[152:155],a[160:163],a[192:195],v[152:155]
s_add_u32 s6, s77, s5
C v194,v196,v197
C v195,v198,v199
M v[152:155],a[164:167],a[212:215],v[152:155]
s_waitcnt lgkmcnt(6)
v_mul_f32 v200, s92, v200
v_mul_f32 v201, s92, v201
M v[28:31],a[168:171],a[176:179],v[28:31]
s_waitcnt lgkmcnt(4)
T a[152:153],v6,1032
v_mul_f32 v202, s92, v202
M v[28:31],a[172:175],a[196:199],v[28:31]
v_mul_f32 v203, s92, v203
C v196,v200,v201
M v[60:63],a[172:175],a[200:203],v[60:63]
T a[154:155],v6,1288
C v197,v202,v203
M v[60:63],a[168:171],a[180:183],v[60:63]
v_mul_f32 v204, s92, v204
v_mul_f32 v205, s92, v205
M v[92:95],a[168:171],a[184:187],v[92:95]
T a[156:157],v6,9480
v_mul_f32 v206, s92, v206
M v[92:95],a[172:175],a[204:207],v[92:95]
v_mul_f32 v207, s92, v207
C v198,v204,v205
M v[124:127],a[172:175],a[208:211],v[124:127]
T a[158:159],v6,9736
C v199,v206,v207
M v[124:127],a[168:171],a[188:191],v[124:127]
v_mul_f32 v208, s92, v208
v_mul_f32 v209, s92, v209
M v[156:159],a[168:171],a[192:195],v[156:159]
v_mul_f32 v210, s92, v210
v_mul_f32 v211, s92, v211
M v[156:159],a[172:175],a[212:215],v[156:159]
s_waitcnt lgkmcnt(6)
C v208,v208,v209
C v209,v210,v211
M v[32:35],a[144:147],a[176:179],v[32:35]
s_waitcnt lgkmcnt(4)
T a[160:161],v6,1536
v_mul_f32 v212, s92, v212
M v[32:35],a[148:151],a[196:199],v[32:35]
v_mul_f32 v213, s92, v213
v_mul_f32 v214, s92, v214
M v[64:67],a[148:151],a[200:203],v[64:67]
T a[162:163],v6,1792
v_mul_f32 v215, s92, v215
M v[64:67],a[144:147],a[180:183],v[64:67]
C v210,v212,v213
C v211,v214,v215
M v[96:99],a[144:147],a[184:187],v[96:99]
T a[164:165],v6,9984
v_mul_f32 v216, s92, v216
M v[96:99],a[148:151],a[204:207],v[96:99]
v_mul_f32 v217, s92, v217
v_mul_f32 v218, s92, v218
M v[128:131],a[148:151],a[208:211],v[128:131]
T a[166:167],v6,10240
v_mul_f32 v219, s92, v219
M v[128:131],a[144:147],a[188:191],v[128:131]
C v212,v216,v217
C v213,v218,v219
M v[160:163],a[144:147],a[192:195],v[160:163]
v_mul_f32 v220, s92, v220
v_mul_f32 v221, s92, v221
M v[160:163],a[148:151],a[212:215],v[160:163]
s_waitcnt lgkmcnt(6)
v_mul_f32 v222, s92, v222
v_mul_f32 v223, s92, v223
M v[36:39],a[152:155],a[176:179],v[36:39]
s_waitcnt lgkmcnt(4)
T a[168:169],v6,1544
C v214,v220,v221
M v[36:39],a[156:159],a[196:199],v[36:39]
C v215,v222,v223
v_mul_f32 v224, s92, v224
M v[68:71],a[156:159],a[200:203],v[68:71]
T a[170:171],v6,1800
v_mul_f32 v225, s92, v225
M v[68:71],a[152:155],a[180:183],v[68:71]
v_mul_f32 v226, s92, v226
v_mul_f32 v227, s92, v227
M v[100:103],a[152:155],a[184:187],v[100:103]
T a[172:173],v6,9992
C v224,v224,v225
M v[100:103],a[156:159],a[204:207],v[100:103]
C v225,v226,v227
v_mul_f32 v228, s92, v228
M v[132:135],a[156:159],a[208:211],v[132:135]
T a[174:175],v6,10248
v_mul_f32 v229, s92, v229
M v[132:135],a[152:155],a[188:191],v[132:135]
v_mul_f32 v230, s92, v230
v_mul_f32 v231, s92, v231
M v[164:167],a[152:155],a[192:195],v[164:167]
C v226,v228,v229
C v227,v230,v231
M v[164:167],a[156:159],a[212:215],v[164:167]
s_waitcnt lgkmcnt(6)
v_mul_f32 v232, s92, v232
v_mul_f32 v233, s92, v233
M v[40:43],a[160:163],a[176:179],v[40:43]
s_waitcnt lgkmcnt(4)
v_mul_f32 v234, s92, v234
v_mul_f32 v235, s92, v235
M v[40:43],a[164:167],a[196:199],v[40:43]
s_waitcnt vmcnt(8) lgkmcnt(0)
C v228,v232,v233
C v229,v234,v235
M v[72:75],a[164:167],a[200:203],v[72:75]
s_barrier
M v[72:75],a[160:163],a[180:183],v[72:75]
s_add_u32 s70, s73, 65536
v_mul_f32 v236, s92, v236
v_mul_f32 v237, s92, v237
M v[104:107],a[160:163],a[184:187],v[104:107]
s_mov_b32 m0, s70
v_mul_f32 v238, s92, v238
M v[104:107],a[164:167],a[204:207],v[104:107]
buffer_load_dwordx4 v8, s[44:47], s6 offen nt lds
v_mul_f32 v239, s92, v239
M v[136:139],a[164:167],a[208:211],v[136:139]
C v230,v236,v237
C v231,v238,v239
M v[136:139],a[160:163],a[188:191],v[136:139]
v_mul_f32 v240, s92, v240
v_mul_f32 v241, s92, v241
M v[168:171],a[160:163],a[192:195],v[168:171]
v_mul_f32 v242, s92, v242
v_mul_f32 v243, s92, v243
M v[168:171],a[164:167],a[212:215],v[168:171]
s_add_u32 s77, s77, 8192
buffer_load_dwordx4 v8, s[44:47], s6 offen offset:2048 nt lds
M v[44:47],a[168:171],a[176:179],v[44:47]
C v240,v240,v241
C v241,v242,v243
M v[44:47],a[172:175],a[196:199],v[44:47]
v_mul_f32 v244, s92, v244
v_mul_f32 v245, s92, v245
M v[76:79],a[172:175],a[200:203],v[76:79]
s_add_u32 m0, s70, 896
v_mul_f32 v246, s92, v246
v_mul_f32 v247, s92, v247
M v[76:79],a[168:171],a[180:183],v[76:79]
buffer_load_dwordx4 v8, s[44:47], s6 offen offset:128 nt lds
C v242,v244,v245
M v[108:111],a[168:171],a[184:187],v[108:111]
C v243,v246,v247
v_mul_f32 v248, s92, v248
M v[108:111],a[172:175],a[204:207],v[108:111]
v_mul_f32 v249, s92, v249
v_mul_f32 v250, s92, v250
M v[140:143],a[172:175],a[208:211],v[140:143]
v_mul_f32 v251, s92, v251
C v244,v248,v249
M v[140:143],a[168:171],a[188:191],v[140:143]
s_add_u32 s71, s6, 4096
buffer_load_dwordx4 v8, s[44:47], s6 offen offset:2176 nt lds
C v245,v250,v251
M v[172:175],a[168:171],a[192:195],v[172:175]
v_mul_f32 v252, s92, v252
v_mul_f32 v253, s92, v253
M v[172:175],a[172:175],a[212:215],v[172:175]
v_mul_f32 v254, s92, v254
v_mul_f32 v255, s92, v255
D a[160:163],v2,0
D a[164:167],v3,0
D a[168:171],v2,1024
D a[172:175],v3,1024
D a[176:179],v2,4096
D a[180:183],v3,4096
D a[184:187],v2,5120
D a[188:191],v3,5120
s_nop 3
C v246,v252,v253
C v247,v254,v255
R v184,a0
s_add_u32 m0, s70, 4096
s_nop 0
buffer_load_dwordx4 v8, s[44:47], s71 offen nt lds
R v185,a1
R v186,a2
R v187,a3
v_pk_mul_f32 v[184:185], v[184:185], s[92:93] op_sel_hi:[1,0]
v_pk_mul_f32 v[186:187], v[186:187], s[92:93] op_sel_hi:[1,0]
C v184,v184,v185
C v185,v186,v187
R v186,a4
s_and_b32 s77, s77, 131071
buffer_load_dwordx4 v8, s[44:47], s71 offen offset:2048 nt lds
R v187,a5
R v188,a6
R v189,a7
v_pk_mul_f32 v[186:187], v[186:187], s[92:93] op_sel_hi:[1,0]
v_pk_mul_f32 v[188:189], v[188:189], s[92:93] op_sel_hi:[1,0]
C v186,v186,v187
C v187,v188,v189
R v188,a8
s_add_u32 m0, s70, 4992
s_nop 0
buffer_load_dwordx4 v8, s[44:47], s71 offen offset:128 nt lds
R v189,a9
R v190,a10
R v191,a11
v_pk_mul_f32 v[188:189], v[188:189], s[92:93] op_sel_hi:[1,0]
v_pk_mul_f32 v[190:191], v[190:191], s[92:93] op_sel_hi:[1,0]
C v188,v188,v189
C v189,v190,v191
R v190,a12
s_cmp_eq_u32 s77, s4
s_cselect_b32 s5, 2097152, s5
buffer_load_dwordx4 v8, s[44:47], s71 offen offset:2176 nt lds
R v191,a13
R v12,a14
R v15,a15
v_pk_mul_f32 v[190:191], v[190:191], s[92:93] op_sel_hi:[1,0]
v_mul_f32 v12, s92, v12
v_mul_f32 v15, s92, v15
C v190,v190,v191
C v191,v12,v15
s_add_u32 s6, s77, s5
R v200,a16
R v201,a17
R v202,a18
R v203,a19
v_pk_mul_f32 v[200:201], v[200:201], s[92:93] op_sel_hi:[1,0]
v_pk_mul_f32 v[202:203], v[202:203], s[92:93] op_sel_hi:[1,0]
C v200,v200,v201
C v201,v202,v203
R v202,a20
R v203,a21
R v204,a22
R v205,a23
v_pk_mul_f32 v[202:203], v[202:203], s[92:93] op_sel_hi:[1,0]
v_pk_mul_f32 v[204:205], v[204:205], s[92:93] op_sel_hi:[1,0]
C v202,v202,v203
C v203,v204,v205
R v204,a24
R v205,a25
R v206,a26
R v207,a27
v_pk_mul_f32 v[204:205], v[204:205], s[92:93] op_sel_hi:[1,0]
v_pk_mul_f32 v[206:207], v[206:207], s[92:93] op_sel_hi:[1,0]
C v204,v204,v205
C v205,v206,v207
R v206,a28
R v207,a29
R v12,a30
R v15,a31
v_pk_mul_f32 v[206:207], v[206:207], s[92:93] op_sel_hi:[1,0]
v_mul_f32 v12, s92, v12
v_mul_f32 v15, s92, v15
C v206,v206,v207
C v207,v12,v15
R v216,a32
R v217,a33
R v218,a34
R v219,a35
v_pk_mul_f32 v[216:217], v[216:217], s[92:93] op_sel_hi:[1,0]
v_pk_mul_f32 v[218:219], v[218:219], s[92:93] op_sel_hi:[1,0]
C v216,v216,v217
C v217,v218,v219
R v218,a36
R v219,a37
R v220,a38
R v221,a39
v_pk_mul_f32 v[218:219], v[218:219], s[92:93] op_sel_hi:[1,0]
v_pk_mul_f32 v[220:221], v[220:221], s[92:93] op_sel_hi:[1,0]
C v218,v218,v219
C v219,v220,v221
R v220,a40
R v221,a41
R v222,a42
R v223,a43
v_pk_mul_f32 v[220:221], v[220:221], s[92:93] op_sel_hi:[1,0]
v_pk_mul_f32 v[222:223], v[222:223], s[92:93] op_sel_hi:[1,0]
C v220,v220,v221
C v221,v222,v223
R v222,a44
R v223,a45
R v12,a46
R v15,a47
v_pk_mul_f32 v[222:223], v[222:223], s[92:93] op_sel_hi:[1,0]
v_mul_f32 v12, s92, v12
v_mul_f32 v15, s92, v15
C v222,v222,v223
C v223,v12,v15
R v232,a48
R v233,a49
R v234,a50
R v235,a51
v_pk_mul_f32 v[232:233], v[232:233], s[92:93] op_sel_hi:[1,0]
v_pk_mul_f32 v[234:235], v[234:235], s[92:93] op_sel_hi:[1,0]
C v232,v232,v233
C v233,v234,v235
R v234,a52
R v235,a53
R v236,a54
R v237,a55
v_pk_mul_f32 v[234:235], v[234:235], s[92:93] op_sel_hi:[1,0]
v_pk_mul_f32 v[236:237], v[236:237], s[92:93] op_sel_hi:[1,0]
C v234,v234,v235
C v235,v236,v237
R v236,a56
R v237,a57
R v238,a58
R v239,a59
v_pk_mul_f32 v[236:237], v[236:237], s[92:93] op_sel_hi:[1,0]
v_pk_mul_f32 v[238:239], v[238:239], s[92:93] op_sel_hi:[1,0]
C v236,v236,v237
C v237,v238,v239
R v238,a60
R v239,a61
R v12,a62
R v15,a63
v_pk_mul_f32 v[238:239], v[238:239], s[92:93] op_sel_hi:[1,0]
v_mul_f32 v12, s92, v12
v_mul_f32 v15, s92, v15
C v238,v238,v239
C v239,v12,v15
R v248,a64
R v249,a65
R v250,a66
R v251,a67
v_pk_mul_f32 v[248:249], v[248:249], s[92:93] op_sel_hi:[1,0]
v_pk_mul_f32 v[250:251], v[250:251], s[92:93] op_sel_hi:[1,0]
C v248,v248,v249
C v249,v250,v251
R v250,a68
R v251,a69
R v252,a70
R v253,a71
v_pk_mul_f32 v[250:251], v[250:251], s[92:93] op_sel_hi:[1,0]
v_pk_mul_f32 v[252:253], v[252:253], s[92:93] op_sel_hi:[1,0]
C v250,v250,v251
C v251,v252,v253
R v252,a72
R v253,a73
R v254,a74
R v255,a75
v_pk_mul_f32 v[252:253], v[252:253], s[92:93] op_sel_hi:[1,0]
v_pk_mul_f32 v[254:255], v[254:255], s[92:93] op_sel_hi:[1,0]
C v252,v252,v253
C v253,v254,v255
R v254,a76
R v255,a77
R v12,a78
R v15,a79
v_pk_mul_f32 v[254:255], v[254:255], s[92:93] op_sel_hi:[1,0]
v_mul_f32 v12, s92, v12
v_mul_f32 v15, s92, v15
C v254,v254,v255
C v255,v12,v15
v_pk_mul_f32 v[16:17], v[16:17], s[92:93] op_sel_hi:[1,0]
v_pk_mul_f32 v[18:19], v[18:19], s[92:93] op_sel_hi:[1,0]
C v16,v16,v17
C v17,v18,v19
v_pk_mul_f32 v[20:21], v[20:21], s[92:93] op_sel_hi:[1,0]
v_pk_mul_f32 v[22:23], v[22:23], s[92:93] op_sel_hi:[1,0]
C v18,v20,v21
C v19,v22,v23
v_pk_mul_f32 v[24:25], v[24:25], s[92:93] op_sel_hi:[1,0]
v_pk_mul_f32 v[26:27], v[26:27], s[92:93] op_sel_hi:[1,0]
C v20,v24,v25
C v21,v26,v27
v_pk_mul_f32 v[28:29], v[28:29], s[92:93] op_sel_hi:[1,0]
v_pk_mul_f32 v[30:31], v[30:31], s[92:93] op_sel_hi:[1,0]
C v22,v28,v29
C v23,v30,v31
v_pk_mul_f32 v[32:33], v[32:33], s[92:93] op_sel_hi:[1,0]
v_pk_mul_f32 v[34:35], v[34:35], s[92:93] op_sel_hi:[1,0]
C v24,v32,v33
C v25,v34,v35
v_pk_mul_f32 v[36:37], v[36:37], s[92:93] op_sel_hi:[1,0]
v_pk_mul_f32 v[38:39], v[38:39], s[92:93] op_sel_hi:[1,0]
C v26,v36,v37
C v27,v38,v39
v_pk_mul_f32 v[40:41], v[40:41], s[92:93] op_sel_hi:[1,0]
v_pk_mul_f32 v[42:43], v[42:43], s[92:93] op_sel_hi:[1,0]
C v28,v40,v41
C v29,v42,v43
v_pk_mul_f32 v[44:45], v[44:45], s[92:93] op_sel_hi:[1,0]
v_pk_mul_f32 v[46:47], v[46:47], s[92:93] op_sel_hi:[1,0]
C v30,v44,v45
C v31,v46,v47
v_pk_mul_f32 v[48:49], v[48:49], s[92:93] op_sel_hi:[1,0]
v_pk_mul_f32 v[50:51], v[50:51], s[92:93] op_sel_hi:[1,0]
C v48,v48,v49
C v49,v50,v51
v_pk_mul_f32 v[52:53], v[52:53], s[92:93] op_sel_hi:[1,0]
v_pk_mul_f32 v[54:55], v[54:55], s[92:93] op_sel_hi:[1,0]
C v50,v52,v53
C v51,v54,v55
v_pk_mul_f32 v[56:57], v[56:57], s[92:93] op_sel_hi:[1,0]
v_pk_mul_f32 v[58:59], v[58:59], s[92:93] op_sel_hi:[1,0]
C v52,v56,v57
C v53,v58,v59
v_pk_mul_f32 v[60:61], v[60:61], s[92:93] op_sel_hi:[1,0]
v_pk_mul_f32 v[62:63], v[62:63], s[92:93] op_sel_hi:[1,0]
C v54,v60,v61
C v55,v62,v63
v_pk_mul_f32 v[64:65], v[64:65], s[92:93] op_sel_hi:[1,0]
v_pk_mul_f32 v[66:67], v[66:67], s[92:93] op_sel_hi:[1,0]
C v56,v64,v65
C v57,v66,v67
v_pk_mul_f32 v[68:69], v[68:69], s[92:93] op_sel_hi:[1,0]
v_pk_mul_f32 v[70:71], v[70:71], s[92:93] op_sel_hi:[1,0]
C v58,v68,v69
C v59,v70,v71
v_pk_mul_f32 v[72:73], v[72:73], s[92:93] op_sel_hi:[1,0]
v_pk_mul_f32 v[74:75], v[74:75], s[92:93] op_sel_hi:[1,0]
C v60,v72,v73
C v61,v74,v75
v_pk_mul_f32 v[76:77], v[76:77], s[92:93] op_sel_hi:[1,0]
v_pk_mul_f32 v[78:79], v[78:79], s[92:93] op_sel_hi:[1,0]
C v62,v76,v77
)"
R"(C v63,v78,v79
v_pk_mul_f32 v[80:81], v[80:81], s[92:93] op_sel_hi:[1,0]
v_pk_mul_f32 v[82:83], v[82:83], s[92:93] op_sel_hi:[1,0]
C v80,v80,v81
C v81,v82,v83
v_pk_mul_f32 v[84:85], v[84:85], s[92:93] op_sel_hi:[1,0]
v_pk_mul_f32 v[86:87], v[86:87], s[92:93] op_sel_hi:[1,0]
C v82,v84,v85
C v83,v86,v87
v_pk_mul_f32 v[88:89], v[88:89], s[92:93] op_sel_hi:[1,0]
v_pk_mul_f32 v[90:91], v[90:91], s[92:93] op_sel_hi:[1,0]
C v84,v88,v89
C v85,v90,v91
v_pk_mul_f32 v[92:93], v[92:93], s[92:93] op_sel_hi:[1,0]
v_pk_mul_f32 v[94:95], v[94:95], s[92:93] op_sel_hi:[1,0]
C v86,v92,v93
C v87,v94,v95
v_pk_mul_f32 v[96:97], v[96:97], s[92:93] op_sel_hi:[1,0]
v_pk_mul_f32 v[98:99], v[98:99], s[92:93] op_sel_hi:[1,0]
C v88,v96,v97
C v89,v98,v99
v_pk_mul_f32 v[100:101], v[100:101], s[92:93] op_sel_hi:[1,0]
v_pk_mul_f32 v[102:103], v[102:103], s[92:93] op_sel_hi:[1,0]
C v90,v100,v101
C v91,v102,v103
v_pk_mul_f32 v[104:105], v[104:105], s[92:93] op_sel_hi:[1,0]
v_pk_mul_f32 v[106:107], v[106:107], s[92:93] op_sel_hi:[1,0]
C v92,v104,v105
C v93,v106,v107
v_pk_mul_f32 v[108:109], v[108:109], s[92:93] op_sel_hi:[1,0]
v_pk_mul_f32 v[110:111], v[110:111], s[92:93] op_sel_hi:[1,0]
C v94,v108,v109
C v95,v110,v111
v_pk_mul_f32 v[112:113], v[112:113], s[92:93] op_sel_hi:[1,0]
v_pk_mul_f32 v[114:115], v[114:115], s[92:93] op_sel_hi:[1,0]
C v112,v112,v113
C v113,v114,v115
v_pk_mul_f32 v[116:117], v[116:117], s[92:93] op_sel_hi:[1,0]
v_pk_mul_f32 v[118:119], v[118:119], s[92:93] op_sel_hi:[1,0]
C v114,v116,v117
C v115,v118,v119
v_pk_mul_f32 v[120:121], v[120:121], s[92:93] op_sel_hi:[1,0]
v_pk_mul_f32 v[122:123], v[122:123], s[92:93] op_sel_hi:[1,0]
C v116,v120,v121
C v117,v122,v123
v_pk_mul_f32 v[124:125], v[124:125], s[92:93] op_sel_hi:[1,0]
v_pk_mul_f32 v[126:127], v[126:127], s[92:93] op_sel_hi:[1,0]
C v118,v124,v125
C v119,v126,v127
v_pk_mul_f32 v[128:129], v[128:129], s[92:93] op_sel_hi:[1,0]
v_pk_mul_f32 v[130:131], v[130:131], s[92:93] op_sel_hi:[1,0]
C v120,v128,v129
C v121,v130,v131
v_pk_mul_f32 v[132:133], v[132:133], s[92:93] op_sel_hi:[1,0]
v_pk_mul_f32 v[134:135], v[134:135], s[92:93] op_sel_hi:[1,0]
C v122,v132,v133
C v123,v134,v135
v_pk_mul_f32 v[136:137], v[136:137], s[92:93] op_sel_hi:[1,0]
v_pk_mul_f32 v[138:139], v[138:139], s[92:93] op_sel_hi:[1,0]
C v124,v136,v137
C v125,v138,v139
v_pk_mul_f32 v[140:141], v[140:141], s[92:93] op_sel_hi:[1,0]
v_pk_mul_f32 v[142:143], v[142:143], s[92:93] op_sel_hi:[1,0]
C v126,v140,v141
C v127,v142,v143
v_pk_mul_f32 v[144:145], v[144:145], s[92:93] op_sel_hi:[1,0]
v_pk_mul_f32 v[146:147], v[146:147], s[92:93] op_sel_hi:[1,0]
C v144,v144,v145
C v145,v146,v147
v_pk_mul_f32 v[148:149], v[148:149], s[92:93] op_sel_hi:[1,0]
v_pk_mul_f32 v[150:151], v[150:151], s[92:93] op_sel_hi:[1,0]
C v146,v148,v149
C v147,v150,v151
v_pk_mul_f32 v[152:153], v[152:153], s[92:93] op_sel_hi:[1,0]
v_pk_mul_f32 v[154:155], v[154:155], s[92:93] op_sel_hi:[1,0]
C v148,v152,v153
C v149,v154,v155
v_pk_mul_f32 v[156:157], v[156:157], s[92:93] op_sel_hi:[1,0]
v_pk_mul_f32 v[158:159], v[158:159], s[92:93] op_sel_hi:[1,0]
C v150,v156,v157
C v151,v158,v159
v_pk_mul_f32 v[160:161], v[160:161], s[92:93] op_sel_hi:[1,0]
v_pk_mul_f32 v[162:163], v[162:163], s[92:93] op_sel_hi:[1,0]
C v152,v160,v161
C v153,v162,v163
v_pk_mul_f32 v[164:165], v[164:165], s[92:93] op_sel_hi:[1,0]
v_pk_mul_f32 v[166:167], v[166:167], s[92:93] op_sel_hi:[1,0]
C v154,v164,v165
C v155,v166,v167
v_pk_mul_f32 v[168:169], v[168:169], s[92:93] op_sel_hi:[1,0]
v_pk_mul_f32 v[170:171], v[170:171], s[92:93] op_sel_hi:[1,0]
C v156,v168,v169
C v157,v170,v171
v_pk_mul_f32 v[172:173], v[172:173], s[92:93] op_sel_hi:[1,0]
v_pk_mul_f32 v[174:175], v[174:175], s[92:93] op_sel_hi:[1,0]
C v158,v172,v173
C v159,v174,v175
s_mov_b32 s65, 0
s_mov_b32 s76, 32768
s_mov_b32 s90, -131072
L_ph_at0X:
s_memrealtime s[18:19]
s_waitcnt lgkmcnt(0)
s_add_u32 s3, s18, 44
M v[36:39],a[160:163],v[176:179],0
T a[192:193],v4,0
M v[36:39],a[164:167],v[180:183],v[36:39]
T a[194:195],v4,4096
M v[36:39],a[168:171],v[184:187],v[36:39]
T a[196:197],v5,0
M v[36:39],a[172:175],v[188:191],v[36:39]
T a[198:199],v5,4096
M v[40:43],a[176:179],v[176:179],0
T a[200:201],v4,512
M v[40:43],a[180:183],v[180:183],v[40:43]
T a[202:203],v4,4608
M v[40:43],a[184:187],v[184:187],v[40:43]
T a[204:205],v5,512
M v[40:43],a[188:191],v[188:191],v[40:43]
T a[206:207],v5,4608
v_max3_f32 v32, v36, v37, v38
T a[208:209],v4,1024
T a[210:211],v4,5120
s_nop 3
v_max3_f32 v33, v40, v41, v42
v_max3_f32 v32, v32, v39, v43
v_max_f32 v32, v32, v33
v_mov_b32 v33, v32
s_nop 1
v_permlane32_swap_b32 v32, v33
v_max_f32 v32, v32, v33
v_mov_b32 v33, v32
s_nop 1
v_permlane16_swap_b32 v32, v33
M v[68:71],a[160:163],v[192:195],0
v_max_f32 v32, v32, v33
v_sub_f32 v33, 0, v32
M v[68:71],a[164:167],v[196:199],v[68:71]
v_add_f32 v36, v33, v36
v_add_f32 v37, v33, v37
M v[68:71],a[168:171],v[200:203],v[68:71]
v_add_f32 v38, v33, v38
v_add_f32 v39, v33, v39
M v[68:71],a[172:175],v[204:207],v[68:71]
E v36,v36
M v[72:75],a[176:179],v[192:195],0
v_add_f32 v40, v33, v40
v_add_f32 v41, v33, v41
M v[72:75],a[180:183],v[196:199],v[72:75]
E v37,v37
M v[72:75],a[184:187],v[200:203],v[72:75]
E v38,v38
M v[72:75],a[188:191],v[204:207],v[72:75]
v_add_f32 v42, v33, v42
v_add_f32 v43, v33, v43
T a[212:213],v5,1024
E v39,v39
v_max3_f32 v64, v68, v69, v70
E v40,v40
v_mov_b32 v32, v33
E v43,v43
v_max3_f32 v65, v72, v73, v74
v_max3_f32 v64, v64, v71, v75
v_max_f32 v64, v64, v65
E v41,v41
E v42,v42
v_mov_b32 v65, v64
s_nop 1
v_permlane32_swap_b32 v64, v65
v_mov_b32 v34, v33
v_mov_b32 v35, v33
C v44,v36,v37
C v45,v38,v39
v_max_f32 v64, v64, v65
v_mov_b32 v65, v64
M v[36:39],a[160:163],v[208:211],0
s_nop 0
v_permlane16_swap_b32 v64, v65
M v[36:39],a[164:167],v[212:215],v[36:39]
v_max_f32 v64, v64, v65
v_sub_f32 v65, 0, v64
M v[36:39],a[168:171],v[216:219],v[36:39]
C v46,v40,v41
C v47,v42,v43
M v[36:39],a[172:175],v[220:223],v[36:39]
T a[222:223],v5,5632
v_add_f32 v68, v65, v68
v_add_f32 v69, v65, v69
M v[40:43],a[176:179],v[208:211],0
T a[214:215],v5,5120
v_add_f32 v70, v65, v70
M v[40:43],a[180:183],v[212:215],v[40:43]
T a[216:217],v4,1536
v_add_f32 v71, v65, v71
M v[40:43],a[184:187],v[216:219],v[40:43]
T a[218:219],v4,5632
v_add_f32 v72, v65, v72
M v[40:43],a[188:191],v[220:223],v[40:43]
s_waitcnt lgkmcnt(13)
T a[220:221],v5,1536
E v68,v68
M a[0:3],a[192:195],v[44:47],0
s_waitcnt lgkmcnt(12)
E v69,v69
M a[4:7],a[196:199],v[44:47],0
s_waitcnt lgkmcnt(10)
v_add_f32 v73, v65, v73
E v70,v70
M a[8:11],a[200:203],v[44:47],0
s_waitcnt lgkmcnt(8)
v_add_f32 v74, v65, v74
E v71,v71
M a[12:15],a[204:207],v[44:47],0
v_add_f32 v75, v65, v75
E v72,v72
v_max3_f32 v96, v36, v37, v38
v_max3_f32 v97, v40, v41, v42
M a[228:231],a[224:227],v[44:47],0
s_waitcnt lgkmcnt(6)
M a[16:19],a[208:211],v[44:47],0
s_waitcnt lgkmcnt(3)
v_mov_b32 v66, v65
E v73,v73
E v75,v75
v_max3_f32 v96, v96, v39, v43
v_max_f32 v96, v96, v97
M a[20:23],a[212:215],v[44:47],0
s_waitcnt lgkmcnt(1)
E v74,v74
v_mov_b32 v97, v96
M a[24:27],a[216:219],v[44:47],0
s_waitcnt lgkmcnt(0)
v_mov_b32 v64, v65
v_permlane32_swap_b32 v96, v97
M a[28:31],a[220:223],v[44:47],0
s_memrealtime s[18:19]
v_mov_b32 v67, v65
C v76,v68,v69
C v77,v70,v71
v_max_f32 v96, v96, v97
v_mov_b32 v97, v96
M v[68:71],a[160:163],v[224:227],0
s_nop 0
v_permlane16_swap_b32 v96, v97
M v[68:71],a[164:167],v[228:231],v[68:71]
v_max_f32 v96, v96, v97
v_sub_f32 v97, 0, v96
M v[68:71],a[168:171],v[232:235],v[68:71]
s_mov_b32 s64, s65
C v78,v72,v73
C v79,v74,v75
M v[68:71],a[172:175],v[236:239],v[68:71]
s_cmp_eq_u32 s65, 131072
s_cselect_b32 s66, s90, s76
v_add_f32 v36, v97, v36
M v[72:75],a[176:179],v[224:227],0
s_add_u32 s65, s65, s66
v_add_f32 v37, v97, v37
v_add_f32 v38, v97, v38
v_mov_b32 v96, v97
M v[72:75],a[180:183],v[228:231],v[72:75]
v_add_f32 v39, v97, v39
v_add_f32 v40, v97, v40
M v[72:75],a[184:187],v[232:235],v[72:75]
s_waitcnt lgkmcnt(0)
E v36,v36
M v[72:75],a[188:191],v[236:239],v[72:75]
s_sub_u32 s19, s3, s18
E v37,v37
M a[32:35],a[192:195],v[76:79],0
s_cmp_gt_i32 s19, 14
s_cbranch_scc1 L_pc0X0
L_pe0X0:
v_add_f32 v41, v97, v41
v_add_f32 v42, v97, v42
v_mov_b32 v99, v97
C v44,v36,v37
M a[36:39],a[196:199],v[76:79],0
s_add_u32 s3, s18, 110
E v38,v38
M a[40:43],a[200:203],v[76:79],0
s_waitcnt vmcnt(8) lgkmcnt(0)
E v39,v39
M a[44:47],a[204:207],v[76:79],0
s_barrier
v_add_f32 v43, v97, v43
E v40,v40
M a[232:235],a[224:227],v[76:79],0
M a[48:51],a[208:211],v[76:79],0
E v41,v41
M a[52:55],a[212:215],v[76:79],0
E v42,v42
M a[56:59],a[216:219],v[76:79],0
E v43,v43
M a[60:63],a[220:223],v[76:79],0
v_mov_b32 v98, v97
C v45,v38,v39
C v46,v40,v41
C v47,v42,v43
v_add_u32 v2, s66, v2
v_add_u32 v3, s66, v3
M v[100:103],a[160:163],v[240:243],0
v_max3_f32 v128, v68, v69, v70
v_max3_f32 v129, v72, v73, v74
v_max3_f32 v128, v128, v71, v75
M v[100:103],a[164:167],v[244:247],v[100:103]
v_max_f32 v128, v128, v129
v_mov_b32 v129, v128
M v[100:103],a[168:171],v[248:251],v[100:103]
s_nop 0
v_permlane32_swap_b32 v128, v129
M v[100:103],a[172:175],v[252:255],v[100:103]
v_max_f32 v128, v128, v129
v_mov_b32 v129, v128
M v[104:107],a[176:179],v[240:243],0
D a[160:163],v2,0
v_permlane16_swap_b32 v128, v129
M v[104:107],a[180:183],v[244:247],v[104:107]
D a[176:179],v2,4096
v_max_f32 v128, v128, v129
v_sub_f32 v129, 0, v128
v_add_f32 v68, v129, v68
v_add_f32 v69, v129, v69
v_add_f32 v70, v129, v70
E v68,v68
M v[104:107],a[184:187],v[248:251],v[104:107]
D a[172:175],v3,1024
v_add_f32 v72, v129, v72
v_add_f32 v73, v129, v73
E v69,v69
M v[104:107],a[188:191],v[252:255],v[104:107]
D a[184:187],v2,5120
v_add_f32 v71, v129, v71
v_add_f32 v74, v129, v74
v_mov_b32 v130, v129
v_add_u32 v5, s66, v5
M a[64:67],a[192:195],v[44:47],0
E v73,v73
E v74,v74
M a[68:71],a[196:199],v[44:47],0
D a[164:167],v3,0
E v70,v70
E v71,v71
E v72,v72
v_max3_f32 v160, v100, v101, v102
M a[72:75],a[200:203],v[44:47],0
D a[168:171],v2,1024
C v76,v68,v69
M a[76:79],a[204:207],v[44:47],0
v_add_f32 v75, v129, v75
E v75,v75
v_max3_f32 v161, v104, v105, v106
v_max3_f32 v160, v160, v103, v107
v_max_f32 v160, v160, v161
v_mov_b32 v161, v160
M a[236:239],a[224:227],v[44:47],0
M a[80:83],a[208:211],v[44:47],0
v_mov_b32 v131, v129
v_permlane32_swap_b32 v160, v161
M a[84:87],a[212:215],v[44:47],0
D a[180:183],v3,4096
C v77,v70,v71
v_max_f32 v160, v160, v161
v_mov_b32 v161, v160
M a[88:91],a[216:219],v[44:47],0
s_nop 0
v_permlane16_swap_b32 v160, v161
M a[92:95],a[220:223],v[44:47],0
s_waitcnt lgkmcnt(6)
D a[188:191],v3,5120
v_mov_b32 v128, v129
v_max_f32 v160, v160, v161
v_sub_f32 v161, 0, v160
M v[36:39],a[160:163],v[176:179],v[32:35]
s_waitcnt lgkmcnt(3)
v_add_f32 v100, v161, v100
v_add_f32 v105, v161, v105
M v[36:39],a[164:167],v[180:183],v[36:39]
s_waitcnt lgkmcnt(2)
C v79,v74,v75
v_add_f32 v102, v161, v102
v_add_f32 v104, v161, v104
E v100,v100
M v[36:39],a[168:171],v[184:187],v[36:39]
v_add_f32 v101, v161, v101
E v101,v101
M v[36:39],a[172:175],v[188:191],v[36:39]
s_add_u32 s64, s65, -98304
C v78,v72,v73
M v[40:43],a[176:179],v[176:179],v[32:35]
s_waitcnt lgkmcnt(1)
s_cmp_lt_i32 s64, 0
s_cselect_b32 s67, 163840, 0
E v102,v102
M v[40:43],a[180:183],v[180:183],v[40:43]
s_add_u32 s64, s64, s67
v_add_f32 v107, v161, v107
M v[40:43],a[184:187],v[184:187],v[40:43]
s_waitcnt lgkmcnt(0)
s_add_u32 s70, s73, s64
v_add_f32 v103, v161, v103
M v[40:43],a[188:191],v[188:191],v[40:43]
s_mov_b32 m0, s70
E v104,v104
M a[96:99],a[192:195],v[76:79],0
buffer_load_dwordx4 v8, s[44:47], s6 offen nt lds
v_mov_b32 v160, v161
M a[100:103],a[196:199],v[76:79],0
v_add_f32 v106, v161, v106
v_mov_b32 v163, v161
M a[104:107],a[200:203],v[76:79],0
E v103,v103
M a[108:111],a[204:207],v[76:79],0
E v105,v105
M a[240:243],a[224:227],v[76:79],0
M a[112:115],a[208:211],v[76:79],0
s_add_u32 s77, s77, 8192
buffer_load_dwordx4 v8, s[44:47], s6 offen offset:2048 nt lds
E v106,v106
M a[116:119],a[212:215],v[76:79],0
E v107,v107
M a[120:123],a[216:219],v[76:79],0
v_mov_b32 v162, v161
C v108,v100,v101
M a[124:127],a[220:223],v[76:79],0
s_add_u32 m0, s70, 896
C v109,v102,v103
C v111,v106,v107
M v[68:71],a[160:163],v[192:195],v[64:67]
buffer_load_dwordx4 v8, s[44:47], s6 offen offset:128 nt lds
C v110,v104,v105
M v[68:71],a[164:167],v[196:199],v[68:71]
E v42,v42
M v[68:71],a[168:171],v[200:203],v[68:71]
E v40,v40
M v[68:71],a[172:175],v[204:207],v[68:71]
E v38,v38
M v[72:75],a[176:179],v[192:195],v[64:67]
s_add_u32 s71, s6, 4096
buffer_load_dwordx4 v8, s[44:47], s6 offen offset:2176 nt lds
v_add_u32 v4, s66, v4
M v[72:75],a[180:183],v[196:199],v[72:75]
E v39,v39
M v[72:75],a[184:187],v[200:203],v[72:75]
E v43,v43
M v[72:75],a[188:191],v[204:207],v[72:75]
s_add_u32 m0, s70, 4096
E v41,v41
M a[128:131],a[192:195],v[108:111],0
buffer_load_dwordx4 v8, s[44:47], s71 offen nt lds
T a[192:193],v4,0
M a[132:135],a[196:199],v[108:111],0
T a[198:199],v5,4096
C v45,v38,v39
M a[136:139],a[200:203],v[108:111],0
E v36,v36
M a[140:143],a[204:207],v[108:111],0
T a[196:197],v5,0
C v46,v40,v41
M a[244:247],a[224:227],v[108:111],0
M a[144:147],a[208:211],v[108:111],0
s_and_b32 s77, s77, 131071
buffer_load_dwordx4 v8, s[44:47], s71 offen offset:2048 nt lds
T a[208:209],v4,1024
M a[148:151],a[212:215],v[108:111],0
T a[194:195],v4,4096
E v37,v37
M a[152:155],a[216:219],v[108:111],0
T a[218:219],v4,5632
C v47,v42,v43
M a[156:159],a[220:223],v[108:111],0
s_add_u32 m0, s70, 4992
T a[204:205],v5,512
C v44,v36,v37
M v[36:39],a[160:163],v[208:211],v[96:99]
buffer_load_dwordx4 v8, s[44:47], s71 offen offset:128 nt lds
T a[220:221],v5,1536
M v[36:39],a[164:167],v[212:215],v[36:39]
T a[200:201],v4,512
T a[202:203],v4,4608
M v[36:39],a[168:171],v[216:219],v[36:39]
T a[210:211],v4,5120
M v[36:39],a[172:175],v[220:223],v[36:39]
T a[206:207],v5,4608
T a[214:215],v5,5120
M v[40:43],a[176:179],v[208:211],v[96:99]
s_cmp_eq_u32 s77, s4
s_cselect_b32 s5, 2097152, s5
buffer_load_dwordx4 v8, s[44:47], s71 offen offset:2176 nt lds
T a[222:223],v5,5632
M v[40:43],a[180:183],v[212:215],v[40:43]
s_add_u32 s6, s77, s5
T a[212:213],v5,1024
T a[216:217],v4,1536
M v[40:43],a[184:187],v[216:219],v[40:43]
E v69,v69
M v[40:43],a[188:191],v[220:223],v[40:43]
s_waitcnt lgkmcnt(11)
E v68,v68
M a[0:3],a[192:195],v[44:47],a[0:3]
)"
R"(E v70,v70
M a[4:7],a[196:199],v[44:47],a[4:7]
s_waitcnt lgkmcnt(6)
E v71,v71
M a[8:11],a[200:203],v[44:47],a[8:11]
s_waitcnt lgkmcnt(4)
E v74,v74
M a[12:15],a[204:207],v[44:47],a[12:15]
E v72,v72
M a[228:231],a[224:227],v[44:47],a[228:231]
M a[16:19],a[208:211],v[44:47],a[16:19]
s_waitcnt lgkmcnt(1)
E v75,v75
M a[20:23],a[212:215],v[44:47],a[20:23]
s_waitcnt lgkmcnt(0)
E v38,v38
M a[24:27],a[216:219],v[44:47],a[24:27]
E v73,v73
M a[28:31],a[220:223],v[44:47],a[28:31]
s_memrealtime s[18:19]
C v76,v68,v69
C v77,v70,v71
M v[68:71],a[160:163],v[224:227],v[128:131]
C v79,v74,v75
M v[68:71],a[164:167],v[228:231],v[68:71]
E v37,v37
M v[68:71],a[168:171],v[232:235],v[68:71]
s_mov_b32 s64, s65
M v[68:71],a[172:175],v[236:239],v[68:71]
s_cmp_eq_u32 s65, 131072
s_cselect_b32 s66, s90, s76
C v78,v72,v73
M v[72:75],a[176:179],v[224:227],v[128:131]
s_add_u32 s65, s65, s66
E v36,v36
M v[72:75],a[180:183],v[228:231],v[72:75]
E v41,v41
M v[72:75],a[184:187],v[232:235],v[72:75]
s_waitcnt lgkmcnt(0)
E v39,v39
M v[72:75],a[188:191],v[236:239],v[72:75]
s_sub_u32 s19, s3, s18
E v43,v43
M a[32:35],a[192:195],v[76:79],a[32:35]
s_cmp_gt_i32 s19, 14
s_cbranch_scc1 L_pc0X1
L_pe0X1:
v_add_u32 v2, s66, v2
M a[36:39],a[196:199],v[76:79],a[36:39]
s_add_u32 s3, s18, 110
E v42,v42
M a[40:43],a[200:203],v[76:79],a[40:43]
s_waitcnt vmcnt(8) lgkmcnt(0)
E v40,v40
M a[44:47],a[204:207],v[76:79],a[44:47]
s_barrier
M a[232:235],a[224:227],v[76:79],a[232:235]
M a[48:51],a[208:211],v[76:79],a[48:51]
C v47,v42,v43
M a[52:55],a[212:215],v[76:79],a[52:55]
C v44,v36,v37
C v46,v40,v41
M a[56:59],a[216:219],v[76:79],a[56:59]
v_add_u32 v3, s66, v3
M a[60:63],a[220:223],v[76:79],a[60:63]
C v45,v38,v39
s_mov_b32 s69, 14
.p2align 3
L_atX_2:
M v[100:103],a[160:163],v[240:243],v[160:163]
E v72,v72
M v[100:103],a[164:167],v[244:247],v[100:103]
E v71,v71
M v[100:103],a[168:171],v[248:251],v[100:103]
D a[168:171],v2,1024
E v69,v69
M v[100:103],a[172:175],v[252:255],v[100:103]
E v68,v68
M v[104:107],a[176:179],v[240:243],v[160:163]
D a[164:167],v3,0
M v[104:107],a[180:183],v[244:247],v[104:107]
E v70,v70
M v[104:107],a[184:187],v[248:251],v[104:107]
D a[172:175],v3,1024
M v[104:107],a[188:191],v[252:255],v[104:107]
D a[160:163],v2,0
C v76,v68,v69
M a[64:67],a[192:195],v[44:47],a[64:67]
E v73,v73
M a[68:71],a[196:199],v[44:47],a[68:71]
D a[176:179],v2,4096
E v75,v75
M a[72:75],a[200:203],v[44:47],a[72:75]
D a[184:187],v2,5120
C v77,v70,v71
M a[76:79],a[204:207],v[44:47],a[76:79]
E v106,v106
M a[236:239],a[224:227],v[44:47],a[236:239]
M a[80:83],a[208:211],v[44:47],a[80:83]
E v74,v74
M a[84:87],a[212:215],v[44:47],a[84:87]
D a[180:183],v3,4096
C v79,v74,v75
M a[88:91],a[216:219],v[44:47],a[88:91]
D a[188:191],v3,5120
M a[92:95],a[220:223],v[44:47],a[92:95]
s_waitcnt lgkmcnt(4)
E v101,v101
M v[36:39],a[160:163],v[176:179],v[32:35]
C v78,v72,v73
M v[36:39],a[164:167],v[180:183],v[36:39]
E v105,v105
M v[36:39],a[168:171],v[184:187],v[36:39]
E v104,v104
M v[36:39],a[172:175],v[188:191],v[36:39]
s_waitcnt lgkmcnt(3)
s_add_u32 s64, s65, -98304
M v[40:43],a[176:179],v[176:179],v[32:35]
s_waitcnt lgkmcnt(1)
s_cmp_lt_i32 s64, 0
s_cselect_b32 s67, 163840, 0
M v[40:43],a[180:183],v[180:183],v[40:43]
s_add_u32 s64, s64, s67
v_add_u32 v5, s66, v5
M v[40:43],a[184:187],v[184:187],v[40:43]
s_waitcnt lgkmcnt(0)
s_add_u32 s70, s73, s64
v_add_u32 v4, s66, v4
M v[40:43],a[188:191],v[188:191],v[40:43]
s_mov_b32 m0, s70
E v102,v102
M a[96:99],a[192:195],v[76:79],a[96:99]
buffer_load_dwordx4 v8, s[44:47], s6 offen nt lds
M a[100:103],a[196:199],v[76:79],a[100:103]
E v107,v107
M a[104:107],a[200:203],v[76:79],a[104:107]
E v100,v100
M a[108:111],a[204:207],v[76:79],a[108:111]
E v103,v103
M a[240:243],a[224:227],v[76:79],a[240:243]
M a[112:115],a[208:211],v[76:79],a[112:115]
s_add_u32 s77, s77, 8192
buffer_load_dwordx4 v8, s[44:47], s6 offen offset:2048 nt lds
C v110,v104,v105
M a[116:119],a[212:215],v[76:79],a[116:119]
E v42,v42
M a[120:123],a[216:219],v[76:79],a[120:123]
C v108,v100,v101
C v109,v102,v103
M a[124:127],a[220:223],v[76:79],a[124:127]
s_add_u32 m0, s70, 896
E v39,v39
M v[68:71],a[160:163],v[192:195],v[64:67]
buffer_load_dwordx4 v8, s[44:47], s6 offen offset:128 nt lds
C v111,v106,v107
M v[68:71],a[164:167],v[196:199],v[68:71]
E v43,v43
M v[68:71],a[168:171],v[200:203],v[68:71]
E v40,v40
M v[68:71],a[172:175],v[204:207],v[68:71]
E v37,v37
M v[72:75],a[176:179],v[192:195],v[64:67]
s_add_u32 s71, s6, 4096
buffer_load_dwordx4 v8, s[44:47], s6 offen offset:2176 nt lds
E v41,v41
M v[72:75],a[180:183],v[196:199],v[72:75]
E v36,v36
M v[72:75],a[184:187],v[200:203],v[72:75]
E v38,v38
M v[72:75],a[188:191],v[204:207],v[72:75]
s_add_u32 m0, s70, 4096
E v70,v70
M a[128:131],a[192:195],v[108:111],a[128:131]
buffer_load_dwordx4 v8, s[44:47], s71 offen nt lds
T a[192:193],v4,0
M a[132:135],a[196:199],v[108:111],a[132:135]
T a[194:195],v4,4096
M a[136:139],a[200:203],v[108:111],a[136:139]
T a[200:201],v4,512
M a[140:143],a[204:207],v[108:111],a[140:143]
T a[204:205],v5,512
M a[244:247],a[224:227],v[108:111],a[244:247]
M a[144:147],a[208:211],v[108:111],a[144:147]
s_and_b32 s77, s77, 131071
buffer_load_dwordx4 v8, s[44:47], s71 offen offset:2048 nt lds
T a[206:207],v5,4608
M a[148:151],a[212:215],v[108:111],a[148:151]
T a[196:197],v5,0
C v44,v36,v37
M a[152:155],a[216:219],v[108:111],a[152:155]
T a[212:213],v5,1024
C v45,v38,v39
M a[156:159],a[220:223],v[108:111],a[156:159]
s_add_u32 m0, s70, 4992
T a[208:209],v4,1024
C v46,v40,v41
M v[36:39],a[160:163],v[208:211],v[96:99]
buffer_load_dwordx4 v8, s[44:47], s71 offen offset:128 nt lds
T a[210:211],v4,5120
M v[36:39],a[164:167],v[212:215],v[36:39]
T a[198:199],v5,4096
M v[36:39],a[168:171],v[216:219],v[36:39]
T a[202:203],v4,4608
M v[36:39],a[172:175],v[220:223],v[36:39]
T a[216:217],v4,1536
C v47,v42,v43
M v[40:43],a[176:179],v[208:211],v[96:99]
s_cmp_eq_u32 s77, s4
s_cselect_b32 s5, 2097152, s5
buffer_load_dwordx4 v8, s[44:47], s71 offen offset:2176 nt lds
T a[218:219],v4,5632
M v[40:43],a[180:183],v[212:215],v[40:43]
s_add_u32 s6, s77, s5
T a[214:215],v5,5120
M v[40:43],a[184:187],v[216:219],v[40:43]
T a[222:223],v5,5632
M v[40:43],a[188:191],v[220:223],v[40:43]
s_waitcnt lgkmcnt(13)
T a[220:221],v5,1536
M a[0:3],a[192:195],v[44:47],a[0:3]
s_waitcnt lgkmcnt(6)
E v68,v68
M a[4:7],a[196:199],v[44:47],a[4:7]
s_waitcnt lgkmcnt(5)
E v71,v71
M a[8:11],a[200:203],v[44:47],a[8:11]
E v73,v73
M a[12:15],a[204:207],v[44:47],a[12:15]
E v69,v69
M a[228:231],a[224:227],v[44:47],a[228:231]
M a[16:19],a[208:211],v[44:47],a[16:19]
s_waitcnt lgkmcnt(2)
E v72,v72
M a[20:23],a[212:215],v[44:47],a[20:23]
E v74,v74
M a[24:27],a[216:219],v[44:47],a[24:27]
s_waitcnt lgkmcnt(0)
E v75,v75
M a[28:31],a[220:223],v[44:47],a[28:31]
s_memrealtime s[18:19]
C v76,v68,v69
C v77,v70,v71
M v[68:71],a[160:163],v[224:227],v[128:131]
E v41,v41
M v[68:71],a[164:167],v[228:231],v[68:71]
E v38,v38
M v[68:71],a[168:171],v[232:235],v[68:71]
s_mov_b32 s64, s65
C v79,v74,v75
M v[68:71],a[172:175],v[236:239],v[68:71]
s_cmp_eq_u32 s65, 131072
s_cselect_b32 s66, s90, s76
C v78,v72,v73
M v[72:75],a[176:179],v[224:227],v[128:131]
s_add_u32 s65, s65, s66
E v40,v40
M v[72:75],a[180:183],v[228:231],v[72:75]
E v36,v36
M v[72:75],a[184:187],v[232:235],v[72:75]
s_waitcnt lgkmcnt(0)
E v43,v43
M v[72:75],a[188:191],v[236:239],v[72:75]
s_sub_u32 s19, s3, s18
v_add_u32 v2, s66, v2
M a[32:35],a[192:195],v[76:79],a[32:35]
s_cmp_gt_i32 s19, 14
s_cbranch_scc1 L_pc0XL
L_pe0XL:
E v42,v42
M a[36:39],a[196:199],v[76:79],a[36:39]
s_add_u32 s3, s18, 110
v_add_u32 v3, s66, v3
M a[40:43],a[200:203],v[76:79],a[40:43]
s_waitcnt vmcnt(8) lgkmcnt(0)
E v37,v37
M a[44:47],a[204:207],v[76:79],a[44:47]
s_barrier
M a[232:235],a[224:227],v[76:79],a[232:235]
M a[48:51],a[208:211],v[76:79],a[48:51]
C v47,v42,v43
M a[52:55],a[212:215],v[76:79],a[52:55]
s_sub_u32 s69, s69, 1
E v39,v39
M a[56:59],a[216:219],v[76:79],a[56:59]
s_cmp_lg_u32 s69, 0
C v44,v36,v37
C v45,v38,v39
M a[60:63],a[220:223],v[76:79],a[60:63]
C v46,v40,v41
s_cbranch_scc1 L_atX_2
M v[100:103],a[160:163],v[240:243],v[160:163]
E v71,v71
M v[100:103],a[164:167],v[244:247],v[100:103]
E v69,v69
M v[100:103],a[168:171],v[248:251],v[100:103]
s_add_u32 s64, s65, -98304
D a[168:171],v2,1024
E v74,v74
M v[100:103],a[172:175],v[252:255],v[100:103]
s_cmp_lt_i32 s64, 0
s_cselect_b32 s67, 163840, 0
E v70,v70
M v[104:107],a[176:179],v[240:243],v[160:163]
s_add_u32 s64, s64, s67
D a[176:179],v2,4096
C v77,v70,v71
M v[104:107],a[180:183],v[244:247],v[104:107]
s_add_u32 s70, s73, s64
D a[160:163],v2,0
v_add_u32 v5, s66, v5
M v[104:107],a[184:187],v[248:251],v[104:107]
s_mov_b32 m0, s70
E v73,v73
M v[104:107],a[188:191],v[252:255],v[104:107]
buffer_load_dwordx4 v8, s[44:47], s6 offen nt lds
D a[164:167],v3,0
M a[64:67],a[192:195],v[44:47],a[64:67]
D a[172:175],v3,1024
v_add_u32 v4, s66, v4
M a[68:71],a[196:199],v[44:47],a[68:71]
D a[184:187],v2,5120
E v101,v101
M a[72:75],a[200:203],v[44:47],a[72:75]
E v102,v102
M a[76:79],a[204:207],v[44:47],a[76:79]
s_add_u32 s77, s77, 8192
buffer_load_dwordx4 v8, s[44:47], s6 offen offset:2048 nt lds
E v72,v72
M a[236:239],a[224:227],v[44:47],a[236:239]
M a[80:83],a[208:211],v[44:47],a[80:83]
D a[180:183],v3,4096
E v68,v68
M a[84:87],a[212:215],v[44:47],a[84:87]
E v75,v75
M a[88:91],a[216:219],v[44:47],a[88:91]
s_add_u32 m0, s70, 896
C v76,v68,v69
C v79,v74,v75
M a[92:95],a[220:223],v[44:47],a[92:95]
buffer_load_dwordx4 v8, s[44:47], s6 offen offset:128 nt lds
D a[188:191],v3,5120
C v78,v72,v73
s_add_u32 s71, s6, 4096
buffer_load_dwordx4 v8, s[44:47], s6 offen offset:2176 nt lds
s_add_u32 m0, s70, 4096
E v107,v107
buffer_load_dwordx4 v8, s[44:47], s71 offen nt lds
M a[96:99],a[192:195],v[76:79],a[96:99]
E v106,v106
M a[100:103],a[196:199],v[76:79],a[100:103]
E v104,v104
M a[104:107],a[200:203],v[76:79],a[104:107]
E v103,v103
M a[108:111],a[204:207],v[76:79],a[108:111]
s_and_b32 s77, s77, 131071
buffer_load_dwordx4 v8, s[44:47], s71 offen offset:2048 nt lds
C v111,v106,v107
M a[240:243],a[224:227],v[76:79],a[240:243]
M a[112:115],a[208:211],v[76:79],a[112:115]
E v100,v100
M a[116:119],a[212:215],v[76:79],a[116:119]
C v108,v100,v101
C v109,v102,v103
M a[120:123],a[216:219],v[76:79],a[120:123]
s_add_u32 m0, s70, 4992
E v105,v105
M a[124:127],a[220:223],v[76:79],a[124:127]
buffer_load_dwordx4 v8, s[44:47], s71 offen offset:128 nt lds
C v110,v104,v105
s_cmp_eq_u32 s77, s4
s_cselect_b32 s5, 2097152, s5
buffer_load_dwordx4 v8, s[44:47], s71 offen offset:2176 nt lds
s_add_u32 s6, s77, s5
M a[128:131],a[192:195],v[108:111],a[128:131]
M a[132:135],a[196:199],v[108:111],a[132:135]
M a[136:139],a[200:203],v[108:111],a[136:139]
M a[140:143],a[204:207],v[108:111],a[140:143]
M a[244:247],a[224:227],v[108:111],a[244:247]
M a[144:147],a[208:211],v[108:111],a[144:147]
M a[148:151],a[212:215],v[108:111],a[148:151]
M a[152:155],a[216:219],v[108:111],a[152:155]
M a[156:159],a[220:223],v[108:111],a[156:159]
L_ph_oe0X:
s_mov_b64 s[98:99], 0
s_mov_b32 s31, 0x5d800000
R v132,a228
R v133,a232
R v134,a236
R v135,a240
R v136,a244
v_max3_f32 v137, v132, v133, v134
v_max3_f32 v137, v137, v135, v136
v_cmp_ngt_f32 vcc, s31, v137
v_cmp_u_f32 s[98:99], v132, v133
s_nop 1
s_or_b64 vcc, vcc, s[98:99]
v_cmp_u_f32 s[98:99], v134, v135
s_nop 1
s_or_b64 vcc, vcc, s[98:99]
v_cmp_u_f32 s[98:99], v136, v136
s_nop 1
s_or_b64 vcc, vcc, s[98:99]
s_cbranch_scc0 L_ret0X
s_mov_b32 s97, 0
s_add_u32 s31, s8, L_slow1-L_code0-4
s_addc_u32 s23, s9, 0
s_mov_b32 s18, s31
s_mov_b32 s19, s23
s_swappc_b64 s[98:99], s[18:19]
L_ret0X:
v_rcp_f32 v32, v132
v_rcp_f32 v64, v133
v_rcp_f32 v96, v134
v_rcp_f32 v128, v135
v_rcp_f32 v160, v136
R v164,a0
R v165,a1
R v166,a2
R v167,a3
v_pk_mul_f32 v[164:165], v[164:165], v[32:33] op_sel_hi:[1,0]
v_pk_mul_f32 v[166:167], v[166:167], v[32:33] op_sel_hi:[1,0]
C v176,v164,v165
C v177,v166,v167
R v168,a4
R v169,a5
R v170,a6
R v171,a7
v_pk_mul_f32 v[168:169], v[168:169], v[32:33] op_sel_hi:[1,0]
v_pk_mul_f32 v[170:171], v[170:171], v[32:33] op_sel_hi:[1,0]
C v178,v168,v169
C v179,v170,v171
R v172,a8
R v173,a9
R v174,a10
R v175,a11
v_pk_mul_f32 v[172:173], v[172:173], v[32:33] op_sel_hi:[1,0]
v_pk_mul_f32 v[174:175], v[174:175], v[32:33] op_sel_hi:[1,0]
C v180,v172,v173
C v181,v174,v175
R v164,a12
R v165,a13
R v166,a14
R v167,a15
v_pk_mul_f32 v[164:165], v[164:165], v[32:33] op_sel_hi:[1,0]
v_pk_mul_f32 v[166:167], v[166:167], v[32:33] op_sel_hi:[1,0]
C v182,v164,v165
C v183,v166,v167
R v168,a16
R v169,a17
R v170,a18
R v171,a19
v_pk_mul_f32 v[168:169], v[168:169], v[32:33] op_sel_hi:[1,0]
v_pk_mul_f32 v[170:171], v[170:171], v[32:33] op_sel_hi:[1,0]
C v184,v168,v169
C v185,v170,v171
R v172,a20
R v173,a21
R v174,a22
R v175,a23
v_pk_mul_f32 v[172:173], v[172:173], v[32:33] op_sel_hi:[1,0]
v_pk_mul_f32 v[174:175], v[174:175], v[32:33] op_sel_hi:[1,0]
C v186,v172,v173
C v187,v174,v175
R v164,a24
R v165,a25
R v166,a26
R v167,a27
v_pk_mul_f32 v[164:165], v[164:165], v[32:33] op_sel_hi:[1,0]
v_pk_mul_f32 v[166:167], v[166:167], v[32:33] op_sel_hi:[1,0]
C v188,v164,v165
C v189,v166,v167
R v168,a28
R v169,a29
R v170,a30
R v171,a31
v_pk_mul_f32 v[168:169], v[168:169], v[32:33] op_sel_hi:[1,0]
v_pk_mul_f32 v[170:171], v[170:171], v[32:33] op_sel_hi:[1,0]
C v190,v168,v169
C v191,v170,v171
R v172,a32
R v173,a33
R v174,a34
R v175,a35
v_pk_mul_f32 v[172:173], v[172:173], v[64:65] op_sel_hi:[1,0]
v_pk_mul_f32 v[174:175], v[174:175], v[64:65] op_sel_hi:[1,0]
C v192,v172,v173
C v193,v174,v175
R v164,a36
R v165,a37
R v166,a38
R v167,a39
v_pk_mul_f32 v[164:165], v[164:165], v[64:65] op_sel_hi:[1,0]
v_pk_mul_f32 v[166:167], v[166:167], v[64:65] op_sel_hi:[1,0]
C v194,v164,v165
C v195,v166,v167
R v168,a40
R v169,a41
R v170,a42
R v171,a43
v_pk_mul_f32 v[168:169], v[168:169], v[64:65] op_sel_hi:[1,0]
)"
R"(v_pk_mul_f32 v[170:171], v[170:171], v[64:65] op_sel_hi:[1,0]
C v196,v168,v169
C v197,v170,v171
R v172,a44
R v173,a45
R v174,a46
R v175,a47
v_pk_mul_f32 v[172:173], v[172:173], v[64:65] op_sel_hi:[1,0]
v_pk_mul_f32 v[174:175], v[174:175], v[64:65] op_sel_hi:[1,0]
C v198,v172,v173
C v199,v174,v175
R v164,a48
R v165,a49
R v166,a50
R v167,a51
v_pk_mul_f32 v[164:165], v[164:165], v[64:65] op_sel_hi:[1,0]
v_pk_mul_f32 v[166:167], v[166:167], v[64:65] op_sel_hi:[1,0]
C v200,v164,v165
C v201,v166,v167
R v168,a52
R v169,a53
R v170,a54
R v171,a55
v_pk_mul_f32 v[168:169], v[168:169], v[64:65] op_sel_hi:[1,0]
v_pk_mul_f32 v[170:171], v[170:171], v[64:65] op_sel_hi:[1,0]
C v202,v168,v169
C v203,v170,v171
R v172,a56
R v173,a57
R v174,a58
R v175,a59
v_pk_mul_f32 v[172:173], v[172:173], v[64:65] op_sel_hi:[1,0]
v_pk_mul_f32 v[174:175], v[174:175], v[64:65] op_sel_hi:[1,0]
C v204,v172,v173
C v205,v174,v175
R v164,a60
R v165,a61
R v166,a62
R v167,a63
v_pk_mul_f32 v[164:165], v[164:165], v[64:65] op_sel_hi:[1,0]
v_pk_mul_f32 v[166:167], v[166:167], v[64:65] op_sel_hi:[1,0]
C v206,v164,v165
C v207,v166,v167
R v168,a64
R v169,a65
R v170,a66
R v171,a67
v_pk_mul_f32 v[168:169], v[168:169], v[96:97] op_sel_hi:[1,0]
v_pk_mul_f32 v[170:171], v[170:171], v[96:97] op_sel_hi:[1,0]
C v208,v168,v169
C v209,v170,v171
R v172,a68
R v173,a69
R v174,a70
R v175,a71
v_pk_mul_f32 v[172:173], v[172:173], v[96:97] op_sel_hi:[1,0]
v_pk_mul_f32 v[174:175], v[174:175], v[96:97] op_sel_hi:[1,0]
C v210,v172,v173
C v211,v174,v175
R v164,a72
R v165,a73
R v166,a74
R v167,a75
v_pk_mul_f32 v[164:165], v[164:165], v[96:97] op_sel_hi:[1,0]
v_pk_mul_f32 v[166:167], v[166:167], v[96:97] op_sel_hi:[1,0]
C v212,v164,v165
C v213,v166,v167
R v168,a76
R v169,a77
R v170,a78
R v171,a79
v_pk_mul_f32 v[168:169], v[168:169], v[96:97] op_sel_hi:[1,0]
v_pk_mul_f32 v[170:171], v[170:171], v[96:97] op_sel_hi:[1,0]
C v214,v168,v169
C v215,v170,v171
R v172,a80
R v173,a81
R v174,a82
R v175,a83
v_pk_mul_f32 v[172:173], v[172:173], v[96:97] op_sel_hi:[1,0]
v_pk_mul_f32 v[174:175], v[174:175], v[96:97] op_sel_hi:[1,0]
C v216,v172,v173
C v217,v174,v175
R v164,a84
R v165,a85
R v166,a86
R v167,a87
v_pk_mul_f32 v[164:165], v[164:165], v[96:97] op_sel_hi:[1,0]
v_pk_mul_f32 v[166:167], v[166:167], v[96:97] op_sel_hi:[1,0]
C v218,v164,v165
C v219,v166,v167
R v168,a88
R v169,a89
R v170,a90
R v171,a91
v_pk_mul_f32 v[168:169], v[168:169], v[96:97] op_sel_hi:[1,0]
v_pk_mul_f32 v[170:171], v[170:171], v[96:97] op_sel_hi:[1,0]
C v220,v168,v169
C v221,v170,v171
R v172,a92
R v173,a93
R v174,a94
R v175,a95
v_pk_mul_f32 v[172:173], v[172:173], v[96:97] op_sel_hi:[1,0]
v_pk_mul_f32 v[174:175], v[174:175], v[96:97] op_sel_hi:[1,0]
C v222,v172,v173
C v223,v174,v175
R v164,a96
R v165,a97
R v166,a98
R v167,a99
v_pk_mul_f32 v[164:165], v[164:165], v[128:129] op_sel_hi:[1,0]
v_pk_mul_f32 v[166:167], v[166:167], v[128:129] op_sel_hi:[1,0]
C v224,v164,v165
C v225,v166,v167
R v168,a100
R v169,a101
R v170,a102
R v171,a103
v_pk_mul_f32 v[168:169], v[168:169], v[128:129] op_sel_hi:[1,0]
v_pk_mul_f32 v[170:171], v[170:171], v[128:129] op_sel_hi:[1,0]
C v226,v168,v169
C v227,v170,v171
R v172,a104
R v173,a105
R v174,a106
R v175,a107
v_pk_mul_f32 v[172:173], v[172:173], v[128:129] op_sel_hi:[1,0]
v_pk_mul_f32 v[174:175], v[174:175], v[128:129] op_sel_hi:[1,0]
C v228,v172,v173
C v229,v174,v175
R v164,a108
R v165,a109
R v166,a110
R v167,a111
v_pk_mul_f32 v[164:165], v[164:165], v[128:129] op_sel_hi:[1,0]
v_pk_mul_f32 v[166:167], v[166:167], v[128:129] op_sel_hi:[1,0]
C v230,v164,v165
C v231,v166,v167
R v168,a112
R v169,a113
R v170,a114
R v171,a115
v_pk_mul_f32 v[168:169], v[168:169], v[128:129] op_sel_hi:[1,0]
v_pk_mul_f32 v[170:171], v[170:171], v[128:129] op_sel_hi:[1,0]
C v232,v168,v169
C v233,v170,v171
R v172,a116
R v173,a117
R v174,a118
R v175,a119
v_pk_mul_f32 v[172:173], v[172:173], v[128:129] op_sel_hi:[1,0]
v_pk_mul_f32 v[174:175], v[174:175], v[128:129] op_sel_hi:[1,0]
C v234,v172,v173
C v235,v174,v175
R v164,a120
R v165,a121
R v166,a122
R v167,a123
v_pk_mul_f32 v[164:165], v[164:165], v[128:129] op_sel_hi:[1,0]
v_pk_mul_f32 v[166:167], v[166:167], v[128:129] op_sel_hi:[1,0]
C v236,v164,v165
C v237,v166,v167
R v168,a124
R v169,a125
R v170,a126
R v171,a127
v_pk_mul_f32 v[168:169], v[168:169], v[128:129] op_sel_hi:[1,0]
v_pk_mul_f32 v[170:171], v[170:171], v[128:129] op_sel_hi:[1,0]
C v238,v168,v169
C v239,v170,v171
R v172,a128
R v173,a129
R v174,a130
R v175,a131
v_pk_mul_f32 v[172:173], v[172:173], v[160:161] op_sel_hi:[1,0]
v_pk_mul_f32 v[174:175], v[174:175], v[160:161] op_sel_hi:[1,0]
C v240,v172,v173
C v241,v174,v175
R v164,a132
R v165,a133
R v166,a134
R v167,a135
v_pk_mul_f32 v[164:165], v[164:165], v[160:161] op_sel_hi:[1,0]
v_pk_mul_f32 v[166:167], v[166:167], v[160:161] op_sel_hi:[1,0]
C v242,v164,v165
C v243,v166,v167
R v168,a136
R v169,a137
R v170,a138
R v171,a139
v_pk_mul_f32 v[168:169], v[168:169], v[160:161] op_sel_hi:[1,0]
v_pk_mul_f32 v[170:171], v[170:171], v[160:161] op_sel_hi:[1,0]
C v244,v168,v169
C v245,v170,v171
R v172,a140
R v173,a141
R v174,a142
R v175,a143
v_pk_mul_f32 v[172:173], v[172:173], v[160:161] op_sel_hi:[1,0]
v_pk_mul_f32 v[174:175], v[174:175], v[160:161] op_sel_hi:[1,0]
C v246,v172,v173
C v247,v174,v175
R v164,a144
R v165,a145
R v166,a146
R v167,a147
v_pk_mul_f32 v[164:165], v[164:165], v[160:161] op_sel_hi:[1,0]
v_pk_mul_f32 v[166:167], v[166:167], v[160:161] op_sel_hi:[1,0]
C v248,v164,v165
C v249,v166,v167
R v168,a148
R v169,a149
R v170,a150
R v171,a151
v_pk_mul_f32 v[168:169], v[168:169], v[160:161] op_sel_hi:[1,0]
v_pk_mul_f32 v[170:171], v[170:171], v[160:161] op_sel_hi:[1,0]
C v250,v168,v169
C v251,v170,v171
R v172,a152
R v173,a153
R v174,a154
R v175,a155
v_pk_mul_f32 v[172:173], v[172:173], v[160:161] op_sel_hi:[1,0]
v_pk_mul_f32 v[174:175], v[174:175], v[160:161] op_sel_hi:[1,0]
C v252,v172,v173
C v253,v174,v175
R v164,a156
R v165,a157
R v166,a158
R v167,a159
v_pk_mul_f32 v[164:165], v[164:165], v[160:161] op_sel_hi:[1,0]
v_pk_mul_f32 v[166:167], v[166:167], v[160:161] op_sel_hi:[1,0]
C v254,v164,v165
C v255,v166,v167
s_mov_b32 s95, 65536
s_mov_b32 s80, 0
s_mov_b32 s7, 131072
s_waitcnt lgkmcnt(0)
s_branch L_shared
L_entryY:
L_ph_qp0Y:
s_mov_b32 s65, 33792
s_mov_b32 s76, 33792
s_mov_b32 s90, -67584
s_mov_b32 s7, 0
s_mov_b32 s68, 20480
s_waitcnt vmcnt(14) lgkmcnt(0)
s_barrier
T a[144:145],v6,0
T a[146:147],v6,256
T a[148:149],v6,8448
T a[150:151],v6,8704
D a[176:179],v7,0
D a[196:199],v11,0
D a[180:183],v7,2048
D a[200:203],v11,2048
D a[184:187],v7,4096
D a[204:207],v11,4096
D a[188:191],v7,6144
D a[208:211],v11,6144
D a[192:195],v7,8192
D a[212:215],v11,8192
T a[152:153],v6,8
T a[154:155],v6,264
s_waitcnt lgkmcnt(11)
M v[16:19],a[144:147],a[176:179],0
s_waitcnt lgkmcnt(10)
T a[160:161],v6,512
M v[16:19],a[148:151],a[196:199],v[16:19]
s_waitcnt lgkmcnt(9)
T a[156:157],v6,8456
M v[48:51],a[148:151],a[200:203],0
s_mov_b32 m0, s75
T a[162:163],v6,768
M v[48:51],a[144:147],a[180:183],v[48:51]
s_waitcnt lgkmcnt(10)
T a[158:159],v6,8712
buffer_load_dwordx4 v10, s[36:39], s79 offen lds
M v[80:83],a[144:147],a[184:187],0
s_waitcnt lgkmcnt(10)
T a[164:165],v6,8960
M v[80:83],a[148:151],a[204:207],v[80:83]
s_waitcnt lgkmcnt(9)
M v[112:115],a[148:151],a[208:211],0
T a[166:167],v6,9216
M v[112:115],a[144:147],a[188:191],v[112:115]
s_waitcnt lgkmcnt(9)
s_add_u32 s71, s79, 7168
buffer_load_dwordx4 v10, s[36:39], s71 offen offset:1024 lds
M v[144:147],a[144:147],a[192:195],0
s_waitcnt lgkmcnt(8)
M v[144:147],a[148:151],a[212:215],v[144:147]
s_waitcnt lgkmcnt(6)
M v[20:23],a[152:155],a[176:179],0
s_waitcnt lgkmcnt(2)
T a[168:169],v6,520
M v[20:23],a[156:159],a[196:199],v[20:23]
s_add_u32 s72, s79, 14336
buffer_load_dwordx4 v10, s[36:39], s72 offen offset:2048 lds
M v[52:55],a[156:159],a[200:203],0
T a[170:171],v6,776
M v[52:55],a[152:155],a[180:183],v[52:55]
M v[84:87],a[152:155],a[184:187],0
T a[172:173],v6,8968
M v[84:87],a[156:159],a[204:207],v[84:87]
s_add_u32 s71, s79, 21504
buffer_load_dwordx4 v10, s[36:39], s71 offen offset:3072 lds
M v[116:119],a[156:159],a[208:211],0
T a[174:175],v6,9224
M v[116:119],a[152:155],a[188:191],v[116:119]
s_add_u32 s72, s79, 32768
M v[148:151],a[152:155],a[192:195],0
s_add_u32 m0, s75, 4096
M v[148:151],a[156:159],a[212:215],v[148:151]
buffer_load_dwordx4 v10, s[36:39], s72 offen lds
M v[24:27],a[160:163],a[176:179],0
s_waitcnt lgkmcnt(4)
s_add_u32 s79, s79, 128
T a[144:145],v6,1024
M v[24:27],a[164:167],a[196:199],v[24:27]
s_cmp_eq_u32 s7, 40960
s_cselect_b32 s67, -40960, s68
s_add_u32 s75, s75, 20480
M v[56:59],a[164:167],a[200:203],0
s_cmp_eq_u32 s75, s95
s_cselect_b32 s75, s80, s75
T a[146:147],v6,1280
M v[56:59],a[160:163],a[180:183],v[56:59]
s_add_u32 s7, s7, s67
s_add_u32 s70, s74, 0
s_mov_b32 m0, s70
M v[88:91],a[160:163],a[184:187],0
T a[148:149],v6,9472
buffer_load_dwordx4 v9, s[40:43], s78 offen lds
M v[88:91],a[164:167],a[204:207],v[88:91]
v_add_u32 v7, s67, v7
M v[120:123],a[164:167],a[208:211],0
T a[150:151],v6,9728
M v[120:123],a[160:163],a[188:191],v[120:123]
v_add_u32 v11, s67, v11
M v[152:155],a[160:163],a[192:195],0
s_add_u32 s71, s78, 129024
buffer_load_dwordx4 v9, s[40:43], s71 offen offset:2048 lds
M v[152:155],a[164:167],a[212:215],v[152:155]
s_waitcnt lgkmcnt(6)
M v[28:31],a[168:171],a[176:179],0
s_waitcnt lgkmcnt(4)
T a[152:153],v6,1032
M v[28:31],a[172:175],a[196:199],v[28:31]
s_add_u32 m0, s70, 896
M v[60:63],a[172:175],a[200:203],0
T a[154:155],v6,1288
buffer_load_dwordx4 v9, s[40:43], s78 offen offset:128 lds
M v[60:63],a[168:171],a[180:183],v[60:63]
M v[92:95],a[168:171],a[184:187],0
T a[156:157],v6,9480
M v[92:95],a[172:175],a[204:207],v[92:95]
M v[124:127],a[172:175],a[208:211],0
T a[158:159],v6,9736
buffer_load_dwordx4 v9, s[40:43], s71 offen offset:2176 lds
M v[124:127],a[168:171],a[188:191],v[124:127]
M v[156:159],a[168:171],a[192:195],0
s_add_u32 s78, s78, 65536
M v[156:159],a[172:175],a[212:215],v[156:159]
s_waitcnt lgkmcnt(6)
s_add_u32 m0, s70, 4104
M v[32:35],a[144:147],a[176:179],0
s_waitcnt lgkmcnt(4)
T a[160:161],v6,1536
buffer_load_dwordx4 v9, s[40:43], s78 offen lds
M v[32:35],a[148:151],a[196:199],v[32:35]
M v[64:67],a[148:151],a[200:203],0
T a[162:163],v6,1792
M v[64:67],a[144:147],a[180:183],v[64:67]
M v[96:99],a[144:147],a[184:187],0
s_add_u32 s71, s78, 129024
T a[164:165],v6,9984
buffer_load_dwordx4 v9, s[40:43], s71 offen offset:2048 lds
M v[96:99],a[148:151],a[204:207],v[96:99]
M v[128:131],a[148:151],a[208:211],0
T a[166:167],v6,10240
M v[128:131],a[144:147],a[188:191],v[128:131]
s_add_u32 m0, s70, 5000
M v[160:163],a[144:147],a[192:195],0
buffer_load_dwordx4 v9, s[40:43], s78 offen offset:128 lds
M v[160:163],a[148:151],a[212:215],v[160:163]
s_waitcnt lgkmcnt(6)
M v[36:39],a[152:155],a[176:179],0
s_waitcnt lgkmcnt(4)
T a[168:169],v6,1544
M v[36:39],a[156:159],a[196:199],v[36:39]
M v[68:71],a[156:159],a[200:203],0
s_add_u32 s78, s78, 458752
T a[170:171],v6,1800
buffer_load_dwordx4 v9, s[40:43], s71 offen offset:2176 lds
M v[68:71],a[152:155],a[180:183],v[68:71]
M v[100:103],a[152:155],a[184:187],0
T a[172:173],v6,9992
M v[100:103],a[156:159],a[204:207],v[100:103]
M v[132:135],a[156:159],a[208:211],0
T a[174:175],v6,10248
M v[132:135],a[152:155],a[188:191],v[132:135]
s_mov_b32 s64, s65
M v[164:167],a[152:155],a[192:195],0
s_cmp_eq_u32 s65, 67584
s_cselect_b32 s66, s90, s76
M v[164:167],a[156:159],a[212:215],v[164:167]
s_waitcnt lgkmcnt(6)
s_add_u32 s65, s65, s66
M v[40:43],a[160:163],a[176:179],0
s_waitcnt lgkmcnt(4)
M v[40:43],a[164:167],a[196:199],v[40:43]
s_waitcnt vmcnt(14) lgkmcnt(0)
v_add_u32 v6, s66, v6
M v[72:75],a[164:167],a[200:203],0
s_barrier
M v[72:75],a[160:163],a[180:183],v[72:75]
T a[144:145],v6,0
M v[104:107],a[160:163],a[184:187],0
s_mov_b32 m0, s75
T a[146:147],v6,256
M v[104:107],a[164:167],a[204:207],v[104:107]
T a[148:149],v6,8448
buffer_load_dwordx4 v10, s[36:39], s79 offen lds
M v[136:139],a[164:167],a[208:211],0
T a[150:151],v6,8704
M v[136:139],a[160:163],a[188:191],v[136:139]
T a[152:153],v6,8
M v[168:171],a[160:163],a[192:195],0
T a[154:155],v6,264
M v[168:171],a[164:167],a[212:215],v[168:171]
M v[44:47],a[168:171],a[176:179],0
s_add_u32 s71, s79, 7168
D a[176:179],v7,0
buffer_load_dwordx4 v10, s[36:39], s71 offen offset:1024 lds
M v[44:47],a[172:175],a[196:199],v[44:47]
D a[196:199],v11,0
M v[76:79],a[172:175],a[200:203],0
D a[200:203],v11,2048
M v[76:79],a[168:171],a[180:183],v[76:79]
D a[180:183],v7,2048
M v[108:111],a[168:171],a[184:187],0
D a[184:187],v7,4096
M v[108:111],a[172:175],a[204:207],v[108:111]
s_add_u32 s72, s79, 14336
D a[204:207],v11,4096
buffer_load_dwordx4 v10, s[36:39], s72 offen offset:2048 lds
M v[140:143],a[172:175],a[208:211],0
D a[208:211],v11,6144
M v[140:143],a[168:171],a[188:191],v[140:143]
D a[188:191],v7,6144
M v[172:175],a[168:171],a[192:195],0
D a[192:195],v7,8192
M v[172:175],a[172:175],a[212:215],v[172:175]
D a[212:215],v11,8192
s_waitcnt vmcnt(16)
v_add_u32 v12, 1, v12
s_nop 0
v_readlane_b32 s82, v12, 0
v_readlane_b32 s83, v12, 1
v_readlane_b32 s84, v12, 2
v_readlane_b32 s85, v12, 3
v_readlane_b32 s86, v12, 4
v_readlane_b32 s87, v12, 5
v_readlane_b32 s88, v12, 6
v_readlane_b32 s89, v12, 7
s_waitcnt lgkmcnt(9)
M v[16:19],a[144:147],a[176:179],v[16:19]
s_waitcnt lgkmcnt(8)
s_add_u32 s71, s79, 21504
T a[160:161],v6,512
buffer_load_dwordx4 v10, s[36:39], s71 offen offset:3072 lds
M v[16:19],a[148:151],a[196:199],v[16:19]
s_waitcnt lgkmcnt(8)
T a[156:157],v6,8456
M v[48:51],a[148:151],a[200:203],v[48:51]
s_waitcnt lgkmcnt(8)
T a[162:163],v6,768
M v[48:51],a[144:147],a[180:183],v[48:51]
s_waitcnt lgkmcnt(8)
s_add_u32 s72, s79, 32768
T a[158:159],v6,8712
M v[80:83],a[144:147],a[184:187],v[80:83]
s_waitcnt lgkmcnt(8)
s_add_u32 m0, s75, 4096
T a[164:165],v6,8960
M v[80:83],a[148:151],a[204:207],v[80:83]
s_waitcnt lgkmcnt(8)
buffer_load_dwordx4 v10, s[36:39], s72 offen lds
M v[112:115],a[148:151],a[208:211],v[112:115]
s_waitcnt lgkmcnt(7)
s_add_u32 s79, s79, 128
T a[166:167],v6,9216
M v[112:115],a[144:147],a[188:191],v[112:115]
s_waitcnt lgkmcnt(7)
s_add_u32 s75, s75, 20480
M v[144:147],a[144:147],a[192:195],v[144:147]
s_waitcnt lgkmcnt(6)
s_cmp_eq_u32 s75, s95
)"
R"(s_cselect_b32 s75, s80, s75
M v[144:147],a[148:151],a[212:215],v[144:147]
s_add_u32 s70, s74, s64
s_mov_b32 m0, s70
M v[20:23],a[152:155],a[176:179],v[20:23]
s_waitcnt lgkmcnt(2)
T a[168:169],v6,520
buffer_load_dwordx4 v9, s[40:43], s78 offen lds
M v[20:23],a[156:159],a[196:199],v[20:23]
M v[52:55],a[156:159],a[200:203],v[52:55]
T a[170:171],v6,776
M v[52:55],a[152:155],a[180:183],v[52:55]
M v[84:87],a[152:155],a[184:187],v[84:87]
T a[172:173],v6,8968
M v[84:87],a[156:159],a[204:207],v[84:87]
s_add_u32 s71, s78, 129024
buffer_load_dwordx4 v9, s[40:43], s71 offen offset:2048 lds
M v[116:119],a[156:159],a[208:211],v[116:119]
T a[174:175],v6,9224
M v[116:119],a[152:155],a[188:191],v[116:119]
M v[148:151],a[152:155],a[192:195],v[148:151]
M v[148:151],a[156:159],a[212:215],v[148:151]
s_add_u32 m0, s70, 896
M v[24:27],a[160:163],a[176:179],v[24:27]
s_waitcnt lgkmcnt(4)
T a[144:145],v6,1024
buffer_load_dwordx4 v9, s[40:43], s78 offen offset:128 lds
M v[24:27],a[164:167],a[196:199],v[24:27]
s_cmp_eq_u32 s7, 40960
s_cselect_b32 s67, -40960, s68
M v[56:59],a[164:167],a[200:203],v[56:59]
T a[146:147],v6,1280
M v[56:59],a[160:163],a[180:183],v[56:59]
s_add_u32 s7, s7, s67
M v[88:91],a[160:163],a[184:187],v[88:91]
T a[148:149],v6,9472
M v[88:91],a[164:167],a[204:207],v[88:91]
buffer_load_dwordx4 v9, s[40:43], s71 offen offset:2176 lds
v_add_u32 v7, s67, v7
M v[120:123],a[164:167],a[208:211],v[120:123]
T a[150:151],v6,9728
M v[120:123],a[160:163],a[188:191],v[120:123]
v_add_u32 v11, s67, v11
M v[152:155],a[160:163],a[192:195],v[152:155]
s_add_u32 s78, s78, 65536
M v[152:155],a[164:167],a[212:215],v[152:155]
s_waitcnt lgkmcnt(6)
s_add_u32 m0, s70, 4104
M v[28:31],a[168:171],a[176:179],v[28:31]
s_waitcnt lgkmcnt(4)
T a[152:153],v6,1032
buffer_load_dwordx4 v9, s[40:43], s78 offen lds
M v[28:31],a[172:175],a[196:199],v[28:31]
M v[60:63],a[172:175],a[200:203],v[60:63]
T a[154:155],v6,1288
M v[60:63],a[168:171],a[180:183],v[60:63]
M v[92:95],a[168:171],a[184:187],v[92:95]
T a[156:157],v6,9480
M v[92:95],a[172:175],a[204:207],v[92:95]
s_add_u32 s71, s78, 129024
buffer_load_dwordx4 v9, s[40:43], s71 offen offset:2048 lds
M v[124:127],a[172:175],a[208:211],v[124:127]
T a[158:159],v6,9736
M v[124:127],a[168:171],a[188:191],v[124:127]
M v[156:159],a[168:171],a[192:195],v[156:159]
M v[156:159],a[172:175],a[212:215],v[156:159]
s_waitcnt lgkmcnt(6)
s_add_u32 m0, s70, 5000
M v[32:35],a[144:147],a[176:179],v[32:35]
s_waitcnt lgkmcnt(4)
T a[160:161],v6,1536
buffer_load_dwordx4 v9, s[40:43], s78 offen offset:128 lds
M v[32:35],a[148:151],a[196:199],v[32:35]
M v[64:67],a[148:151],a[200:203],v[64:67]
T a[162:163],v6,1792
M v[64:67],a[144:147],a[180:183],v[64:67]
M v[96:99],a[144:147],a[184:187],v[96:99]
T a[164:165],v6,9984
M v[96:99],a[148:151],a[204:207],v[96:99]
s_add_u32 s78, s78, 458752
buffer_load_dwordx4 v9, s[40:43], s71 offen offset:2176 lds
M v[128:131],a[148:151],a[208:211],v[128:131]
T a[166:167],v6,10240
M v[128:131],a[144:147],a[188:191],v[128:131]
M v[160:163],a[144:147],a[192:195],v[160:163]
M v[160:163],a[148:151],a[212:215],v[160:163]
s_waitcnt lgkmcnt(6)
M v[36:39],a[152:155],a[176:179],v[36:39]
s_waitcnt lgkmcnt(4)
T a[168:169],v6,1544
M v[36:39],a[156:159],a[196:199],v[36:39]
M v[68:71],a[156:159],a[200:203],v[68:71]
T a[170:171],v6,1800
M v[68:71],a[152:155],a[180:183],v[68:71]
M v[100:103],a[152:155],a[184:187],v[100:103]
T a[172:173],v6,9992
M v[100:103],a[156:159],a[204:207],v[100:103]
M v[132:135],a[156:159],a[208:211],v[132:135]
T a[174:175],v6,10248
M v[132:135],a[152:155],a[188:191],v[132:135]
s_mov_b32 s64, s65
M v[164:167],a[152:155],a[192:195],v[164:167]
s_cmp_eq_u32 s65, 67584
s_cselect_b32 s66, s90, s76
M v[164:167],a[156:159],a[212:215],v[164:167]
s_waitcnt lgkmcnt(6)
s_add_u32 s65, s65, s66
M v[40:43],a[160:163],a[176:179],v[40:43]
s_waitcnt lgkmcnt(4)
M v[40:43],a[164:167],a[196:199],v[40:43]
s_waitcnt vmcnt(13) lgkmcnt(0)
v_add_u32 v6, s66, v6
M v[72:75],a[164:167],a[200:203],v[72:75]
s_barrier
M v[72:75],a[160:163],a[180:183],v[72:75]
T a[144:145],v6,0
M v[104:107],a[160:163],a[184:187],v[104:107]
s_mov_b32 m0, s75
T a[146:147],v6,256
M v[104:107],a[164:167],a[204:207],v[104:107]
T a[148:149],v6,8448
buffer_load_dwordx4 v10, s[36:39], s79 offen lds
M v[136:139],a[164:167],a[208:211],v[136:139]
T a[150:151],v6,8704
M v[136:139],a[160:163],a[188:191],v[136:139]
T a[152:153],v6,8
M v[168:171],a[160:163],a[192:195],v[168:171]
T a[154:155],v6,264
M v[168:171],a[164:167],a[212:215],v[168:171]
M v[44:47],a[168:171],a[176:179],v[44:47]
s_add_u32 s71, s79, 7168
D a[176:179],v7,0
buffer_load_dwordx4 v10, s[36:39], s71 offen offset:1024 lds
M v[44:47],a[172:175],a[196:199],v[44:47]
D a[196:199],v11,0
M v[76:79],a[172:175],a[200:203],v[76:79]
D a[200:203],v11,2048
M v[76:79],a[168:171],a[180:183],v[76:79]
D a[180:183],v7,2048
M v[108:111],a[168:171],a[184:187],v[108:111]
D a[184:187],v7,4096
M v[108:111],a[172:175],a[204:207],v[108:111]
s_add_u32 s72, s79, 14336
D a[204:207],v11,4096
buffer_load_dwordx4 v10, s[36:39], s72 offen offset:2048 lds
M v[140:143],a[172:175],a[208:211],v[140:143]
D a[208:211],v11,6144
M v[140:143],a[168:171],a[188:191],v[140:143]
D a[188:191],v7,6144
M v[172:175],a[168:171],a[192:195],v[172:175]
D a[192:195],v7,8192
M v[172:175],a[172:175],a[212:215],v[172:175]
D a[212:215],v11,8192
s_mov_b32 s69, 3
.p2align 3
L_qpY_3:
s_waitcnt lgkmcnt(9)
M v[16:19],a[144:147],a[176:179],v[16:19]
s_waitcnt lgkmcnt(8)
s_add_u32 s71, s79, 21504
T a[160:161],v6,512
buffer_load_dwordx4 v10, s[36:39], s71 offen offset:3072 lds
M v[16:19],a[148:151],a[196:199],v[16:19]
s_waitcnt lgkmcnt(8)
T a[156:157],v6,8456
M v[48:51],a[148:151],a[200:203],v[48:51]
s_waitcnt lgkmcnt(8)
T a[162:163],v6,768
M v[48:51],a[144:147],a[180:183],v[48:51]
s_waitcnt lgkmcnt(8)
s_add_u32 s72, s79, 32768
T a[158:159],v6,8712
M v[80:83],a[144:147],a[184:187],v[80:83]
s_waitcnt lgkmcnt(8)
s_add_u32 m0, s75, 4096
T a[164:165],v6,8960
M v[80:83],a[148:151],a[204:207],v[80:83]
s_waitcnt lgkmcnt(8)
buffer_load_dwordx4 v10, s[36:39], s72 offen lds
M v[112:115],a[148:151],a[208:211],v[112:115]
s_waitcnt lgkmcnt(7)
s_add_u32 s79, s79, 128
T a[166:167],v6,9216
M v[112:115],a[144:147],a[188:191],v[112:115]
s_waitcnt lgkmcnt(7)
s_add_u32 s75, s75, 20480
M v[144:147],a[144:147],a[192:195],v[144:147]
s_waitcnt lgkmcnt(6)
s_cmp_eq_u32 s75, s95
s_cselect_b32 s75, s80, s75
M v[144:147],a[148:151],a[212:215],v[144:147]
s_add_u32 s70, s74, s64
s_mov_b32 m0, s70
M v[20:23],a[152:155],a[176:179],v[20:23]
s_waitcnt lgkmcnt(2)
T a[168:169],v6,520
buffer_load_dwordx4 v9, s[40:43], s78 offen lds
M v[20:23],a[156:159],a[196:199],v[20:23]
M v[52:55],a[156:159],a[200:203],v[52:55]
T a[170:171],v6,776
M v[52:55],a[152:155],a[180:183],v[52:55]
M v[84:87],a[152:155],a[184:187],v[84:87]
T a[172:173],v6,8968
M v[84:87],a[156:159],a[204:207],v[84:87]
s_add_u32 s71, s78, 129024
buffer_load_dwordx4 v9, s[40:43], s71 offen offset:2048 lds
M v[116:119],a[156:159],a[208:211],v[116:119]
T a[174:175],v6,9224
M v[116:119],a[152:155],a[188:191],v[116:119]
M v[148:151],a[152:155],a[192:195],v[148:151]
M v[148:151],a[156:159],a[212:215],v[148:151]
s_add_u32 m0, s70, 896
M v[24:27],a[160:163],a[176:179],v[24:27]
s_waitcnt lgkmcnt(4)
T a[144:145],v6,1024
buffer_load_dwordx4 v9, s[40:43], s78 offen offset:128 lds
M v[24:27],a[164:167],a[196:199],v[24:27]
s_cmp_eq_u32 s7, 40960
s_cselect_b32 s67, -40960, s68
M v[56:59],a[164:167],a[200:203],v[56:59]
T a[146:147],v6,1280
M v[56:59],a[160:163],a[180:183],v[56:59]
s_add_u32 s7, s7, s67
M v[88:91],a[160:163],a[184:187],v[88:91]
T a[148:149],v6,9472
M v[88:91],a[164:167],a[204:207],v[88:91]
buffer_load_dwordx4 v9, s[40:43], s71 offen offset:2176 lds
v_add_u32 v7, s67, v7
M v[120:123],a[164:167],a[208:211],v[120:123]
T a[150:151],v6,9728
M v[120:123],a[160:163],a[188:191],v[120:123]
v_add_u32 v11, s67, v11
M v[152:155],a[160:163],a[192:195],v[152:155]
s_add_u32 s78, s78, 65536
M v[152:155],a[164:167],a[212:215],v[152:155]
s_waitcnt lgkmcnt(6)
s_add_u32 m0, s70, 4104
M v[28:31],a[168:171],a[176:179],v[28:31]
s_waitcnt lgkmcnt(4)
T a[152:153],v6,1032
buffer_load_dwordx4 v9, s[40:43], s78 offen lds
M v[28:31],a[172:175],a[196:199],v[28:31]
M v[60:63],a[172:175],a[200:203],v[60:63]
T a[154:155],v6,1288
M v[60:63],a[168:171],a[180:183],v[60:63]
M v[92:95],a[168:171],a[184:187],v[92:95]
T a[156:157],v6,9480
M v[92:95],a[172:175],a[204:207],v[92:95]
s_add_u32 s71, s78, 129024
buffer_load_dwordx4 v9, s[40:43], s71 offen offset:2048 lds
M v[124:127],a[172:175],a[208:211],v[124:127]
T a[158:159],v6,9736
M v[124:127],a[168:171],a[188:191],v[124:127]
M v[156:159],a[168:171],a[192:195],v[156:159]
M v[156:159],a[172:175],a[212:215],v[156:159]
s_waitcnt lgkmcnt(6)
s_add_u32 m0, s70, 5000
M v[32:35],a[144:147],a[176:179],v[32:35]
s_waitcnt lgkmcnt(4)
T a[160:161],v6,1536
buffer_load_dwordx4 v9, s[40:43], s78 offen offset:128 lds
M v[32:35],a[148:151],a[196:199],v[32:35]
M v[64:67],a[148:151],a[200:203],v[64:67]
T a[162:163],v6,1792
M v[64:67],a[144:147],a[180:183],v[64:67]
M v[96:99],a[144:147],a[184:187],v[96:99]
T a[164:165],v6,9984
M v[96:99],a[148:151],a[204:207],v[96:99]
s_add_u32 s78, s78, 458752
buffer_load_dwordx4 v9, s[40:43], s71 offen offset:2176 lds
M v[128:131],a[148:151],a[208:211],v[128:131]
T a[166:167],v6,10240
M v[128:131],a[144:147],a[188:191],v[128:131]
M v[160:163],a[144:147],a[192:195],v[160:163]
M v[160:163],a[148:151],a[212:215],v[160:163]
s_waitcnt lgkmcnt(6)
M v[36:39],a[152:155],a[176:179],v[36:39]
s_waitcnt lgkmcnt(4)
T a[168:169],v6,1544
M v[36:39],a[156:159],a[196:199],v[36:39]
M v[68:71],a[156:159],a[200:203],v[68:71]
T a[170:171],v6,1800
M v[68:71],a[152:155],a[180:183],v[68:71]
M v[100:103],a[152:155],a[184:187],v[100:103]
T a[172:173],v6,9992
M v[100:103],a[156:159],a[204:207],v[100:103]
M v[132:135],a[156:159],a[208:211],v[132:135]
T a[174:175],v6,10248
M v[132:135],a[152:155],a[188:191],v[132:135]
s_mov_b32 s64, s65
M v[164:167],a[152:155],a[192:195],v[164:167]
s_cmp_eq_u32 s65, 67584
s_cselect_b32 s66, s90, s76
M v[164:167],a[156:159],a[212:215],v[164:167]
s_waitcnt lgkmcnt(6)
s_add_u32 s65, s65, s66
M v[40:43],a[160:163],a[176:179],v[40:43]
s_waitcnt lgkmcnt(4)
M v[40:43],a[164:167],a[196:199],v[40:43]
s_waitcnt vmcnt(13) lgkmcnt(0)
v_add_u32 v6, s66, v6
M v[72:75],a[164:167],a[200:203],v[72:75]
s_barrier
M v[72:75],a[160:163],a[180:183],v[72:75]
T a[144:145],v6,0
M v[104:107],a[160:163],a[184:187],v[104:107]
s_mov_b32 m0, s75
T a[146:147],v6,256
M v[104:107],a[164:167],a[204:207],v[104:107]
T a[148:149],v6,8448
buffer_load_dwordx4 v10, s[36:39], s79 offen lds
M v[136:139],a[164:167],a[208:211],v[136:139]
T a[150:151],v6,8704
M v[136:139],a[160:163],a[188:191],v[136:139]
T a[152:153],v6,8
M v[168:171],a[160:163],a[192:195],v[168:171]
T a[154:155],v6,264
M v[168:171],a[164:167],a[212:215],v[168:171]
M v[44:47],a[168:171],a[176:179],v[44:47]
s_add_u32 s71, s79, 7168
D a[176:179],v7,0
buffer_load_dwordx4 v10, s[36:39], s71 offen offset:1024 lds
M v[44:47],a[172:175],a[196:199],v[44:47]
D a[196:199],v11,0
M v[76:79],a[172:175],a[200:203],v[76:79]
D a[200:203],v11,2048
M v[76:79],a[168:171],a[180:183],v[76:79]
D a[180:183],v7,2048
M v[108:111],a[168:171],a[184:187],v[108:111]
D a[184:187],v7,4096
M v[108:111],a[172:175],a[204:207],v[108:111]
s_add_u32 s72, s79, 14336
D a[204:207],v11,4096
buffer_load_dwordx4 v10, s[36:39], s72 offen offset:2048 lds
M v[140:143],a[172:175],a[208:211],v[140:143]
D a[208:211],v11,6144
M v[140:143],a[168:171],a[188:191],v[140:143]
D a[188:191],v7,6144
M v[172:175],a[168:171],a[192:195],v[172:175]
s_sub_u32 s69, s69, 1
D a[192:195],v7,8192
M v[172:175],a[172:175],a[212:215],v[172:175]
s_cmp_lg_u32 s69, 0
D a[212:215],v11,8192
s_cbranch_scc1 L_qpY_3
s_waitcnt lgkmcnt(9)
M v[16:19],a[144:147],a[176:179],v[16:19]
s_waitcnt lgkmcnt(8)
s_add_u32 s71, s79, 21504
T a[160:161],v6,512
buffer_load_dwordx4 v10, s[36:39], s71 offen offset:3072 lds
M v[16:19],a[148:151],a[196:199],v[16:19]
s_waitcnt lgkmcnt(8)
T a[156:157],v6,8456
M v[48:51],a[148:151],a[200:203],v[48:51]
s_waitcnt lgkmcnt(8)
T a[162:163],v6,768
M v[48:51],a[144:147],a[180:183],v[48:51]
s_waitcnt lgkmcnt(8)
s_add_u32 s72, s79, 32768
T a[158:159],v6,8712
M v[80:83],a[144:147],a[184:187],v[80:83]
s_waitcnt lgkmcnt(8)
s_add_u32 m0, s75, 4096
T a[164:165],v6,8960
M v[80:83],a[148:151],a[204:207],v[80:83]
s_waitcnt lgkmcnt(8)
buffer_load_dwordx4 v10, s[36:39], s72 offen lds
M v[112:115],a[148:151],a[208:211],v[112:115]
s_waitcnt lgkmcnt(7)
s_add_u32 s79, s79, 128
T a[166:167],v6,9216
M v[112:115],a[144:147],a[188:191],v[112:115]
s_waitcnt lgkmcnt(7)
s_add_u32 s75, s75, 20480
M v[144:147],a[144:147],a[192:195],v[144:147]
s_waitcnt lgkmcnt(6)
s_cmp_eq_u32 s75, s95
s_cselect_b32 s75, s80, s75
M v[144:147],a[148:151],a[212:215],v[144:147]
s_add_u32 s70, s74, s64
s_mov_b32 m0, s70
M v[20:23],a[152:155],a[176:179],v[20:23]
s_waitcnt lgkmcnt(2)
T a[168:169],v6,520
buffer_load_dwordx4 v9, s[40:43], s78 offen lds
M v[20:23],a[156:159],a[196:199],v[20:23]
M v[52:55],a[156:159],a[200:203],v[52:55]
T a[170:171],v6,776
M v[52:55],a[152:155],a[180:183],v[52:55]
M v[84:87],a[152:155],a[184:187],v[84:87]
T a[172:173],v6,8968
M v[84:87],a[156:159],a[204:207],v[84:87]
s_add_u32 s71, s78, 129024
buffer_load_dwordx4 v9, s[40:43], s71 offen offset:2048 lds
M v[116:119],a[156:159],a[208:211],v[116:119]
T a[174:175],v6,9224
M v[116:119],a[152:155],a[188:191],v[116:119]
M v[148:151],a[152:155],a[192:195],v[148:151]
M v[148:151],a[156:159],a[212:215],v[148:151]
s_add_u32 m0, s70, 896
M v[24:27],a[160:163],a[176:179],v[24:27]
s_waitcnt lgkmcnt(4)
T a[144:145],v6,1024
buffer_load_dwordx4 v9, s[40:43], s78 offen offset:128 lds
M v[24:27],a[164:167],a[196:199],v[24:27]
s_cmp_eq_u32 s7, 40960
s_cselect_b32 s67, -40960, s68
M v[56:59],a[164:167],a[200:203],v[56:59]
T a[146:147],v6,1280
M v[56:59],a[160:163],a[180:183],v[56:59]
s_add_u32 s7, s7, s67
M v[88:91],a[160:163],a[184:187],v[88:91]
T a[148:149],v6,9472
M v[88:91],a[164:167],a[204:207],v[88:91]
buffer_load_dwordx4 v9, s[40:43], s71 offen offset:2176 lds
v_add_u32 v7, s67, v7
M v[120:123],a[164:167],a[208:211],v[120:123]
T a[150:151],v6,9728
M v[120:123],a[160:163],a[188:191],v[120:123]
v_add_u32 v11, s67, v11
M v[152:155],a[160:163],a[192:195],v[152:155]
s_add_u32 s78, s78, 65536
M v[152:155],a[164:167],a[212:215],v[152:155]
s_waitcnt lgkmcnt(6)
s_add_u32 m0, s70, 4104
M v[28:31],a[168:171],a[176:179],v[28:31]
s_waitcnt lgkmcnt(4)
T a[152:153],v6,1032
buffer_load_dwordx4 v9, s[40:43], s78 offen lds
M v[28:31],a[172:175],a[196:199],v[28:31]
M v[60:63],a[172:175],a[200:203],v[60:63]
T a[154:155],v6,1288
M v[60:63],a[168:171],a[180:183],v[60:63]
M v[92:95],a[168:171],a[184:187],v[92:95]
T a[156:157],v6,9480
M v[92:95],a[172:175],a[204:207],v[92:95]
s_add_u32 s71, s78, 129024
buffer_load_dwordx4 v9, s[40:43], s71 offen offset:2048 lds
M v[124:127],a[172:175],a[208:211],v[124:127]
T a[158:159],v6,9736
M v[124:127],a[168:171],a[188:191],v[124:127]
M v[156:159],a[168:171],a[192:195],v[156:159]
M v[156:159],a[172:175],a[212:215],v[156:159]
s_waitcnt lgkmcnt(6)
s_add_u32 m0, s70, 5000
M v[32:35],a[144:147],a[176:179],v[32:35]
s_waitcnt lgkmcnt(4)
T a[160:161],v6,1536
buffer_load_dwordx4 v9, s[40:43], s78 offen offset:128 lds
M v[32:35],a[148:151],a[196:199],v[32:35]
M v[64:67],a[148:151],a[200:203],v[64:67]
T a[162:163],v6,1792
M v[64:67],a[144:147],a[180:183],v[64:67]
M v[96:99],a[144:147],a[184:187],v[96:99]
T a[164:165],v6,9984
M v[96:99],a[148:151],a[204:207],v[96:99]
s_add_u32 s78, s78, 458752
buffer_load_dwordx4 v9, s[40:43], s71 offen offset:2176 lds
M v[128:131],a[148:151],a[208:211],v[128:131]
T a[166:167],v6,10240
M v[128:131],a[144:147],a[188:191],v[128:131]
M v[160:163],a[144:147],a[192:195],v[160:163]
M v[160:163],a[148:151],a[212:215],v[160:163]
s_waitcnt lgkmcnt(6)
M v[36:39],a[152:155],a[176:179],v[36:39]
s_waitcnt lgkmcnt(4)
T a[168:169],v6,1544
M v[36:39],a[156:159],a[196:199],v[36:39]
M v[68:71],a[156:159],a[200:203],v[68:71]
T a[170:171],v6,1800
M v[68:71],a[152:155],a[180:183],v[68:71]
M v[100:103],a[152:155],a[184:187],v[100:103]
T a[172:173],v6,9992
M v[100:103],a[156:159],a[204:207],v[100:103]
M v[132:135],a[156:159],a[208:211],v[132:135]
T a[174:175],v6,10248
M v[132:135],a[152:155],a[188:191],v[132:135]
s_mov_b32 s64, s65
M v[164:167],a[152:155],a[192:195],v[164:167]
s_cmp_eq_u32 s65, 67584
s_cselect_b32 s66, s90, s76
M v[164:167],a[156:159],a[212:215],v[164:167]
s_waitcnt lgkmcnt(6)
s_add_u32 s65, s65, s66
M v[40:43],a[160:163],a[176:179],v[40:43]
s_waitcnt lgkmcnt(4)
M v[40:43],a[164:167],a[196:199],v[40:43]
s_waitcnt vmcnt(13) lgkmcnt(0)
v_add_u32 v6, s66, v6
M v[72:75],a[164:167],a[200:203],v[72:75]
s_barrier
M v[72:75],a[160:163],a[180:183],v[72:75]
s_add_u32 s70, s73, 0
T a[144:145],v6,0
M v[104:107],a[160:163],a[184:187],v[104:107]
s_mov_b32 m0, s70
T a[146:147],v6,256
M v[104:107],a[164:167],a[204:207],v[104:107]
T a[148:149],v6,8448
)"
R"(buffer_load_dwordx4 v8, s[44:47], s6 offen nt lds
M v[136:139],a[164:167],a[208:211],v[136:139]
T a[150:151],v6,8704
M v[136:139],a[160:163],a[188:191],v[136:139]
T a[152:153],v6,8
M v[168:171],a[160:163],a[192:195],v[168:171]
T a[154:155],v6,264
M v[168:171],a[164:167],a[212:215],v[168:171]
M v[44:47],a[168:171],a[176:179],v[44:47]
D a[176:179],v7,0
M v[44:47],a[172:175],a[196:199],v[44:47]
s_add_u32 s77, s77, 8192
D a[196:199],v11,0
buffer_load_dwordx4 v8, s[44:47], s6 offen offset:2048 nt lds
M v[76:79],a[172:175],a[200:203],v[76:79]
D a[200:203],v11,2048
M v[76:79],a[168:171],a[180:183],v[76:79]
D a[180:183],v7,2048
M v[108:111],a[168:171],a[184:187],v[108:111]
D a[184:187],v7,4096
M v[108:111],a[172:175],a[204:207],v[108:111]
D a[204:207],v11,4096
M v[140:143],a[172:175],a[208:211],v[140:143]
s_add_u32 m0, s70, 896
D a[208:211],v11,6144
M v[140:143],a[168:171],a[188:191],v[140:143]
D a[188:191],v7,6144
buffer_load_dwordx4 v8, s[44:47], s6 offen offset:128 nt lds
M v[172:175],a[168:171],a[192:195],v[172:175]
D a[192:195],v7,8192
M v[172:175],a[172:175],a[212:215],v[172:175]
D a[212:215],v11,8192
s_waitcnt lgkmcnt(9)
M v[16:19],a[144:147],a[176:179],v[16:19]
s_waitcnt lgkmcnt(8)
T a[160:161],v6,512
M v[16:19],a[148:151],a[196:199],v[16:19]
s_waitcnt lgkmcnt(8)
T a[156:157],v6,8456
M v[48:51],a[148:151],a[200:203],v[48:51]
s_waitcnt lgkmcnt(8)
T a[162:163],v6,768
M v[48:51],a[144:147],a[180:183],v[48:51]
s_waitcnt lgkmcnt(8)
s_add_u32 s71, s6, 4096
T a[158:159],v6,8712
buffer_load_dwordx4 v8, s[44:47], s6 offen offset:2176 nt lds
M v[80:83],a[144:147],a[184:187],v[80:83]
s_waitcnt lgkmcnt(8)
T a[164:165],v6,8960
M v[80:83],a[148:151],a[204:207],v[80:83]
s_waitcnt lgkmcnt(8)
M v[112:115],a[148:151],a[208:211],v[112:115]
s_waitcnt lgkmcnt(7)
T a[166:167],v6,9216
M v[112:115],a[144:147],a[188:191],v[112:115]
s_waitcnt lgkmcnt(7)
M v[144:147],a[144:147],a[192:195],v[144:147]
s_waitcnt lgkmcnt(6)
s_add_u32 m0, s70, 4096
M v[144:147],a[148:151],a[212:215],v[144:147]
buffer_load_dwordx4 v8, s[44:47], s71 offen nt lds
M v[20:23],a[152:155],a[176:179],v[20:23]
s_waitcnt lgkmcnt(2)
T a[168:169],v6,520
M v[20:23],a[156:159],a[196:199],v[20:23]
M v[52:55],a[156:159],a[200:203],v[52:55]
T a[170:171],v6,776
M v[52:55],a[152:155],a[180:183],v[52:55]
M v[84:87],a[152:155],a[184:187],v[84:87]
T a[172:173],v6,8968
M v[84:87],a[156:159],a[204:207],v[84:87]
s_and_b32 s77, s77, 131071
buffer_load_dwordx4 v8, s[44:47], s71 offen offset:2048 nt lds
M v[116:119],a[156:159],a[208:211],v[116:119]
T a[174:175],v6,9224
M v[116:119],a[152:155],a[188:191],v[116:119]
M v[148:151],a[152:155],a[192:195],v[148:151]
M v[148:151],a[156:159],a[212:215],v[148:151]
M v[24:27],a[160:163],a[176:179],v[24:27]
s_waitcnt lgkmcnt(4)
s_add_u32 m0, s70, 4992
T a[144:145],v6,1024
M v[24:27],a[164:167],a[196:199],v[24:27]
s_cmp_eq_u32 s7, 40960
s_cselect_b32 s67, -40960, s68
buffer_load_dwordx4 v8, s[44:47], s71 offen offset:128 nt lds
M v[56:59],a[164:167],a[200:203],v[56:59]
T a[146:147],v6,1280
M v[56:59],a[160:163],a[180:183],v[56:59]
s_add_u32 s7, s7, s67
M v[88:91],a[160:163],a[184:187],v[88:91]
T a[148:149],v6,9472
M v[88:91],a[164:167],a[204:207],v[88:91]
v_add_u32 v7, s67, v7
M v[120:123],a[164:167],a[208:211],v[120:123]
T a[150:151],v6,9728
M v[120:123],a[160:163],a[188:191],v[120:123]
s_cmp_eq_u32 s77, s4
s_cselect_b32 s5, 2097152, s5
buffer_load_dwordx4 v8, s[44:47], s71 offen offset:2176 nt lds
v_add_u32 v11, s67, v11
M v[152:155],a[160:163],a[192:195],v[152:155]
s_add_u32 s6, s77, s5
M v[152:155],a[164:167],a[212:215],v[152:155]
s_waitcnt lgkmcnt(6)
M v[28:31],a[168:171],a[176:179],v[28:31]
s_waitcnt lgkmcnt(4)
T a[152:153],v6,1032
M v[28:31],a[172:175],a[196:199],v[28:31]
M v[60:63],a[172:175],a[200:203],v[60:63]
T a[154:155],v6,1288
M v[60:63],a[168:171],a[180:183],v[60:63]
M v[92:95],a[168:171],a[184:187],v[92:95]
T a[156:157],v6,9480
M v[92:95],a[172:175],a[204:207],v[92:95]
M v[124:127],a[172:175],a[208:211],v[124:127]
T a[158:159],v6,9736
M v[124:127],a[168:171],a[188:191],v[124:127]
M v[156:159],a[168:171],a[192:195],v[156:159]
M v[156:159],a[172:175],a[212:215],v[156:159]
s_waitcnt lgkmcnt(6)
M v[32:35],a[144:147],a[176:179],v[32:35]
s_waitcnt lgkmcnt(4)
T a[160:161],v6,1536
M v[32:35],a[148:151],a[196:199],v[32:35]
M v[64:67],a[148:151],a[200:203],v[64:67]
T a[162:163],v6,1792
M v[64:67],a[144:147],a[180:183],v[64:67]
M v[96:99],a[144:147],a[184:187],v[96:99]
T a[164:165],v6,9984
M v[96:99],a[148:151],a[204:207],v[96:99]
M v[128:131],a[148:151],a[208:211],v[128:131]
T a[166:167],v6,10240
M v[128:131],a[144:147],a[188:191],v[128:131]
M v[160:163],a[144:147],a[192:195],v[160:163]
M v[160:163],a[148:151],a[212:215],v[160:163]
s_waitcnt lgkmcnt(6)
M v[36:39],a[152:155],a[176:179],v[36:39]
s_waitcnt lgkmcnt(4)
T a[168:169],v6,1544
M v[36:39],a[156:159],a[196:199],v[36:39]
M v[68:71],a[156:159],a[200:203],v[68:71]
T a[170:171],v6,1800
M v[68:71],a[152:155],a[180:183],v[68:71]
M v[100:103],a[152:155],a[184:187],v[100:103]
T a[172:173],v6,9992
M v[100:103],a[156:159],a[204:207],v[100:103]
M v[132:135],a[156:159],a[208:211],v[132:135]
T a[174:175],v6,10248
M v[132:135],a[152:155],a[188:191],v[132:135]
s_mov_b32 s64, s65
M v[164:167],a[152:155],a[192:195],v[164:167]
s_cmp_eq_u32 s65, 67584
s_cselect_b32 s66, s90, s76
M v[164:167],a[156:159],a[212:215],v[164:167]
s_waitcnt lgkmcnt(6)
s_add_u32 s65, s65, s66
M v[40:43],a[160:163],a[176:179],v[40:43]
s_waitcnt lgkmcnt(4)
M v[40:43],a[164:167],a[196:199],v[40:43]
s_waitcnt vmcnt(8) lgkmcnt(0)
v_add_u32 v6, s66, v6
M v[72:75],a[164:167],a[200:203],v[72:75]
s_barrier
M v[72:75],a[160:163],a[180:183],v[72:75]
s_add_u32 s70, s73, 32768
T a[144:145],v6,0
M v[104:107],a[160:163],a[184:187],v[104:107]
s_mov_b32 m0, s70
T a[146:147],v6,256
M v[104:107],a[164:167],a[204:207],v[104:107]
T a[148:149],v6,8448
buffer_load_dwordx4 v8, s[44:47], s6 offen nt lds
M v[136:139],a[164:167],a[208:211],v[136:139]
T a[150:151],v6,8704
M v[136:139],a[160:163],a[188:191],v[136:139]
T a[152:153],v6,8
M v[168:171],a[160:163],a[192:195],v[168:171]
T a[154:155],v6,264
M v[168:171],a[164:167],a[212:215],v[168:171]
M v[44:47],a[168:171],a[176:179],v[44:47]
D a[176:179],v7,0
M v[44:47],a[172:175],a[196:199],v[44:47]
s_add_u32 s77, s77, 8192
D a[196:199],v11,0
buffer_load_dwordx4 v8, s[44:47], s6 offen offset:2048 nt lds
M v[76:79],a[172:175],a[200:203],v[76:79]
D a[200:203],v11,2048
M v[76:79],a[168:171],a[180:183],v[76:79]
D a[180:183],v7,2048
M v[108:111],a[168:171],a[184:187],v[108:111]
D a[184:187],v7,4096
M v[108:111],a[172:175],a[204:207],v[108:111]
D a[204:207],v11,4096
M v[140:143],a[172:175],a[208:211],v[140:143]
s_add_u32 m0, s70, 896
D a[208:211],v11,6144
M v[140:143],a[168:171],a[188:191],v[140:143]
D a[188:191],v7,6144
buffer_load_dwordx4 v8, s[44:47], s6 offen offset:128 nt lds
M v[172:175],a[168:171],a[192:195],v[172:175]
D a[192:195],v7,8192
M v[172:175],a[172:175],a[212:215],v[172:175]
D a[212:215],v11,8192
s_waitcnt lgkmcnt(9)
M v[16:19],a[144:147],a[176:179],v[16:19]
s_waitcnt lgkmcnt(8)
T a[160:161],v6,512
M v[16:19],a[148:151],a[196:199],v[16:19]
s_waitcnt lgkmcnt(8)
T a[156:157],v6,8456
M v[48:51],a[148:151],a[200:203],v[48:51]
s_waitcnt lgkmcnt(8)
T a[162:163],v6,768
M v[48:51],a[144:147],a[180:183],v[48:51]
s_waitcnt lgkmcnt(8)
s_add_u32 s71, s6, 4096
T a[158:159],v6,8712
buffer_load_dwordx4 v8, s[44:47], s6 offen offset:2176 nt lds
M v[80:83],a[144:147],a[184:187],v[80:83]
s_waitcnt lgkmcnt(8)
T a[164:165],v6,8960
M v[80:83],a[148:151],a[204:207],v[80:83]
s_waitcnt lgkmcnt(8)
M v[112:115],a[148:151],a[208:211],v[112:115]
s_waitcnt lgkmcnt(7)
T a[166:167],v6,9216
M v[112:115],a[144:147],a[188:191],v[112:115]
s_waitcnt lgkmcnt(7)
M v[144:147],a[144:147],a[192:195],v[144:147]
s_waitcnt lgkmcnt(6)
s_add_u32 m0, s70, 4096
M v[144:147],a[148:151],a[212:215],v[144:147]
buffer_load_dwordx4 v8, s[44:47], s71 offen nt lds
M v[20:23],a[152:155],a[176:179],v[20:23]
s_waitcnt lgkmcnt(2)
T a[168:169],v6,520
M v[20:23],a[156:159],a[196:199],v[20:23]
M v[52:55],a[156:159],a[200:203],v[52:55]
T a[170:171],v6,776
M v[52:55],a[152:155],a[180:183],v[52:55]
M v[84:87],a[152:155],a[184:187],v[84:87]
T a[172:173],v6,8968
M v[84:87],a[156:159],a[204:207],v[84:87]
s_and_b32 s77, s77, 131071
buffer_load_dwordx4 v8, s[44:47], s71 offen offset:2048 nt lds
M v[116:119],a[156:159],a[208:211],v[116:119]
T a[174:175],v6,9224
M v[116:119],a[152:155],a[188:191],v[116:119]
M v[148:151],a[152:155],a[192:195],v[148:151]
M v[148:151],a[156:159],a[212:215],v[148:151]
M v[24:27],a[160:163],a[176:179],v[24:27]
s_waitcnt lgkmcnt(4)
s_add_u32 m0, s70, 4992
T a[144:145],v6,1024
M v[24:27],a[164:167],a[196:199],v[24:27]
buffer_load_dwordx4 v8, s[44:47], s71 offen offset:128 nt lds
M v[56:59],a[164:167],a[200:203],v[56:59]
T a[146:147],v6,1280
M v[56:59],a[160:163],a[180:183],v[56:59]
M v[88:91],a[160:163],a[184:187],v[88:91]
T a[148:149],v6,9472
M v[88:91],a[164:167],a[204:207],v[88:91]
M v[120:123],a[164:167],a[208:211],v[120:123]
T a[150:151],v6,9728
M v[120:123],a[160:163],a[188:191],v[120:123]
s_cmp_eq_u32 s77, s4
s_cselect_b32 s5, 2097152, s5
buffer_load_dwordx4 v8, s[44:47], s71 offen offset:2176 nt lds
M v[152:155],a[160:163],a[192:195],v[152:155]
s_add_u32 s6, s77, s5
M v[152:155],a[164:167],a[212:215],v[152:155]
s_waitcnt lgkmcnt(6)
M v[28:31],a[168:171],a[176:179],v[28:31]
s_waitcnt lgkmcnt(4)
T a[152:153],v6,1032
M v[28:31],a[172:175],a[196:199],v[28:31]
M v[60:63],a[172:175],a[200:203],v[60:63]
T a[154:155],v6,1288
M v[60:63],a[168:171],a[180:183],v[60:63]
M v[92:95],a[168:171],a[184:187],v[92:95]
T a[156:157],v6,9480
M v[92:95],a[172:175],a[204:207],v[92:95]
M v[124:127],a[172:175],a[208:211],v[124:127]
T a[158:159],v6,9736
M v[124:127],a[168:171],a[188:191],v[124:127]
M v[156:159],a[168:171],a[192:195],v[156:159]
M v[156:159],a[172:175],a[212:215],v[156:159]
s_waitcnt lgkmcnt(6)
M v[32:35],a[144:147],a[176:179],v[32:35]
s_waitcnt lgkmcnt(4)
T a[160:161],v6,1536
M v[32:35],a[148:151],a[196:199],v[32:35]
M v[64:67],a[148:151],a[200:203],v[64:67]
T a[162:163],v6,1792
M v[64:67],a[144:147],a[180:183],v[64:67]
M v[96:99],a[144:147],a[184:187],v[96:99]
T a[164:165],v6,9984
M v[96:99],a[148:151],a[204:207],v[96:99]
M v[128:131],a[148:151],a[208:211],v[128:131]
T a[166:167],v6,10240
M v[128:131],a[144:147],a[188:191],v[128:131]
M v[160:163],a[144:147],a[192:195],v[160:163]
M v[160:163],a[148:151],a[212:215],v[160:163]
s_waitcnt lgkmcnt(6)
M v[36:39],a[152:155],a[176:179],v[36:39]
s_waitcnt lgkmcnt(4)
T a[168:169],v6,1544
M v[36:39],a[156:159],a[196:199],v[36:39]
M v[68:71],a[156:159],a[200:203],v[68:71]
T a[170:171],v6,1800
M v[68:71],a[152:155],a[180:183],v[68:71]
M v[100:103],a[152:155],a[184:187],v[100:103]
T a[172:173],v6,9992
M v[100:103],a[156:159],a[204:207],v[100:103]
M v[132:135],a[156:159],a[208:211],v[132:135]
T a[174:175],v6,10248
M v[132:135],a[152:155],a[188:191],v[132:135]
M v[164:167],a[152:155],a[192:195],v[164:167]
M v[164:167],a[156:159],a[212:215],v[164:167]
s_waitcnt lgkmcnt(6)
M v[40:43],a[160:163],a[176:179],v[40:43]
s_waitcnt lgkmcnt(4)
M v[40:43],a[164:167],a[196:199],v[40:43]
s_waitcnt vmcnt(8) lgkmcnt(0)
M v[72:75],a[164:167],a[200:203],v[72:75]
s_barrier
M v[72:75],a[160:163],a[180:183],v[72:75]
s_add_u32 s70, s73, 65536
M v[104:107],a[160:163],a[184:187],v[104:107]
s_mov_b32 m0, s70
M v[104:107],a[164:167],a[204:207],v[104:107]
buffer_load_dwordx4 v8, s[44:47], s6 offen nt lds
M v[136:139],a[164:167],a[208:211],v[136:139]
M v[136:139],a[160:163],a[188:191],v[136:139]
M v[168:171],a[160:163],a[192:195],v[168:171]
M v[168:171],a[164:167],a[212:215],v[168:171]
s_add_u32 s77, s77, 8192
buffer_load_dwordx4 v8, s[44:47], s6 offen offset:2048 nt lds
M v[44:47],a[168:171],a[176:179],v[44:47]
M v[44:47],a[172:175],a[196:199],v[44:47]
M v[76:79],a[172:175],a[200:203],v[76:79]
s_add_u32 m0, s70, 896
M v[76:79],a[168:171],a[180:183],v[76:79]
buffer_load_dwordx4 v8, s[44:47], s6 offen offset:128 nt lds
M v[108:111],a[168:171],a[184:187],v[108:111]
M v[108:111],a[172:175],a[204:207],v[108:111]
M v[140:143],a[172:175],a[208:211],v[140:143]
M v[140:143],a[168:171],a[188:191],v[140:143]
s_add_u32 s71, s6, 4096
buffer_load_dwordx4 v8, s[44:47], s6 offen offset:2176 nt lds
M v[172:175],a[168:171],a[192:195],v[172:175]
M v[172:175],a[172:175],a[212:215],v[172:175]
D a[160:163],v2,0
D a[164:167],v3,0
D a[168:171],v2,1024
D a[172:175],v3,1024
D a[176:179],v2,4096
D a[180:183],v3,4096
D a[184:187],v2,5120
D a[188:191],v3,5120
s_nop 3
v_pk_mul_f32 v[16:17], v[16:17], s[92:93] op_sel_hi:[1,0]
v_pk_mul_f32 v[18:19], v[18:19], s[92:93] op_sel_hi:[1,0]
C v16,v16,v17
s_add_u32 m0, s70, 4096
s_nop 0
buffer_load_dwordx4 v8, s[44:47], s71 offen nt lds
C v17,v18,v19
v_pk_mul_f32 v[20:21], v[20:21], s[92:93] op_sel_hi:[1,0]
v_pk_mul_f32 v[22:23], v[22:23], s[92:93] op_sel_hi:[1,0]
C v18,v20,v21
C v19,v22,v23
v_pk_mul_f32 v[24:25], v[24:25], s[92:93] op_sel_hi:[1,0]
v_pk_mul_f32 v[26:27], v[26:27], s[92:93] op_sel_hi:[1,0]
C v20,v24,v25
s_and_b32 s77, s77, 131071
buffer_load_dwordx4 v8, s[44:47], s71 offen offset:2048 nt lds
C v21,v26,v27
v_pk_mul_f32 v[28:29], v[28:29], s[92:93] op_sel_hi:[1,0]
v_pk_mul_f32 v[30:31], v[30:31], s[92:93] op_sel_hi:[1,0]
C v22,v28,v29
C v23,v30,v31
v_pk_mul_f32 v[32:33], v[32:33], s[92:93] op_sel_hi:[1,0]
v_pk_mul_f32 v[34:35], v[34:35], s[92:93] op_sel_hi:[1,0]
C v24,v32,v33
s_add_u32 m0, s70, 4992
s_nop 0
buffer_load_dwordx4 v8, s[44:47], s71 offen offset:128 nt lds
C v25,v34,v35
v_pk_mul_f32 v[36:37], v[36:37], s[92:93] op_sel_hi:[1,0]
v_pk_mul_f32 v[38:39], v[38:39], s[92:93] op_sel_hi:[1,0]
C v26,v36,v37
C v27,v38,v39
v_pk_mul_f32 v[40:41], v[40:41], s[92:93] op_sel_hi:[1,0]
v_pk_mul_f32 v[42:43], v[42:43], s[92:93] op_sel_hi:[1,0]
C v28,v40,v41
s_cmp_eq_u32 s77, s4
s_cselect_b32 s5, 2097152, s5
buffer_load_dwordx4 v8, s[44:47], s71 offen offset:2176 nt lds
C v29,v42,v43
v_pk_mul_f32 v[44:45], v[44:45], s[92:93] op_sel_hi:[1,0]
v_pk_mul_f32 v[46:47], v[46:47], s[92:93] op_sel_hi:[1,0]
C v30,v44,v45
C v31,v46,v47
v_pk_mul_f32 v[48:49], v[48:49], s[92:93] op_sel_hi:[1,0]
v_pk_mul_f32 v[50:51], v[50:51], s[92:93] op_sel_hi:[1,0]
C v48,v48,v49
s_add_u32 s6, s77, s5
C v49,v50,v51
v_pk_mul_f32 v[52:53], v[52:53], s[92:93] op_sel_hi:[1,0]
v_pk_mul_f32 v[54:55], v[54:55], s[92:93] op_sel_hi:[1,0]
C v50,v52,v53
C v51,v54,v55
v_pk_mul_f32 v[56:57], v[56:57], s[92:93] op_sel_hi:[1,0]
v_pk_mul_f32 v[58:59], v[58:59], s[92:93] op_sel_hi:[1,0]
C v52,v56,v57
C v53,v58,v59
v_pk_mul_f32 v[60:61], v[60:61], s[92:93] op_sel_hi:[1,0]
v_pk_mul_f32 v[62:63], v[62:63], s[92:93] op_sel_hi:[1,0]
C v54,v60,v61
C v55,v62,v63
v_pk_mul_f32 v[64:65], v[64:65], s[92:93] op_sel_hi:[1,0]
v_pk_mul_f32 v[66:67], v[66:67], s[92:93] op_sel_hi:[1,0]
C v56,v64,v65
C v57,v66,v67
v_pk_mul_f32 v[68:69], v[68:69], s[92:93] op_sel_hi:[1,0]
v_pk_mul_f32 v[70:71], v[70:71], s[92:93] op_sel_hi:[1,0]
C v58,v68,v69
C v59,v70,v71
v_pk_mul_f32 v[72:73], v[72:73], s[92:93] op_sel_hi:[1,0]
v_pk_mul_f32 v[74:75], v[74:75], s[92:93] op_sel_hi:[1,0]
C v60,v72,v73
C v61,v74,v75
v_pk_mul_f32 v[76:77], v[76:77], s[92:93] op_sel_hi:[1,0]
v_pk_mul_f32 v[78:79], v[78:79], s[92:93] op_sel_hi:[1,0]
C v62,v76,v77
C v63,v78,v79
v_pk_mul_f32 v[80:81], v[80:81], s[92:93] op_sel_hi:[1,0]
v_pk_mul_f32 v[82:83], v[82:83], s[92:93] op_sel_hi:[1,0]
C v80,v80,v81
C v81,v82,v83
v_pk_mul_f32 v[84:85], v[84:85], s[92:93] op_sel_hi:[1,0]
v_pk_mul_f32 v[86:87], v[86:87], s[92:93] op_sel_hi:[1,0]
C v82,v84,v85
C v83,v86,v87
v_pk_mul_f32 v[88:89], v[88:89], s[92:93] op_sel_hi:[1,0]
v_pk_mul_f32 v[90:91], v[90:91], s[92:93] op_sel_hi:[1,0]
C v84,v88,v89
C v85,v90,v91
v_pk_mul_f32 v[92:93], v[92:93], s[92:93] op_sel_hi:[1,0]
v_pk_mul_f32 v[94:95], v[94:95], s[92:93] op_sel_hi:[1,0]
C v86,v92,v93
C v87,v94,v95
v_pk_mul_f32 v[96:97], v[96:97], s[92:93] op_sel_hi:[1,0]
v_pk_mul_f32 v[98:99], v[98:99], s[92:93] op_sel_hi:[1,0]
C v88,v96,v97
C v89,v98,v99
v_pk_mul_f32 v[100:101], v[100:101], s[92:93] op_sel_hi:[1,0]
v_pk_mul_f32 v[102:103], v[102:103], s[92:93] op_sel_hi:[1,0]
C v90,v100,v101
C v91,v102,v103
v_pk_mul_f32 v[104:105], v[104:105], s[92:93] op_sel_hi:[1,0]
v_pk_mul_f32 v[106:107], v[106:107], s[92:93] op_sel_hi:[1,0]
C v92,v104,v105
C v93,v106,v107
v_pk_mul_f32 v[108:109], v[108:109], s[92:93] op_sel_hi:[1,0]
v_pk_mul_f32 v[110:111], v[110:111], s[92:93] op_sel_hi:[1,0]
C v94,v108,v109
C v95,v110,v111
v_pk_mul_f32 v[112:113], v[112:113], s[92:93] op_sel_hi:[1,0]
v_pk_mul_f32 v[114:115], v[114:115], s[92:93] op_sel_hi:[1,0]
C v112,v112,v113
C v113,v114,v115
v_pk_mul_f32 v[116:117], v[116:117], s[92:93] op_sel_hi:[1,0]
v_pk_mul_f32 v[118:119], v[118:119], s[92:93] op_sel_hi:[1,0]
C v114,v116,v117
C v115,v118,v119
v_pk_mul_f32 v[120:121], v[120:121], s[92:93] op_sel_hi:[1,0]
v_pk_mul_f32 v[122:123], v[122:123], s[92:93] op_sel_hi:[1,0]
C v116,v120,v121
C v117,v122,v123
v_pk_mul_f32 v[124:125], v[124:125], s[92:93] op_sel_hi:[1,0]
v_pk_mul_f32 v[126:127], v[126:127], s[92:93] op_sel_hi:[1,0]
C v118,v124,v125
C v119,v126,v127
v_pk_mul_f32 v[128:129], v[128:129], s[92:93] op_sel_hi:[1,0]
v_pk_mul_f32 v[130:131], v[130:131], s[92:93] op_sel_hi:[1,0]
C v120,v128,v129
C v121,v130,v131
v_pk_mul_f32 v[132:133], v[132:133], s[92:93] op_sel_hi:[1,0]
v_pk_mul_f32 v[134:135], v[134:135], s[92:93] op_sel_hi:[1,0]
C v122,v132,v133
C v123,v134,v135
v_pk_mul_f32 v[136:137], v[136:137], s[92:93] op_sel_hi:[1,0]
v_pk_mul_f32 v[138:139], v[138:139], s[92:93] op_sel_hi:[1,0]
C v124,v136,v137
C v125,v138,v139
v_pk_mul_f32 v[140:141], v[140:141], s[92:93] op_sel_hi:[1,0]
)"
R"(v_pk_mul_f32 v[142:143], v[142:143], s[92:93] op_sel_hi:[1,0]
C v126,v140,v141
C v127,v142,v143
v_pk_mul_f32 v[144:145], v[144:145], s[92:93] op_sel_hi:[1,0]
v_pk_mul_f32 v[146:147], v[146:147], s[92:93] op_sel_hi:[1,0]
C v144,v144,v145
C v145,v146,v147
v_pk_mul_f32 v[148:149], v[148:149], s[92:93] op_sel_hi:[1,0]
v_pk_mul_f32 v[150:151], v[150:151], s[92:93] op_sel_hi:[1,0]
C v146,v148,v149
C v147,v150,v151
v_pk_mul_f32 v[152:153], v[152:153], s[92:93] op_sel_hi:[1,0]
v_pk_mul_f32 v[154:155], v[154:155], s[92:93] op_sel_hi:[1,0]
C v148,v152,v153
C v149,v154,v155
v_pk_mul_f32 v[156:157], v[156:157], s[92:93] op_sel_hi:[1,0]
v_pk_mul_f32 v[158:159], v[158:159], s[92:93] op_sel_hi:[1,0]
C v150,v156,v157
C v151,v158,v159
v_pk_mul_f32 v[160:161], v[160:161], s[92:93] op_sel_hi:[1,0]
v_pk_mul_f32 v[162:163], v[162:163], s[92:93] op_sel_hi:[1,0]
C v152,v160,v161
C v153,v162,v163
v_pk_mul_f32 v[164:165], v[164:165], s[92:93] op_sel_hi:[1,0]
v_pk_mul_f32 v[166:167], v[166:167], s[92:93] op_sel_hi:[1,0]
C v154,v164,v165
C v155,v166,v167
v_pk_mul_f32 v[168:169], v[168:169], s[92:93] op_sel_hi:[1,0]
v_pk_mul_f32 v[170:171], v[170:171], s[92:93] op_sel_hi:[1,0]
C v156,v168,v169
C v157,v170,v171
v_pk_mul_f32 v[172:173], v[172:173], s[92:93] op_sel_hi:[1,0]
v_pk_mul_f32 v[174:175], v[174:175], s[92:93] op_sel_hi:[1,0]
C v158,v172,v173
C v159,v174,v175
s_mov_b32 s65, 0
s_mov_b32 s76, 32768
s_mov_b32 s90, -131072
L_ph_at0Y:
s_memrealtime s[18:19]
s_waitcnt lgkmcnt(0)
s_add_u32 s3, s18, 44
M v[36:39],a[160:163],v[16:19],0
T a[192:193],v4,0
M v[36:39],a[164:167],v[20:23],v[36:39]
T a[194:195],v4,4096
M v[36:39],a[168:171],v[24:27],v[36:39]
T a[196:197],v5,0
M v[36:39],a[172:175],v[28:31],v[36:39]
T a[198:199],v5,4096
M v[40:43],a[176:179],v[16:19],0
T a[200:201],v4,512
M v[40:43],a[180:183],v[20:23],v[40:43]
T a[202:203],v4,4608
M v[40:43],a[184:187],v[24:27],v[40:43]
T a[204:205],v5,512
M v[40:43],a[188:191],v[28:31],v[40:43]
T a[206:207],v5,4608
v_max3_f32 v32, v36, v37, v38
T a[208:209],v4,1024
T a[210:211],v4,5120
s_nop 3
v_max3_f32 v33, v40, v41, v42
v_max3_f32 v32, v32, v39, v43
v_max_f32 v32, v32, v33
v_mov_b32 v33, v32
s_nop 1
v_permlane32_swap_b32 v32, v33
v_max_f32 v32, v32, v33
v_mov_b32 v33, v32
s_nop 1
v_permlane16_swap_b32 v32, v33
M v[68:71],a[160:163],v[48:51],0
v_max_f32 v32, v32, v33
v_sub_f32 v33, 0, v32
M v[68:71],a[164:167],v[52:55],v[68:71]
v_add_f32 v36, v33, v36
v_add_f32 v37, v33, v37
M v[68:71],a[168:171],v[56:59],v[68:71]
v_add_f32 v38, v33, v38
v_add_f32 v39, v33, v39
M v[68:71],a[172:175],v[60:63],v[68:71]
E v36,v36
M v[72:75],a[176:179],v[48:51],0
v_add_f32 v40, v33, v40
v_add_f32 v41, v33, v41
M v[72:75],a[180:183],v[52:55],v[72:75]
E v37,v37
M v[72:75],a[184:187],v[56:59],v[72:75]
E v38,v38
M v[72:75],a[188:191],v[60:63],v[72:75]
v_add_f32 v42, v33, v42
v_add_f32 v43, v33, v43
T a[212:213],v5,1024
E v39,v39
v_max3_f32 v64, v68, v69, v70
E v40,v40
v_mov_b32 v32, v33
E v43,v43
v_max3_f32 v65, v72, v73, v74
v_max3_f32 v64, v64, v71, v75
v_max_f32 v64, v64, v65
E v41,v41
E v42,v42
v_mov_b32 v65, v64
s_nop 1
v_permlane32_swap_b32 v64, v65
v_mov_b32 v34, v33
v_mov_b32 v35, v33
C v44,v36,v37
C v45,v38,v39
v_max_f32 v64, v64, v65
v_mov_b32 v65, v64
M v[36:39],a[160:163],v[80:83],0
s_nop 0
v_permlane16_swap_b32 v64, v65
M v[36:39],a[164:167],v[84:87],v[36:39]
v_max_f32 v64, v64, v65
v_sub_f32 v65, 0, v64
M v[36:39],a[168:171],v[88:91],v[36:39]
C v46,v40,v41
C v47,v42,v43
M v[36:39],a[172:175],v[92:95],v[36:39]
T a[222:223],v5,5632
v_add_f32 v68, v65, v68
v_add_f32 v69, v65, v69
M v[40:43],a[176:179],v[80:83],0
T a[214:215],v5,5120
v_add_f32 v70, v65, v70
M v[40:43],a[180:183],v[84:87],v[40:43]
T a[216:217],v4,1536
v_add_f32 v71, v65, v71
M v[40:43],a[184:187],v[88:91],v[40:43]
T a[218:219],v4,5632
v_add_f32 v72, v65, v72
M v[40:43],a[188:191],v[92:95],v[40:43]
s_waitcnt lgkmcnt(13)
T a[220:221],v5,1536
E v68,v68
M a[0:3],a[192:195],v[44:47],0
s_waitcnt lgkmcnt(12)
E v69,v69
M a[4:7],a[196:199],v[44:47],0
s_waitcnt lgkmcnt(10)
v_add_f32 v73, v65, v73
E v70,v70
M a[8:11],a[200:203],v[44:47],0
s_waitcnt lgkmcnt(8)
v_add_f32 v74, v65, v74
E v71,v71
M a[12:15],a[204:207],v[44:47],0
v_add_f32 v75, v65, v75
E v72,v72
v_max3_f32 v96, v36, v37, v38
v_max3_f32 v97, v40, v41, v42
M a[228:231],a[224:227],v[44:47],0
s_waitcnt lgkmcnt(6)
M a[16:19],a[208:211],v[44:47],0
s_waitcnt lgkmcnt(3)
v_mov_b32 v66, v65
E v73,v73
E v75,v75
v_max3_f32 v96, v96, v39, v43
v_max_f32 v96, v96, v97
M a[20:23],a[212:215],v[44:47],0
s_waitcnt lgkmcnt(1)
E v74,v74
v_mov_b32 v97, v96
M a[24:27],a[216:219],v[44:47],0
s_waitcnt lgkmcnt(0)
v_mov_b32 v64, v65
v_permlane32_swap_b32 v96, v97
M a[28:31],a[220:223],v[44:47],0
s_memrealtime s[18:19]
v_mov_b32 v67, v65
C v76,v68,v69
C v77,v70,v71
v_max_f32 v96, v96, v97
v_mov_b32 v97, v96
M v[68:71],a[160:163],v[112:115],0
s_nop 0
v_permlane16_swap_b32 v96, v97
M v[68:71],a[164:167],v[116:119],v[68:71]
v_max_f32 v96, v96, v97
v_sub_f32 v97, 0, v96
M v[68:71],a[168:171],v[120:123],v[68:71]
s_mov_b32 s64, s65
C v78,v72,v73
C v79,v74,v75
M v[68:71],a[172:175],v[124:127],v[68:71]
s_cmp_eq_u32 s65, 131072
s_cselect_b32 s66, s90, s76
v_add_f32 v36, v97, v36
M v[72:75],a[176:179],v[112:115],0
s_add_u32 s65, s65, s66
v_add_f32 v37, v97, v37
v_add_f32 v38, v97, v38
v_mov_b32 v96, v97
M v[72:75],a[180:183],v[116:119],v[72:75]
v_add_f32 v39, v97, v39
v_add_f32 v40, v97, v40
M v[72:75],a[184:187],v[120:123],v[72:75]
s_waitcnt lgkmcnt(0)
E v36,v36
M v[72:75],a[188:191],v[124:127],v[72:75]
s_sub_u32 s19, s3, s18
E v37,v37
M a[32:35],a[192:195],v[76:79],0
s_cmp_gt_i32 s19, 14
s_cbranch_scc1 L_pc0Y0
L_pe0Y0:
v_add_f32 v41, v97, v41
v_add_f32 v42, v97, v42
v_mov_b32 v99, v97
C v44,v36,v37
M a[36:39],a[196:199],v[76:79],0
s_add_u32 s3, s18, 110
E v38,v38
M a[40:43],a[200:203],v[76:79],0
s_waitcnt vmcnt(8) lgkmcnt(0)
E v39,v39
M a[44:47],a[204:207],v[76:79],0
s_barrier
v_add_f32 v43, v97, v43
E v40,v40
M a[232:235],a[224:227],v[76:79],0
M a[48:51],a[208:211],v[76:79],0
E v41,v41
M a[52:55],a[212:215],v[76:79],0
E v42,v42
M a[56:59],a[216:219],v[76:79],0
E v43,v43
M a[60:63],a[220:223],v[76:79],0
v_mov_b32 v98, v97
C v45,v38,v39
C v46,v40,v41
C v47,v42,v43
v_add_u32 v2, s66, v2
v_add_u32 v3, s66, v3
M v[100:103],a[160:163],v[144:147],0
v_max3_f32 v128, v68, v69, v70
v_max3_f32 v129, v72, v73, v74
v_max3_f32 v128, v128, v71, v75
M v[100:103],a[164:167],v[148:151],v[100:103]
v_max_f32 v128, v128, v129
v_mov_b32 v129, v128
M v[100:103],a[168:171],v[152:155],v[100:103]
s_nop 0
v_permlane32_swap_b32 v128, v129
M v[100:103],a[172:175],v[156:159],v[100:103]
v_max_f32 v128, v128, v129
v_mov_b32 v129, v128
M v[104:107],a[176:179],v[144:147],0
D a[160:163],v2,0
v_permlane16_swap_b32 v128, v129
M v[104:107],a[180:183],v[148:151],v[104:107]
D a[176:179],v2,4096
v_max_f32 v128, v128, v129
v_sub_f32 v129, 0, v128
v_add_f32 v68, v129, v68
v_add_f32 v69, v129, v69
v_add_f32 v70, v129, v70
E v68,v68
M v[104:107],a[184:187],v[152:155],v[104:107]
D a[172:175],v3,1024
v_add_f32 v72, v129, v72
v_add_f32 v73, v129, v73
E v69,v69
M v[104:107],a[188:191],v[156:159],v[104:107]
D a[184:187],v2,5120
v_add_f32 v71, v129, v71
v_add_f32 v74, v129, v74
v_mov_b32 v130, v129
v_add_u32 v5, s66, v5
M a[64:67],a[192:195],v[44:47],0
E v73,v73
E v74,v74
M a[68:71],a[196:199],v[44:47],0
D a[164:167],v3,0
E v70,v70
E v71,v71
E v72,v72
v_max3_f32 v160, v100, v101, v102
M a[72:75],a[200:203],v[44:47],0
D a[168:171],v2,1024
C v76,v68,v69
M a[76:79],a[204:207],v[44:47],0
v_add_f32 v75, v129, v75
E v75,v75
v_max3_f32 v161, v104, v105, v106
v_max3_f32 v160, v160, v103, v107
v_max_f32 v160, v160, v161
v_mov_b32 v161, v160
M a[236:239],a[224:227],v[44:47],0
M a[80:83],a[208:211],v[44:47],0
v_mov_b32 v131, v129
v_permlane32_swap_b32 v160, v161
M a[84:87],a[212:215],v[44:47],0
D a[180:183],v3,4096
C v77,v70,v71
v_max_f32 v160, v160, v161
v_mov_b32 v161, v160
M a[88:91],a[216:219],v[44:47],0
s_nop 0
v_permlane16_swap_b32 v160, v161
M a[92:95],a[220:223],v[44:47],0
s_waitcnt lgkmcnt(6)
D a[188:191],v3,5120
v_mov_b32 v128, v129
v_max_f32 v160, v160, v161
v_sub_f32 v161, 0, v160
M v[36:39],a[160:163],v[16:19],v[32:35]
s_waitcnt lgkmcnt(3)
v_add_f32 v100, v161, v100
v_add_f32 v105, v161, v105
M v[36:39],a[164:167],v[20:23],v[36:39]
s_waitcnt lgkmcnt(2)
C v79,v74,v75
v_add_f32 v102, v161, v102
v_add_f32 v104, v161, v104
E v100,v100
M v[36:39],a[168:171],v[24:27],v[36:39]
v_add_f32 v101, v161, v101
E v101,v101
M v[36:39],a[172:175],v[28:31],v[36:39]
s_add_u32 s64, s65, -98304
C v78,v72,v73
M v[40:43],a[176:179],v[16:19],v[32:35]
s_waitcnt lgkmcnt(1)
s_cmp_lt_i32 s64, 0
s_cselect_b32 s67, 163840, 0
E v102,v102
M v[40:43],a[180:183],v[20:23],v[40:43]
s_add_u32 s64, s64, s67
v_add_f32 v107, v161, v107
M v[40:43],a[184:187],v[24:27],v[40:43]
s_waitcnt lgkmcnt(0)
s_add_u32 s70, s73, s64
v_add_f32 v103, v161, v103
M v[40:43],a[188:191],v[28:31],v[40:43]
s_mov_b32 m0, s70
E v104,v104
M a[96:99],a[192:195],v[76:79],0
buffer_load_dwordx4 v8, s[44:47], s6 offen nt lds
v_mov_b32 v160, v161
M a[100:103],a[196:199],v[76:79],0
v_add_f32 v106, v161, v106
v_mov_b32 v163, v161
M a[104:107],a[200:203],v[76:79],0
E v103,v103
M a[108:111],a[204:207],v[76:79],0
E v105,v105
M a[240:243],a[224:227],v[76:79],0
M a[112:115],a[208:211],v[76:79],0
s_add_u32 s77, s77, 8192
buffer_load_dwordx4 v8, s[44:47], s6 offen offset:2048 nt lds
E v106,v106
M a[116:119],a[212:215],v[76:79],0
E v107,v107
M a[120:123],a[216:219],v[76:79],0
v_mov_b32 v162, v161
C v108,v100,v101
M a[124:127],a[220:223],v[76:79],0
s_add_u32 m0, s70, 896
C v109,v102,v103
C v111,v106,v107
M v[68:71],a[160:163],v[48:51],v[64:67]
buffer_load_dwordx4 v8, s[44:47], s6 offen offset:128 nt lds
C v110,v104,v105
M v[68:71],a[164:167],v[52:55],v[68:71]
E v42,v42
M v[68:71],a[168:171],v[56:59],v[68:71]
E v40,v40
M v[68:71],a[172:175],v[60:63],v[68:71]
E v38,v38
M v[72:75],a[176:179],v[48:51],v[64:67]
s_add_u32 s71, s6, 4096
buffer_load_dwordx4 v8, s[44:47], s6 offen offset:2176 nt lds
v_add_u32 v4, s66, v4
M v[72:75],a[180:183],v[52:55],v[72:75]
E v39,v39
M v[72:75],a[184:187],v[56:59],v[72:75]
E v43,v43
M v[72:75],a[188:191],v[60:63],v[72:75]
s_add_u32 m0, s70, 4096
E v41,v41
M a[128:131],a[192:195],v[108:111],0
buffer_load_dwordx4 v8, s[44:47], s71 offen nt lds
T a[192:193],v4,0
M a[132:135],a[196:199],v[108:111],0
T a[198:199],v5,4096
C v45,v38,v39
M a[136:139],a[200:203],v[108:111],0
E v36,v36
M a[140:143],a[204:207],v[108:111],0
T a[196:197],v5,0
C v46,v40,v41
M a[244:247],a[224:227],v[108:111],0
M a[144:147],a[208:211],v[108:111],0
s_and_b32 s77, s77, 131071
buffer_load_dwordx4 v8, s[44:47], s71 offen offset:2048 nt lds
T a[208:209],v4,1024
M a[148:151],a[212:215],v[108:111],0
T a[194:195],v4,4096
E v37,v37
M a[152:155],a[216:219],v[108:111],0
T a[218:219],v4,5632
C v47,v42,v43
M a[156:159],a[220:223],v[108:111],0
s_add_u32 m0, s70, 4992
T a[204:205],v5,512
C v44,v36,v37
M v[36:39],a[160:163],v[80:83],v[96:99]
buffer_load_dwordx4 v8, s[44:47], s71 offen offset:128 nt lds
T a[220:221],v5,1536
M v[36:39],a[164:167],v[84:87],v[36:39]
T a[200:201],v4,512
T a[202:203],v4,4608
M v[36:39],a[168:171],v[88:91],v[36:39]
T a[210:211],v4,5120
M v[36:39],a[172:175],v[92:95],v[36:39]
T a[206:207],v5,4608
T a[214:215],v5,5120
M v[40:43],a[176:179],v[80:83],v[96:99]
s_cmp_eq_u32 s77, s4
s_cselect_b32 s5, 2097152, s5
buffer_load_dwordx4 v8, s[44:47], s71 offen offset:2176 nt lds
T a[222:223],v5,5632
M v[40:43],a[180:183],v[84:87],v[40:43]
s_add_u32 s6, s77, s5
T a[212:213],v5,1024
T a[216:217],v4,1536
M v[40:43],a[184:187],v[88:91],v[40:43]
E v69,v69
M v[40:43],a[188:191],v[92:95],v[40:43]
s_waitcnt lgkmcnt(11)
E v68,v68
M a[0:3],a[192:195],v[44:47],a[0:3]
E v70,v70
M a[4:7],a[196:199],v[44:47],a[4:7]
s_waitcnt lgkmcnt(6)
E v71,v71
M a[8:11],a[200:203],v[44:47],a[8:11]
s_waitcnt lgkmcnt(4)
E v74,v74
M a[12:15],a[204:207],v[44:47],a[12:15]
E v72,v72
M a[228:231],a[224:227],v[44:47],a[228:231]
M a[16:19],a[208:211],v[44:47],a[16:19]
s_waitcnt lgkmcnt(1)
E v75,v75
M a[20:23],a[212:215],v[44:47],a[20:23]
s_waitcnt lgkmcnt(0)
E v38,v38
M a[24:27],a[216:219],v[44:47],a[24:27]
E v73,v73
M a[28:31],a[220:223],v[44:47],a[28:31]
s_memrealtime s[18:19]
C v76,v68,v69
C v77,v70,v71
M v[68:71],a[160:163],v[112:115],v[128:131]
C v79,v74,v75
M v[68:71],a[164:167],v[116:119],v[68:71]
E v37,v37
M v[68:71],a[168:171],v[120:123],v[68:71]
s_mov_b32 s64, s65
M v[68:71],a[172:175],v[124:127],v[68:71]
s_cmp_eq_u32 s65, 131072
s_cselect_b32 s66, s90, s76
C v78,v72,v73
M v[72:75],a[176:179],v[112:115],v[128:131]
s_add_u32 s65, s65, s66
E v36,v36
M v[72:75],a[180:183],v[116:119],v[72:75]
E v41,v41
M v[72:75],a[184:187],v[120:123],v[72:75]
s_waitcnt lgkmcnt(0)
E v39,v39
M v[72:75],a[188:191],v[124:127],v[72:75]
s_sub_u32 s19, s3, s18
E v43,v43
M a[32:35],a[192:195],v[76:79],a[32:35]
s_cmp_gt_i32 s19, 14
s_cbranch_scc1 L_pc0Y1
L_pe0Y1:
v_add_u32 v2, s66, v2
M a[36:39],a[196:199],v[76:79],a[36:39]
s_add_u32 s3, s18, 110
E v42,v42
M a[40:43],a[200:203],v[76:79],a[40:43]
s_waitcnt vmcnt(8) lgkmcnt(0)
E v40,v40
M a[44:47],a[204:207],v[76:79],a[44:47]
s_barrier
M a[232:235],a[224:227],v[76:79],a[232:235]
M a[48:51],a[208:211],v[76:79],a[48:51]
C v47,v42,v43
M a[52:55],a[212:215],v[76:79],a[52:55]
C v44,v36,v37
C v46,v40,v41
)"
R"(M a[56:59],a[216:219],v[76:79],a[56:59]
v_add_u32 v3, s66, v3
M a[60:63],a[220:223],v[76:79],a[60:63]
C v45,v38,v39
s_mov_b32 s69, 12
.p2align 3
L_atY_4:
M v[100:103],a[160:163],v[144:147],v[160:163]
E v72,v72
M v[100:103],a[164:167],v[148:151],v[100:103]
E v71,v71
M v[100:103],a[168:171],v[152:155],v[100:103]
D a[168:171],v2,1024
E v69,v69
M v[100:103],a[172:175],v[156:159],v[100:103]
E v68,v68
M v[104:107],a[176:179],v[144:147],v[160:163]
D a[164:167],v3,0
M v[104:107],a[180:183],v[148:151],v[104:107]
E v70,v70
M v[104:107],a[184:187],v[152:155],v[104:107]
D a[172:175],v3,1024
M v[104:107],a[188:191],v[156:159],v[104:107]
D a[160:163],v2,0
C v76,v68,v69
M a[64:67],a[192:195],v[44:47],a[64:67]
E v73,v73
M a[68:71],a[196:199],v[44:47],a[68:71]
D a[176:179],v2,4096
E v75,v75
M a[72:75],a[200:203],v[44:47],a[72:75]
D a[184:187],v2,5120
C v77,v70,v71
M a[76:79],a[204:207],v[44:47],a[76:79]
E v106,v106
M a[236:239],a[224:227],v[44:47],a[236:239]
M a[80:83],a[208:211],v[44:47],a[80:83]
E v74,v74
M a[84:87],a[212:215],v[44:47],a[84:87]
D a[180:183],v3,4096
C v79,v74,v75
M a[88:91],a[216:219],v[44:47],a[88:91]
D a[188:191],v3,5120
M a[92:95],a[220:223],v[44:47],a[92:95]
s_waitcnt lgkmcnt(4)
E v101,v101
M v[36:39],a[160:163],v[16:19],v[32:35]
C v78,v72,v73
M v[36:39],a[164:167],v[20:23],v[36:39]
E v105,v105
M v[36:39],a[168:171],v[24:27],v[36:39]
E v104,v104
M v[36:39],a[172:175],v[28:31],v[36:39]
s_waitcnt lgkmcnt(3)
s_add_u32 s64, s65, -98304
M v[40:43],a[176:179],v[16:19],v[32:35]
s_waitcnt lgkmcnt(1)
s_cmp_lt_i32 s64, 0
s_cselect_b32 s67, 163840, 0
M v[40:43],a[180:183],v[20:23],v[40:43]
s_add_u32 s64, s64, s67
v_add_u32 v5, s66, v5
M v[40:43],a[184:187],v[24:27],v[40:43]
s_waitcnt lgkmcnt(0)
s_add_u32 s70, s73, s64
v_add_u32 v4, s66, v4
M v[40:43],a[188:191],v[28:31],v[40:43]
s_mov_b32 m0, s70
E v102,v102
M a[96:99],a[192:195],v[76:79],a[96:99]
buffer_load_dwordx4 v8, s[44:47], s6 offen nt lds
M a[100:103],a[196:199],v[76:79],a[100:103]
E v107,v107
M a[104:107],a[200:203],v[76:79],a[104:107]
E v100,v100
M a[108:111],a[204:207],v[76:79],a[108:111]
E v103,v103
M a[240:243],a[224:227],v[76:79],a[240:243]
M a[112:115],a[208:211],v[76:79],a[112:115]
s_add_u32 s77, s77, 8192
buffer_load_dwordx4 v8, s[44:47], s6 offen offset:2048 nt lds
C v110,v104,v105
M a[116:119],a[212:215],v[76:79],a[116:119]
E v42,v42
M a[120:123],a[216:219],v[76:79],a[120:123]
C v108,v100,v101
C v109,v102,v103
M a[124:127],a[220:223],v[76:79],a[124:127]
s_add_u32 m0, s70, 896
E v39,v39
M v[68:71],a[160:163],v[48:51],v[64:67]
buffer_load_dwordx4 v8, s[44:47], s6 offen offset:128 nt lds
C v111,v106,v107
M v[68:71],a[164:167],v[52:55],v[68:71]
E v43,v43
M v[68:71],a[168:171],v[56:59],v[68:71]
E v40,v40
M v[68:71],a[172:175],v[60:63],v[68:71]
E v37,v37
M v[72:75],a[176:179],v[48:51],v[64:67]
s_add_u32 s71, s6, 4096
buffer_load_dwordx4 v8, s[44:47], s6 offen offset:2176 nt lds
E v41,v41
M v[72:75],a[180:183],v[52:55],v[72:75]
E v36,v36
M v[72:75],a[184:187],v[56:59],v[72:75]
E v38,v38
M v[72:75],a[188:191],v[60:63],v[72:75]
s_add_u32 m0, s70, 4096
E v70,v70
M a[128:131],a[192:195],v[108:111],a[128:131]
buffer_load_dwordx4 v8, s[44:47], s71 offen nt lds
T a[192:193],v4,0
M a[132:135],a[196:199],v[108:111],a[132:135]
T a[194:195],v4,4096
M a[136:139],a[200:203],v[108:111],a[136:139]
T a[200:201],v4,512
M a[140:143],a[204:207],v[108:111],a[140:143]
T a[204:205],v5,512
M a[244:247],a[224:227],v[108:111],a[244:247]
M a[144:147],a[208:211],v[108:111],a[144:147]
s_and_b32 s77, s77, 131071
buffer_load_dwordx4 v8, s[44:47], s71 offen offset:2048 nt lds
T a[206:207],v5,4608
M a[148:151],a[212:215],v[108:111],a[148:151]
T a[196:197],v5,0
C v44,v36,v37
M a[152:155],a[216:219],v[108:111],a[152:155]
T a[212:213],v5,1024
C v45,v38,v39
M a[156:159],a[220:223],v[108:111],a[156:159]
s_add_u32 m0, s70, 4992
T a[208:209],v4,1024
C v46,v40,v41
M v[36:39],a[160:163],v[80:83],v[96:99]
buffer_load_dwordx4 v8, s[44:47], s71 offen offset:128 nt lds
T a[210:211],v4,5120
M v[36:39],a[164:167],v[84:87],v[36:39]
T a[198:199],v5,4096
M v[36:39],a[168:171],v[88:91],v[36:39]
T a[202:203],v4,4608
M v[36:39],a[172:175],v[92:95],v[36:39]
T a[216:217],v4,1536
C v47,v42,v43
M v[40:43],a[176:179],v[80:83],v[96:99]
s_cmp_eq_u32 s77, s4
s_cselect_b32 s5, 2097152, s5
buffer_load_dwordx4 v8, s[44:47], s71 offen offset:2176 nt lds
T a[218:219],v4,5632
M v[40:43],a[180:183],v[84:87],v[40:43]
s_add_u32 s6, s77, s5
T a[214:215],v5,5120
M v[40:43],a[184:187],v[88:91],v[40:43]
T a[222:223],v5,5632
M v[40:43],a[188:191],v[92:95],v[40:43]
s_waitcnt lgkmcnt(13)
T a[220:221],v5,1536
M a[0:3],a[192:195],v[44:47],a[0:3]
s_waitcnt lgkmcnt(6)
E v68,v68
M a[4:7],a[196:199],v[44:47],a[4:7]
s_waitcnt lgkmcnt(5)
E v71,v71
M a[8:11],a[200:203],v[44:47],a[8:11]
E v73,v73
M a[12:15],a[204:207],v[44:47],a[12:15]
E v69,v69
M a[228:231],a[224:227],v[44:47],a[228:231]
M a[16:19],a[208:211],v[44:47],a[16:19]
s_waitcnt lgkmcnt(2)
E v72,v72
M a[20:23],a[212:215],v[44:47],a[20:23]
E v74,v74
M a[24:27],a[216:219],v[44:47],a[24:27]
s_waitcnt lgkmcnt(0)
E v75,v75
M a[28:31],a[220:223],v[44:47],a[28:31]
s_memrealtime s[18:19]
C v76,v68,v69
C v77,v70,v71
M v[68:71],a[160:163],v[112:115],v[128:131]
E v41,v41
M v[68:71],a[164:167],v[116:119],v[68:71]
E v38,v38
M v[68:71],a[168:171],v[120:123],v[68:71]
s_mov_b32 s64, s65
C v79,v74,v75
M v[68:71],a[172:175],v[124:127],v[68:71]
s_cmp_eq_u32 s65, 131072
s_cselect_b32 s66, s90, s76
C v78,v72,v73
M v[72:75],a[176:179],v[112:115],v[128:131]
s_add_u32 s65, s65, s66
E v40,v40
M v[72:75],a[180:183],v[116:119],v[72:75]
E v36,v36
M v[72:75],a[184:187],v[120:123],v[72:75]
s_waitcnt lgkmcnt(0)
E v43,v43
M v[72:75],a[188:191],v[124:127],v[72:75]
s_sub_u32 s19, s3, s18
v_add_u32 v2, s66, v2
M a[32:35],a[192:195],v[76:79],a[32:35]
s_cmp_gt_i32 s19, 14
s_cbranch_scc1 L_pc0YL
L_pe0YL:
E v42,v42
M a[36:39],a[196:199],v[76:79],a[36:39]
s_add_u32 s3, s18, 110
v_add_u32 v3, s66, v3
M a[40:43],a[200:203],v[76:79],a[40:43]
s_waitcnt vmcnt(8) lgkmcnt(0)
E v37,v37
M a[44:47],a[204:207],v[76:79],a[44:47]
s_barrier
M a[232:235],a[224:227],v[76:79],a[232:235]
M a[48:51],a[208:211],v[76:79],a[48:51]
C v47,v42,v43
M a[52:55],a[212:215],v[76:79],a[52:55]
s_sub_u32 s69, s69, 1
E v39,v39
M a[56:59],a[216:219],v[76:79],a[56:59]
s_cmp_lg_u32 s69, 0
C v44,v36,v37
C v45,v38,v39
M a[60:63],a[220:223],v[76:79],a[60:63]
C v46,v40,v41
s_cbranch_scc1 L_atY_4
s_sub_u32 s78, s78, 4190208
s_sub_u32 s79, s79, 1024
s_mov_b32 s75, s80
M v[100:103],a[160:163],v[144:147],v[160:163]
E v69,v69
M v[100:103],a[164:167],v[148:151],v[100:103]
E v72,v72
M v[100:103],a[168:171],v[152:155],v[100:103]
E v70,v70
M v[100:103],a[172:175],v[156:159],v[100:103]
E v74,v74
M v[104:107],a[176:179],v[144:147],v[160:163]
E v71,v71
M v[104:107],a[180:183],v[148:151],v[104:107]
D a[160:163],v2,0
M v[104:107],a[184:187],v[152:155],v[104:107]
E v73,v73
M v[104:107],a[188:191],v[156:159],v[104:107]
s_mov_b32 s23, L_ph_oe0Y-L_code0
buffer_load_dword v14, v13, s[8:11], s23 offen
D a[164:167],v3,0
M a[64:67],a[192:195],v[44:47],a[64:67]
D a[168:171],v2,1024
C v77,v70,v71
M a[68:71],a[196:199],v[44:47],a[68:71]
s_mov_b32 m0, s75
E v68,v68
M a[72:75],a[200:203],v[44:47],a[72:75]
buffer_load_dwordx4 v10, s[36:39], s79 offen lds
D a[172:175],v3,1024
M a[76:79],a[204:207],v[44:47],a[76:79]
E v75,v75
M a[236:239],a[224:227],v[44:47],a[236:239]
M a[80:83],a[208:211],v[44:47],a[80:83]
D a[176:179],v2,4096
C v78,v72,v73
M a[84:87],a[212:215],v[44:47],a[84:87]
s_add_u32 s71, s79, 7168
buffer_load_dwordx4 v10, s[36:39], s71 offen offset:1024 lds
D a[180:183],v3,4096
M a[88:91],a[216:219],v[44:47],a[88:91]
D a[184:187],v2,5120
C v76,v68,v69
M a[92:95],a[220:223],v[44:47],a[92:95]
s_waitcnt lgkmcnt(6)
D a[188:191],v3,5120
C v79,v74,v75
M v[36:39],a[160:163],v[16:19],v[32:35]
s_waitcnt lgkmcnt(6)
s_add_u32 s72, s79, 14336
buffer_load_dwordx4 v10, s[36:39], s72 offen offset:2048 lds
M v[36:39],a[164:167],v[20:23],v[36:39]
s_waitcnt lgkmcnt(5)
E v101,v101
M v[36:39],a[168:171],v[24:27],v[36:39]
s_waitcnt lgkmcnt(4)
E v104,v104
M v[36:39],a[172:175],v[28:31],v[36:39]
s_waitcnt lgkmcnt(3)
s_add_u32 s71, s79, 21504
buffer_load_dwordx4 v10, s[36:39], s71 offen offset:3072 lds
M v[40:43],a[176:179],v[16:19],v[32:35]
s_waitcnt lgkmcnt(2)
s_add_u32 s72, s79, 32768
E v103,v103
M v[40:43],a[180:183],v[20:23],v[40:43]
s_waitcnt lgkmcnt(1)
s_add_u32 m0, s75, 4096
M v[40:43],a[184:187],v[24:27],v[40:43]
s_waitcnt lgkmcnt(0)
buffer_load_dwordx4 v10, s[36:39], s72 offen lds
v_add_u32 v5, s66, v5
M v[40:43],a[188:191],v[28:31],v[40:43]
s_add_u32 s79, s79, 128
E v100,v100
M a[96:99],a[192:195],v[76:79],a[96:99]
s_add_u32 s75, s75, 20480
E v105,v105
M a[100:103],a[196:199],v[76:79],a[100:103]
s_cmp_eq_u32 s75, s95
s_cselect_b32 s75, s80, s75
E v106,v106
M a[104:107],a[200:203],v[76:79],a[104:107]
s_add_u32 s70, s74, 33792
s_mov_b32 m0, s70
C v108,v100,v101
M a[108:111],a[204:207],v[76:79],a[108:111]
buffer_load_dwordx4 v9, s[40:43], s78 offen lds
v_add_u32 v4, s66, v4
M a[240:243],a[224:227],v[76:79],a[240:243]
M a[112:115],a[208:211],v[76:79],a[112:115]
E v102,v102
C v110,v104,v105
M a[116:119],a[212:215],v[76:79],a[116:119]
E v107,v107
M a[120:123],a[216:219],v[76:79],a[120:123]
s_add_u32 s71, s78, 129024
buffer_load_dwordx4 v9, s[40:43], s71 offen offset:2048 lds
C v109,v102,v103
M a[124:127],a[220:223],v[76:79],a[124:127]
E v37,v37
M v[68:71],a[160:163],v[48:51],v[64:67]
s_add_u32 m0, s70, 896
E v42,v42
M v[68:71],a[164:167],v[52:55],v[68:71]
buffer_load_dwordx4 v9, s[40:43], s78 offen offset:128 lds
C v111,v106,v107
M v[68:71],a[168:171],v[56:59],v[68:71]
E v40,v40
M v[68:71],a[172:175],v[60:63],v[68:71]
E v39,v39
M v[72:75],a[176:179],v[48:51],v[64:67]
buffer_load_dwordx4 v9, s[40:43], s71 offen offset:2176 lds
M v[72:75],a[180:183],v[52:55],v[72:75]
s_add_u32 s78, s78, 65536
E v38,v38
M v[72:75],a[184:187],v[56:59],v[72:75]
s_add_u32 m0, s70, 4104
E v36,v36
M v[72:75],a[188:191],v[60:63],v[72:75]
buffer_load_dwordx4 v9, s[40:43], s78 offen lds
E v43,v43
M a[128:131],a[192:195],v[108:111],a[128:131]
E v70,v70
M a[132:135],a[196:199],v[108:111],a[132:135]
T a[192:193],v4,0
C v44,v36,v37
M a[136:139],a[200:203],v[108:111],a[136:139]
s_add_u32 s71, s78, 129024
buffer_load_dwordx4 v9, s[40:43], s71 offen offset:2048 lds
T a[198:199],v5,4096
M a[140:143],a[204:207],v[108:111],a[140:143]
T a[200:201],v4,512
M a[244:247],a[224:227],v[108:111],a[244:247]
M a[144:147],a[208:211],v[108:111],a[144:147]
s_add_u32 m0, s70, 5000
T a[194:195],v4,4096
E v41,v41
M a[148:151],a[212:215],v[108:111],a[148:151]
buffer_load_dwordx4 v9, s[40:43], s78 offen offset:128 lds
T a[206:207],v5,4608
M a[152:155],a[216:219],v[108:111],a[152:155]
T a[210:211],v4,5120
C v45,v38,v39
M a[156:159],a[220:223],v[108:111],a[156:159]
T a[216:217],v4,1536
T a[220:221],v5,1536
M v[36:39],a[160:163],v[80:83],v[96:99]
s_add_u32 s78, s78, 458752
buffer_load_dwordx4 v9, s[40:43], s71 offen offset:2176 lds
T a[196:197],v5,0
M v[36:39],a[164:167],v[84:87],v[36:39]
T a[202:203],v4,4608
C v47,v42,v43
M v[36:39],a[168:171],v[88:91],v[36:39]
T a[208:209],v4,1024
T a[212:213],v5,1024
M v[36:39],a[172:175],v[92:95],v[36:39]
T a[204:205],v5,512
C v46,v40,v41
M v[40:43],a[176:179],v[80:83],v[96:99]
E v71,v71
M v[40:43],a[180:183],v[84:87],v[40:43]
T a[214:215],v5,5120
M v[40:43],a[184:187],v[88:91],v[40:43]
T a[218:219],v4,5632
C v77,v70,v71
M v[40:43],a[188:191],v[92:95],v[40:43]
s_waitcnt lgkmcnt(11)
T a[222:223],v5,5632
M a[0:3],a[192:195],v[44:47],a[0:3]
s_waitcnt lgkmcnt(7)
E v36,v36
M a[4:7],a[196:199],v[44:47],a[4:7]
s_waitcnt lgkmcnt(6)
E v74,v74
M a[8:11],a[200:203],v[44:47],a[8:11]
s_waitcnt lgkmcnt(3)
E v69,v69
M a[12:15],a[204:207],v[44:47],a[12:15]
E v68,v68
M a[228:231],a[224:227],v[44:47],a[228:231]
M a[16:19],a[208:211],v[44:47],a[16:19]
s_waitcnt lgkmcnt(2)
E v73,v73
M a[20:23],a[212:215],v[44:47],a[20:23]
s_waitcnt lgkmcnt(1)
E v75,v75
M a[24:27],a[216:219],v[44:47],a[24:27]
s_waitcnt lgkmcnt(0)
E v72,v72
M a[28:31],a[220:223],v[44:47],a[28:31]
C v76,v68,v69
C v78,v72,v73
M v[68:71],a[160:163],v[112:115],v[128:131]
E v39,v39
M v[68:71],a[164:167],v[116:119],v[68:71]
E v37,v37
M v[68:71],a[168:171],v[120:123],v[68:71]
s_mov_b32 s64, s65
E v40,v40
M v[68:71],a[172:175],v[124:127],v[68:71]
s_cmp_eq_u32 s65, 131072
s_cselect_b32 s66, s90, s76
C v79,v74,v75
M v[72:75],a[176:179],v[112:115],v[128:131]
s_add_u32 s65, s65, s66
E v41,v41
M v[72:75],a[180:183],v[116:119],v[72:75]
C v44,v36,v37
M v[72:75],a[184:187],v[120:123],v[72:75]
E v38,v38
M v[72:75],a[188:191],v[124:127],v[72:75]
M a[32:35],a[192:195],v[76:79],a[32:35]
E v42,v42
M a[36:39],a[196:199],v[76:79],a[36:39]
C v46,v40,v41
M a[40:43],a[200:203],v[76:79],a[40:43]
s_waitcnt vmcnt(14) lgkmcnt(0)
C v45,v38,v39
v_add_u32 v3, s66, v3
M a[44:47],a[204:207],v[76:79],a[44:47]
s_barrier
M a[232:235],a[224:227],v[76:79],a[232:235]
M a[48:51],a[208:211],v[76:79],a[48:51]
E v43,v43
M a[52:55],a[212:215],v[76:79],a[52:55]
v_add_u32 v2, s66, v2
M a[56:59],a[216:219],v[76:79],a[56:59]
C v47,v42,v43
M a[60:63],a[220:223],v[76:79],a[60:63]
M v[100:103],a[160:163],v[144:147],v[160:163]
E v74,v74
M v[100:103],a[164:167],v[148:151],v[100:103]
E v71,v71
M v[100:103],a[168:171],v[152:155],v[100:103]
E v69,v69
M v[100:103],a[172:175],v[156:159],v[100:103]
D a[164:167],v3,0
E v70,v70
M v[104:107],a[176:179],v[144:147],v[160:163]
E v68,v68
M v[104:107],a[180:183],v[148:151],v[104:107]
D a[160:163],v2,0
M v[104:107],a[184:187],v[152:155],v[104:107]
s_mov_b32 m0, s75
D a[172:175],v3,1024
C v76,v68,v69
M v[104:107],a[188:191],v[156:159],v[104:107]
buffer_load_dwordx4 v10, s[36:39], s79 offen lds
D a[176:179],v2,4096
M a[64:67],a[192:195],v[44:47],a[64:67]
D a[188:191],v3,5120
C v77,v70,v71
M a[68:71],a[196:199],v[44:47],a[68:71]
E v75,v75
M a[72:75],a[200:203],v[44:47],a[72:75]
D a[168:171],v2,1024
C v79,v74,v75
M a[76:79],a[204:207],v[44:47],a[76:79]
s_add_u32 s71, s79, 7168
buffer_load_dwordx4 v10, s[36:39], s71 offen offset:1024 lds
D a[180:183],v3,4096
M a[236:239],a[224:227],v[44:47],a[236:239]
M a[80:83],a[208:211],v[44:47],a[80:83]
E v72,v72
M a[84:87],a[212:215],v[44:47],a[84:87]
E v73,v73
M a[88:91],a[216:219],v[44:47],a[88:91]
D a[184:187],v2,5120
C v78,v72,v73
M a[92:95],a[220:223],v[44:47],a[92:95]
)"
R"(s_waitcnt lgkmcnt(6)
s_add_u32 s72, s79, 14336
buffer_load_dwordx4 v10, s[36:39], s72 offen offset:2048 lds
M v[36:39],a[160:163],v[16:19],v[32:35]
E v103,v103
M v[36:39],a[164:167],v[20:23],v[36:39]
s_waitcnt lgkmcnt(2)
E v102,v102
M v[36:39],a[168:171],v[24:27],v[36:39]
E v101,v101
M v[36:39],a[172:175],v[28:31],v[36:39]
s_add_u32 s71, s79, 21504
buffer_load_dwordx4 v10, s[36:39], s71 offen offset:3072 lds
M v[40:43],a[176:179],v[16:19],v[32:35]
s_waitcnt lgkmcnt(1)
E v105,v105
M v[40:43],a[180:183],v[20:23],v[40:43]
s_waitcnt lgkmcnt(0)
s_add_u32 s72, s79, 32768
C v109,v102,v103
M v[40:43],a[184:187],v[24:27],v[40:43]
s_add_u32 m0, s75, 4096
E v100,v100
M v[40:43],a[188:191],v[28:31],v[40:43]
buffer_load_dwordx4 v10, s[36:39], s72 offen lds
C v108,v100,v101
M a[96:99],a[192:195],v[76:79],a[96:99]
s_add_u32 s79, s79, 128
E v104,v104
M a[100:103],a[196:199],v[76:79],a[100:103]
s_add_u32 s75, s75, 20480
E v107,v107
M a[104:107],a[200:203],v[76:79],a[104:107]
s_cmp_eq_u32 s75, s95
s_cselect_b32 s75, s80, s75
E v106,v106
M a[108:111],a[204:207],v[76:79],a[108:111]
s_add_u32 s70, s74, 67584
s_mov_b32 m0, s70
C v110,v104,v105
M a[240:243],a[224:227],v[76:79],a[240:243]
M a[112:115],a[208:211],v[76:79],a[112:115]
buffer_load_dwordx4 v9, s[40:43], s78 offen lds
M a[116:119],a[212:215],v[76:79],a[116:119]
E v42,v42
M a[120:123],a[216:219],v[76:79],a[120:123]
E v41,v41
M a[124:127],a[220:223],v[76:79],a[124:127]
E v36,v36
M v[68:71],a[160:163],v[48:51],v[64:67]
s_add_u32 s71, s78, 129024
buffer_load_dwordx4 v9, s[40:43], s71 offen offset:2048 lds
v_add_u32 v5, s66, v5
M v[68:71],a[164:167],v[52:55],v[68:71]
C v111,v106,v107
M v[68:71],a[168:171],v[56:59],v[68:71]
E v39,v39
M v[68:71],a[172:175],v[60:63],v[68:71]
s_add_u32 m0, s70, 896
E v43,v43
M v[72:75],a[176:179],v[48:51],v[64:67]
buffer_load_dwordx4 v9, s[40:43], s78 offen offset:128 lds
v_add_u32 v4, s66, v4
M v[72:75],a[180:183],v[52:55],v[72:75]
E v37,v37
M v[72:75],a[184:187],v[56:59],v[72:75]
E v40,v40
M v[72:75],a[188:191],v[60:63],v[72:75]
E v38,v38
M a[128:131],a[192:195],v[108:111],a[128:131]
buffer_load_dwordx4 v9, s[40:43], s71 offen offset:2176 lds
T a[194:195],v4,4096
M a[132:135],a[196:199],v[108:111],a[132:135]
T a[196:197],v5,0
C v45,v38,v39
M a[136:139],a[200:203],v[108:111],a[136:139]
s_add_u32 s78, s78, 65536
T a[200:201],v4,512
C v44,v36,v37
M a[140:143],a[204:207],v[108:111],a[140:143]
s_add_u32 m0, s70, 4104
T a[192:193],v4,0
T a[198:199],v5,4096
M a[244:247],a[224:227],v[108:111],a[244:247]
M a[144:147],a[208:211],v[108:111],a[144:147]
buffer_load_dwordx4 v9, s[40:43], s78 offen lds
T a[202:203],v4,4608
M a[148:151],a[212:215],v[108:111],a[148:151]
E v69,v69
M a[152:155],a[216:219],v[108:111],a[152:155]
T a[204:205],v5,512
T a[216:217],v4,1536
M a[156:159],a[220:223],v[108:111],a[156:159]
T a[218:219],v4,5632
M v[36:39],a[160:163],v[80:83],v[96:99]
s_add_u32 s71, s78, 129024
buffer_load_dwordx4 v9, s[40:43], s71 offen offset:2048 lds
T a[210:211],v4,5120
M v[36:39],a[164:167],v[84:87],v[36:39]
T a[208:209],v4,1024
C v46,v40,v41
M v[36:39],a[168:171],v[88:91],v[36:39]
T a[222:223],v5,5632
C v47,v42,v43
M v[36:39],a[172:175],v[92:95],v[36:39]
s_add_u32 m0, s70, 5000
T a[206:207],v5,4608
M v[40:43],a[176:179],v[80:83],v[96:99]
buffer_load_dwordx4 v9, s[40:43], s78 offen offset:128 lds
T a[212:213],v5,1024
M v[40:43],a[180:183],v[84:87],v[40:43]
T a[214:215],v5,5120
M v[40:43],a[184:187],v[88:91],v[40:43]
T a[220:221],v5,1536
M v[40:43],a[188:191],v[92:95],v[40:43]
s_waitcnt lgkmcnt(12)
E v68,v68
M a[0:3],a[192:195],v[44:47],a[0:3]
s_waitcnt lgkmcnt(11)
s_add_u32 s78, s78, 458752
buffer_load_dwordx4 v9, s[40:43], s71 offen offset:2176 lds
M a[4:7],a[196:199],v[44:47],a[4:7]
s_waitcnt lgkmcnt(10)
E v70,v70
M a[8:11],a[200:203],v[44:47],a[8:11]
s_waitcnt lgkmcnt(3)
E v75,v75
M a[12:15],a[204:207],v[44:47],a[12:15]
E v73,v73
M a[228:231],a[224:227],v[44:47],a[228:231]
M a[16:19],a[208:211],v[44:47],a[16:19]
s_waitcnt lgkmcnt(1)
E v74,v74
M a[20:23],a[212:215],v[44:47],a[20:23]
E v71,v71
M a[24:27],a[216:219],v[44:47],a[24:27]
s_waitcnt lgkmcnt(0)
E v72,v72
M a[28:31],a[220:223],v[44:47],a[28:31]
C v76,v68,v69
C v77,v70,v71
M v[68:71],a[160:163],v[112:115],v[128:131]
C v78,v72,v73
M v[68:71],a[164:167],v[116:119],v[68:71]
E v36,v36
M v[68:71],a[168:171],v[120:123],v[68:71]
E v41,v41
M v[68:71],a[172:175],v[124:127],v[68:71]
C v79,v74,v75
M v[72:75],a[176:179],v[112:115],v[128:131]
E v39,v39
M v[72:75],a[180:183],v[116:119],v[72:75]
E v42,v42
M v[72:75],a[184:187],v[120:123],v[72:75]
M v[72:75],a[188:191],v[124:127],v[72:75]
E v38,v38
M a[32:35],a[192:195],v[76:79],a[32:35]
C v45,v38,v39
M a[36:39],a[196:199],v[76:79],a[36:39]
E v43,v43
M a[40:43],a[200:203],v[76:79],a[40:43]
E v40,v40
M a[44:47],a[204:207],v[76:79],a[44:47]
M a[232:235],a[224:227],v[76:79],a[232:235]
M a[48:51],a[208:211],v[76:79],a[48:51]
E v37,v37
M a[52:55],a[212:215],v[76:79],a[52:55]
C v47,v42,v43
M a[56:59],a[216:219],v[76:79],a[56:59]
C v44,v36,v37
M a[60:63],a[220:223],v[76:79],a[60:63]
C v46,v40,v41
M v[100:103],a[160:163],v[144:147],v[160:163]
E v68,v68
M v[100:103],a[164:167],v[148:151],v[100:103]
E v73,v73
M v[100:103],a[168:171],v[152:155],v[100:103]
E v71,v71
M v[100:103],a[172:175],v[156:159],v[100:103]
E v69,v69
M v[104:107],a[176:179],v[144:147],v[160:163]
E v72,v72
M v[104:107],a[180:183],v[148:151],v[104:107]
E v75,v75
M v[104:107],a[184:187],v[152:155],v[104:107]
E v74,v74
M v[104:107],a[188:191],v[156:159],v[104:107]
E v70,v70
M a[64:67],a[192:195],v[44:47],a[64:67]
E v103,v103
M a[68:71],a[196:199],v[44:47],a[68:71]
C v78,v72,v73
C v79,v74,v75
M a[72:75],a[200:203],v[44:47],a[72:75]
C v76,v68,v69
C v77,v70,v71
M a[76:79],a[204:207],v[44:47],a[76:79]
E v107,v107
M a[236:239],a[224:227],v[44:47],a[236:239]
M a[80:83],a[208:211],v[44:47],a[80:83]
E v101,v101
M a[84:87],a[212:215],v[44:47],a[84:87]
E v104,v104
M a[88:91],a[216:219],v[44:47],a[88:91]
E v105,v105
M a[92:95],a[220:223],v[44:47],a[92:95]
E v102,v102
M a[96:99],a[192:195],v[76:79],a[96:99]
E v106,v106
M a[100:103],a[196:199],v[76:79],a[100:103]
E v100,v100
M a[104:107],a[200:203],v[76:79],a[104:107]
C v110,v104,v105
M a[108:111],a[204:207],v[76:79],a[108:111]
C v111,v106,v107
M a[240:243],a[224:227],v[76:79],a[240:243]
M a[112:115],a[208:211],v[76:79],a[112:115]
C v108,v100,v101
M a[116:119],a[212:215],v[76:79],a[116:119]
C v109,v102,v103
M a[120:123],a[216:219],v[76:79],a[120:123]
M a[124:127],a[220:223],v[76:79],a[124:127]
M a[128:131],a[192:195],v[108:111],a[128:131]
M a[132:135],a[196:199],v[108:111],a[132:135]
M a[136:139],a[200:203],v[108:111],a[136:139]
M a[140:143],a[204:207],v[108:111],a[140:143]
M a[244:247],a[224:227],v[108:111],a[244:247]
M a[144:147],a[208:211],v[108:111],a[144:147]
M a[148:151],a[212:215],v[108:111],a[148:151]
M a[152:155],a[216:219],v[108:111],a[152:155]
M a[156:159],a[220:223],v[108:111],a[156:159]
L_ph_oe0Y:
s_mov_b64 s[98:99], 0
s_mov_b32 s31, 0x5d800000
R v132,a228
R v133,a232
R v134,a236
R v135,a240
R v136,a244
v_max3_f32 v137, v132, v133, v134
v_max3_f32 v137, v137, v135, v136
v_cmp_ngt_f32 vcc, s31, v137
v_cmp_u_f32 s[98:99], v132, v133
s_nop 1
s_or_b64 vcc, vcc, s[98:99]
v_cmp_u_f32 s[98:99], v134, v135
s_nop 1
s_or_b64 vcc, vcc, s[98:99]
v_cmp_u_f32 s[98:99], v136, v136
s_nop 1
s_or_b64 vcc, vcc, s[98:99]
s_cbranch_scc0 L_ret0Y
s_mov_b32 s97, 0
s_add_u32 s31, s8, L_slow0-L_code0-4
s_addc_u32 s23, s9, 0
s_mov_b32 s18, s31
s_mov_b32 s19, s23
s_swappc_b64 s[98:99], s[18:19]
L_ret0Y:
v_rcp_f32 v244, v132
v_rcp_f32 v245, v133
v_rcp_f32 v246, v134
v_rcp_f32 v247, v135
v_rcp_f32 v15, v136
v_rcp_f32 v160, v136
R v164,a144
R v165,a145
R v166,a146
R v167,a147
v_pk_mul_f32 v[164:165], v[164:165], v[160:161] op_sel_hi:[1,0]
v_pk_mul_f32 v[166:167], v[166:167], v[160:161] op_sel_hi:[1,0]
C v248,v164,v165
C v249,v166,v167
R v168,a148
R v169,a149
R v170,a150
R v171,a151
v_pk_mul_f32 v[168:169], v[168:169], v[160:161] op_sel_hi:[1,0]
v_pk_mul_f32 v[170:171], v[170:171], v[160:161] op_sel_hi:[1,0]
C v250,v168,v169
C v251,v170,v171
R v164,a152
R v165,a153
R v166,a154
R v167,a155
v_pk_mul_f32 v[164:165], v[164:165], v[160:161] op_sel_hi:[1,0]
v_pk_mul_f32 v[166:167], v[166:167], v[160:161] op_sel_hi:[1,0]
C v252,v164,v165
C v253,v166,v167
R v168,a156
R v169,a157
R v170,a158
R v171,a159
v_pk_mul_f32 v[168:169], v[168:169], v[160:161] op_sel_hi:[1,0]
v_pk_mul_f32 v[170:171], v[170:171], v[160:161] op_sel_hi:[1,0]
C v254,v168,v169
C v255,v170,v171
L_ph_qp1Y:
s_mov_b32 s65, 33792
s_mov_b32 s76, 33792
s_mov_b32 s90, -67584
s_mov_b32 s7, 0
s_mov_b32 s68, 20480
v_add_u32 v6, -33792, v6
v_add_u32 v7, -20480, v7
v_add_u32 v11, -20480, v11
s_waitcnt vmcnt(13) lgkmcnt(0)
s_barrier
T a[144:145],v6,0
T a[146:147],v6,256
T a[148:149],v6,8448
T a[150:151],v6,8704
D a[176:179],v7,0
D a[196:199],v11,0
D a[180:183],v7,2048
D a[200:203],v11,2048
D a[184:187],v7,4096
D a[204:207],v11,4096
D a[188:191],v7,6144
D a[208:211],v11,6144
D a[192:195],v7,8192
D a[212:215],v11,8192
T a[152:153],v6,8
T a[154:155],v6,264
s_waitcnt lgkmcnt(11)
M v[16:19],a[144:147],a[176:179],0
s_waitcnt lgkmcnt(10)
T a[160:161],v6,512
M v[16:19],a[148:151],a[196:199],v[16:19]
s_waitcnt lgkmcnt(9)
T a[156:157],v6,8456
M v[48:51],a[148:151],a[200:203],0
s_mov_b32 m0, s75
T a[162:163],v6,768
M v[48:51],a[144:147],a[180:183],v[48:51]
s_waitcnt lgkmcnt(10)
T a[158:159],v6,8712
buffer_load_dwordx4 v10, s[36:39], s79 offen lds
M v[80:83],a[144:147],a[184:187],0
s_waitcnt lgkmcnt(10)
T a[164:165],v6,8960
R v176,a0
M v[80:83],a[148:151],a[204:207],v[80:83]
s_waitcnt lgkmcnt(9)
R v177,a1
R v178,a2
M v[112:115],a[148:151],a[208:211],0
T a[166:167],v6,9216
R v179,a3
M v[112:115],a[144:147],a[188:191],v[112:115]
s_waitcnt lgkmcnt(9)
s_add_u32 s71, s79, 7168
buffer_load_dwordx4 v10, s[36:39], s71 offen offset:1024 lds
v_mul_f32 v176, v244, v176
M v[144:147],a[144:147],a[192:195],0
s_waitcnt lgkmcnt(8)
v_mul_f32 v177, v244, v177
v_mul_f32 v178, v244, v178
M v[144:147],a[148:151],a[212:215],v[144:147]
s_waitcnt lgkmcnt(6)
v_mul_f32 v179, v244, v179
C v176,v176,v177
M v[20:23],a[152:155],a[176:179],0
s_waitcnt lgkmcnt(2)
T a[168:169],v6,520
C v177,v178,v179
M v[20:23],a[156:159],a[196:199],v[20:23]
s_add_u32 s72, s79, 14336
buffer_load_dwordx4 v10, s[36:39], s72 offen offset:2048 lds
R v178,a4
M v[52:55],a[156:159],a[200:203],0
T a[170:171],v6,776
R v179,a5
M v[52:55],a[152:155],a[180:183],v[52:55]
R v180,a6
R v181,a7
M v[84:87],a[152:155],a[184:187],0
T a[172:173],v6,8968
v_mul_f32 v178, v244, v178
M v[84:87],a[156:159],a[204:207],v[84:87]
s_add_u32 s71, s79, 21504
buffer_load_dwordx4 v10, s[36:39], s71 offen offset:3072 lds
v_mul_f32 v179, v244, v179
M v[116:119],a[156:159],a[208:211],0
T a[174:175],v6,9224
v_mul_f32 v180, v244, v180
M v[116:119],a[152:155],a[188:191],v[116:119]
s_add_u32 s72, s79, 32768
v_mul_f32 v181, v244, v181
C v178,v178,v179
M v[148:151],a[152:155],a[192:195],0
s_add_u32 m0, s75, 4096
C v179,v180,v181
R v180,a8
M v[148:151],a[156:159],a[212:215],v[148:151]
buffer_load_dwordx4 v10, s[36:39], s72 offen lds
R v181,a9
M v[24:27],a[160:163],a[176:179],0
s_waitcnt lgkmcnt(4)
s_add_u32 s79, s79, 128
T a[144:145],v6,1024
R v182,a10
M v[24:27],a[164:167],a[196:199],v[24:27]
s_cmp_eq_u32 s7, 40960
s_cselect_b32 s67, -40960, s68
s_add_u32 s75, s75, 20480
R v183,a11
M v[56:59],a[164:167],a[200:203],0
s_cmp_eq_u32 s75, s95
s_cselect_b32 s75, s80, s75
T a[146:147],v6,1280
v_mul_f32 v180, v244, v180
M v[56:59],a[160:163],a[180:183],v[56:59]
s_add_u32 s7, s7, s67
s_add_u32 s70, s74, 0
s_mov_b32 m0, s70
M v[88:91],a[160:163],a[184:187],0
T a[148:149],v6,9472
buffer_load_dwordx4 v9, s[40:43], s78 offen lds
M v[88:91],a[164:167],a[204:207],v[88:91]
v_add_u32 v7, s67, v7
v_mul_f32 v181, v244, v181
M v[120:123],a[164:167],a[208:211],0
T a[150:151],v6,9728
v_mul_f32 v182, v244, v182
M v[120:123],a[160:163],a[188:191],v[120:123]
v_add_u32 v11, s67, v11
v_mul_f32 v183, v244, v183
M v[152:155],a[160:163],a[192:195],0
s_add_u32 s71, s78, 129024
buffer_load_dwordx4 v9, s[40:43], s71 offen offset:2048 lds
C v180,v180,v181
M v[152:155],a[164:167],a[212:215],v[152:155]
s_waitcnt lgkmcnt(6)
C v181,v182,v183
R v182,a12
M v[28:31],a[168:171],a[176:179],0
s_waitcnt lgkmcnt(4)
T a[152:153],v6,1032
R v183,a13
M v[28:31],a[172:175],a[196:199],v[28:31]
s_add_u32 m0, s70, 896
R v184,a14
R v185,a15
M v[60:63],a[172:175],a[200:203],0
T a[154:155],v6,1288
buffer_load_dwordx4 v9, s[40:43], s78 offen offset:128 lds
M v[60:63],a[168:171],a[180:183],v[60:63]
v_mul_f32 v182, v244, v182
v_mul_f32 v183, v244, v183
M v[92:95],a[168:171],a[184:187],0
T a[156:157],v6,9480
v_mul_f32 v184, v244, v184
M v[92:95],a[172:175],a[204:207],v[92:95]
v_mul_f32 v185, v244, v185
C v182,v182,v183
M v[124:127],a[172:175],a[208:211],0
T a[158:159],v6,9736
buffer_load_dwordx4 v9, s[40:43], s71 offen offset:2176 lds
M v[124:127],a[168:171],a[188:191],v[124:127]
C v183,v184,v185
R v184,a16
M v[156:159],a[168:171],a[192:195],0
s_add_u32 s78, s78, 65536
R v185,a17
R v186,a18
M v[156:159],a[172:175],a[212:215],v[156:159]
s_waitcnt lgkmcnt(6)
s_add_u32 m0, s70, 4104
R v187,a19
M v[32:35],a[144:147],a[176:179],0
s_waitcnt lgkmcnt(4)
T a[160:161],v6,1536
buffer_load_dwordx4 v9, s[40:43], s78 offen lds
M v[32:35],a[148:151],a[196:199],v[32:35]
v_mul_f32 v184, v244, v184
v_mul_f32 v185, v244, v185
M v[64:67],a[148:151],a[200:203],0
T a[162:163],v6,1792
v_mul_f32 v186, v244, v186
M v[64:67],a[144:147],a[180:183],v[64:67]
v_mul_f32 v187, v244, v187
C v184,v184,v185
M v[96:99],a[144:147],a[184:187],0
s_add_u32 s71, s78, 129024
T a[164:165],v6,9984
buffer_load_dwordx4 v9, s[40:43], s71 offen offset:2048 lds
M v[96:99],a[148:151],a[204:207],v[96:99]
C v185,v186,v187
R v186,a20
M v[128:131],a[148:151],a[208:211],0
T a[166:167],v6,10240
R v187,a21
M v[128:131],a[144:147],a[188:191],v[128:131]
s_add_u32 m0, s70, 5000
R v188,a22
R v189,a23
M v[160:163],a[144:147],a[192:195],0
buffer_load_dwordx4 v9, s[40:43], s78 offen offset:128 lds
v_mul_f32 v186, v244, v186
)"
R"(M v[160:163],a[148:151],a[212:215],v[160:163]
s_waitcnt lgkmcnt(6)
v_mul_f32 v187, v244, v187
v_mul_f32 v188, v244, v188
M v[36:39],a[152:155],a[176:179],0
s_waitcnt lgkmcnt(4)
T a[168:169],v6,1544
v_mul_f32 v189, v244, v189
M v[36:39],a[156:159],a[196:199],v[36:39]
C v186,v186,v187
C v187,v188,v189
M v[68:71],a[156:159],a[200:203],0
s_add_u32 s78, s78, 458752
T a[170:171],v6,1800
buffer_load_dwordx4 v9, s[40:43], s71 offen offset:2176 lds
M v[68:71],a[152:155],a[180:183],v[68:71]
R v188,a24
R v189,a25
M v[100:103],a[152:155],a[184:187],0
T a[172:173],v6,9992
R v190,a26
M v[100:103],a[156:159],a[204:207],v[100:103]
R v191,a27
v_mul_f32 v188, v244, v188
M v[132:135],a[156:159],a[208:211],0
T a[174:175],v6,10248
v_mul_f32 v189, v244, v189
M v[132:135],a[152:155],a[188:191],v[132:135]
s_mov_b32 s64, s65
v_mul_f32 v190, v244, v190
v_mul_f32 v191, v244, v191
M v[164:167],a[152:155],a[192:195],0
s_cmp_eq_u32 s65, 67584
s_cselect_b32 s66, s90, s76
C v188,v188,v189
M v[164:167],a[156:159],a[212:215],v[164:167]
s_waitcnt lgkmcnt(6)
s_add_u32 s65, s65, s66
C v189,v190,v191
M v[40:43],a[160:163],a[176:179],0
s_waitcnt lgkmcnt(4)
R v190,a28
R v191,a29
M v[40:43],a[164:167],a[196:199],v[40:43]
s_waitcnt vmcnt(13) lgkmcnt(0)
v_add_u32 v6, s66, v6
R v192,a30
M v[72:75],a[164:167],a[200:203],0
s_barrier
M v[72:75],a[160:163],a[180:183],v[72:75]
T a[144:145],v6,0
R v193,a31
M v[104:107],a[160:163],a[184:187],0
s_mov_b32 m0, s75
T a[146:147],v6,256
M v[104:107],a[164:167],a[204:207],v[104:107]
T a[148:149],v6,8448
buffer_load_dwordx4 v10, s[36:39], s79 offen lds
M v[136:139],a[164:167],a[208:211],0
T a[150:151],v6,8704
v_mul_f32 v190, v244, v190
M v[136:139],a[160:163],a[188:191],v[136:139]
T a[152:153],v6,8
v_mul_f32 v191, v244, v191
M v[168:171],a[160:163],a[192:195],0
T a[154:155],v6,264
v_mul_f32 v192, v244, v192
M v[168:171],a[164:167],a[212:215],v[168:171]
v_mul_f32 v193, v244, v193
C v190,v190,v191
M v[44:47],a[168:171],a[176:179],0
s_add_u32 s71, s79, 7168
D a[176:179],v7,0
buffer_load_dwordx4 v10, s[36:39], s71 offen offset:1024 lds
M v[44:47],a[172:175],a[196:199],v[44:47]
D a[196:199],v11,0
C v191,v192,v193
M v[76:79],a[172:175],a[200:203],0
D a[200:203],v11,2048
R v192,a32
M v[76:79],a[168:171],a[180:183],v[76:79]
D a[180:183],v7,2048
R v193,a33
M v[108:111],a[168:171],a[184:187],0
D a[184:187],v7,4096
R v194,a34
M v[108:111],a[172:175],a[204:207],v[108:111]
s_add_u32 s72, s79, 14336
D a[204:207],v11,4096
buffer_load_dwordx4 v10, s[36:39], s72 offen offset:2048 lds
M v[140:143],a[172:175],a[208:211],0
D a[208:211],v11,6144
R v195,a35
M v[140:143],a[168:171],a[188:191],v[140:143]
D a[188:191],v7,6144
v_mul_f32 v192, v245, v192
M v[172:175],a[168:171],a[192:195],0
D a[192:195],v7,8192
v_mul_f32 v193, v245, v193
M v[172:175],a[172:175],a[212:215],v[172:175]
D a[212:215],v11,8192
v_mul_f32 v194, v245, v194
s_waitcnt lgkmcnt(9)
M v[16:19],a[144:147],a[176:179],v[16:19]
s_waitcnt lgkmcnt(8)
s_add_u32 s71, s79, 21504
T a[160:161],v6,512
buffer_load_dwordx4 v10, s[36:39], s71 offen offset:3072 lds
M v[16:19],a[148:151],a[196:199],v[16:19]
s_waitcnt lgkmcnt(8)
T a[156:157],v6,8456
M v[48:51],a[148:151],a[200:203],v[48:51]
s_waitcnt lgkmcnt(8)
T a[162:163],v6,768
M v[48:51],a[144:147],a[180:183],v[48:51]
s_waitcnt lgkmcnt(8)
s_add_u32 s72, s79, 32768
T a[158:159],v6,8712
v_mul_f32 v195, v245, v195
M v[80:83],a[144:147],a[184:187],v[80:83]
s_waitcnt lgkmcnt(8)
s_add_u32 m0, s75, 4096
T a[164:165],v6,8960
M v[80:83],a[148:151],a[204:207],v[80:83]
s_waitcnt lgkmcnt(8)
buffer_load_dwordx4 v10, s[36:39], s72 offen lds
C v192,v192,v193
M v[112:115],a[148:151],a[208:211],v[112:115]
s_waitcnt lgkmcnt(7)
s_add_u32 s79, s79, 128
T a[166:167],v6,9216
C v193,v194,v195
M v[112:115],a[144:147],a[188:191],v[112:115]
s_waitcnt lgkmcnt(7)
s_add_u32 s75, s75, 20480
R v194,a36
R v195,a37
M v[144:147],a[144:147],a[192:195],v[144:147]
s_waitcnt lgkmcnt(6)
s_cmp_eq_u32 s75, s95
s_cselect_b32 s75, s80, s75
R v196,a38
R v197,a39
M v[144:147],a[148:151],a[212:215],v[144:147]
s_add_u32 s70, s74, s64
s_mov_b32 m0, s70
M v[20:23],a[152:155],a[176:179],v[20:23]
s_waitcnt lgkmcnt(2)
T a[168:169],v6,520
buffer_load_dwordx4 v9, s[40:43], s78 offen lds
M v[20:23],a[156:159],a[196:199],v[20:23]
v_mul_f32 v194, v245, v194
v_mul_f32 v195, v245, v195
M v[52:55],a[156:159],a[200:203],v[52:55]
T a[170:171],v6,776
v_mul_f32 v196, v245, v196
M v[52:55],a[152:155],a[180:183],v[52:55]
v_mul_f32 v197, v245, v197
C v194,v194,v195
M v[84:87],a[152:155],a[184:187],v[84:87]
T a[172:173],v6,8968
C v195,v196,v197
M v[84:87],a[156:159],a[204:207],v[84:87]
s_add_u32 s71, s78, 129024
buffer_load_dwordx4 v9, s[40:43], s71 offen offset:2048 lds
R v196,a40
M v[116:119],a[156:159],a[208:211],v[116:119]
T a[174:175],v6,9224
R v197,a41
M v[116:119],a[152:155],a[188:191],v[116:119]
R v198,a42
R v199,a43
M v[148:151],a[152:155],a[192:195],v[148:151]
v_mul_f32 v196, v245, v196
v_mul_f32 v197, v245, v197
M v[148:151],a[156:159],a[212:215],v[148:151]
s_add_u32 m0, s70, 896
v_mul_f32 v198, v245, v198
M v[24:27],a[160:163],a[176:179],v[24:27]
s_waitcnt lgkmcnt(4)
T a[144:145],v6,1024
buffer_load_dwordx4 v9, s[40:43], s78 offen offset:128 lds
M v[24:27],a[164:167],a[196:199],v[24:27]
s_cmp_eq_u32 s7, 40960
s_cselect_b32 s67, -40960, s68
v_mul_f32 v199, v245, v199
C v196,v196,v197
M v[56:59],a[164:167],a[200:203],v[56:59]
T a[146:147],v6,1280
C v197,v198,v199
M v[56:59],a[160:163],a[180:183],v[56:59]
s_add_u32 s7, s7, s67
R v198,a44
R v199,a45
M v[88:91],a[160:163],a[184:187],v[88:91]
T a[148:149],v6,9472
R v200,a46
M v[88:91],a[164:167],a[204:207],v[88:91]
buffer_load_dwordx4 v9, s[40:43], s71 offen offset:2176 lds
v_add_u32 v7, s67, v7
M v[120:123],a[164:167],a[208:211],v[120:123]
T a[150:151],v6,9728
R v201,a47
M v[120:123],a[160:163],a[188:191],v[120:123]
v_add_u32 v11, s67, v11
v_mul_f32 v198, v245, v198
M v[152:155],a[160:163],a[192:195],v[152:155]
s_add_u32 s78, s78, 65536
v_mul_f32 v199, v245, v199
v_mul_f32 v200, v245, v200
M v[152:155],a[164:167],a[212:215],v[152:155]
s_waitcnt lgkmcnt(6)
s_add_u32 m0, s70, 4104
v_mul_f32 v201, v245, v201
M v[28:31],a[168:171],a[176:179],v[28:31]
s_waitcnt lgkmcnt(4)
T a[152:153],v6,1032
buffer_load_dwordx4 v9, s[40:43], s78 offen lds
M v[28:31],a[172:175],a[196:199],v[28:31]
C v198,v198,v199
C v199,v200,v201
M v[60:63],a[172:175],a[200:203],v[60:63]
T a[154:155],v6,1288
R v200,a48
M v[60:63],a[168:171],a[180:183],v[60:63]
R v201,a49
R v202,a50
M v[92:95],a[168:171],a[184:187],v[92:95]
T a[156:157],v6,9480
R v203,a51
M v[92:95],a[172:175],a[204:207],v[92:95]
s_add_u32 s71, s78, 129024
buffer_load_dwordx4 v9, s[40:43], s71 offen offset:2048 lds
v_mul_f32 v200, v245, v200
M v[124:127],a[172:175],a[208:211],v[124:127]
T a[158:159],v6,9736
v_mul_f32 v201, v245, v201
M v[124:127],a[168:171],a[188:191],v[124:127]
v_mul_f32 v202, v245, v202
v_mul_f32 v203, v245, v203
M v[156:159],a[168:171],a[192:195],v[156:159]
C v200,v200,v201
C v201,v202,v203
M v[156:159],a[172:175],a[212:215],v[156:159]
s_waitcnt lgkmcnt(6)
s_add_u32 m0, s70, 5000
R v202,a52
M v[32:35],a[144:147],a[176:179],v[32:35]
s_waitcnt lgkmcnt(4)
T a[160:161],v6,1536
buffer_load_dwordx4 v9, s[40:43], s78 offen offset:128 lds
M v[32:35],a[148:151],a[196:199],v[32:35]
R v203,a53
R v204,a54
M v[64:67],a[148:151],a[200:203],v[64:67]
T a[162:163],v6,1792
R v205,a55
M v[64:67],a[144:147],a[180:183],v[64:67]
v_mul_f32 v202, v245, v202
v_mul_f32 v203, v245, v203
M v[96:99],a[144:147],a[184:187],v[96:99]
T a[164:165],v6,9984
v_mul_f32 v204, v245, v204
M v[96:99],a[148:151],a[204:207],v[96:99]
s_add_u32 s78, s78, 458752
buffer_load_dwordx4 v9, s[40:43], s71 offen offset:2176 lds
v_mul_f32 v205, v245, v205
M v[128:131],a[148:151],a[208:211],v[128:131]
T a[166:167],v6,10240
C v202,v202,v203
M v[128:131],a[144:147],a[188:191],v[128:131]
C v203,v204,v205
R v204,a56
M v[160:163],a[144:147],a[192:195],v[160:163]
R v205,a57
R v206,a58
M v[160:163],a[148:151],a[212:215],v[160:163]
s_waitcnt lgkmcnt(6)
R v207,a59
v_mul_f32 v204, v245, v204
M v[36:39],a[152:155],a[176:179],v[36:39]
s_waitcnt lgkmcnt(4)
T a[168:169],v6,1544
v_mul_f32 v205, v245, v205
M v[36:39],a[156:159],a[196:199],v[36:39]
v_mul_f32 v206, v245, v206
v_mul_f32 v207, v245, v207
M v[68:71],a[156:159],a[200:203],v[68:71]
T a[170:171],v6,1800
C v204,v204,v205
M v[68:71],a[152:155],a[180:183],v[68:71]
C v205,v206,v207
R v206,a60
M v[100:103],a[152:155],a[184:187],v[100:103]
T a[172:173],v6,9992
R v207,a61
M v[100:103],a[156:159],a[204:207],v[100:103]
R v208,a62
R v209,a63
M v[132:135],a[156:159],a[208:211],v[132:135]
T a[174:175],v6,10248
v_mul_f32 v206, v245, v206
M v[132:135],a[152:155],a[188:191],v[132:135]
s_mov_b32 s64, s65
v_mul_f32 v207, v245, v207
v_mul_f32 v208, v245, v208
M v[164:167],a[152:155],a[192:195],v[164:167]
s_cmp_eq_u32 s65, 67584
s_cselect_b32 s66, s90, s76
v_mul_f32 v209, v245, v209
M v[164:167],a[156:159],a[212:215],v[164:167]
s_waitcnt lgkmcnt(6)
s_add_u32 s65, s65, s66
C v206,v206,v207
M v[40:43],a[160:163],a[176:179],v[40:43]
s_waitcnt lgkmcnt(4)
C v207,v208,v209
R v208,a64
M v[40:43],a[164:167],a[196:199],v[40:43]
s_waitcnt vmcnt(13) lgkmcnt(0)
v_add_u32 v6, s66, v6
R v209,a65
M v[72:75],a[164:167],a[200:203],v[72:75]
s_barrier
M v[72:75],a[160:163],a[180:183],v[72:75]
T a[144:145],v6,0
R v210,a66
M v[104:107],a[160:163],a[184:187],v[104:107]
s_mov_b32 m0, s75
T a[146:147],v6,256
M v[104:107],a[164:167],a[204:207],v[104:107]
T a[148:149],v6,8448
buffer_load_dwordx4 v10, s[36:39], s79 offen lds
M v[136:139],a[164:167],a[208:211],v[136:139]
T a[150:151],v6,8704
R v211,a67
M v[136:139],a[160:163],a[188:191],v[136:139]
T a[152:153],v6,8
v_mul_f32 v208, v246, v208
M v[168:171],a[160:163],a[192:195],v[168:171]
T a[154:155],v6,264
v_mul_f32 v209, v246, v209
M v[168:171],a[164:167],a[212:215],v[168:171]
v_mul_f32 v210, v246, v210
v_mul_f32 v211, v246, v211
M v[44:47],a[168:171],a[176:179],v[44:47]
s_add_u32 s71, s79, 7168
D a[176:179],v7,0
buffer_load_dwordx4 v10, s[36:39], s71 offen offset:1024 lds
M v[44:47],a[172:175],a[196:199],v[44:47]
D a[196:199],v11,0
C v208,v208,v209
M v[76:79],a[172:175],a[200:203],v[76:79]
D a[200:203],v11,2048
C v209,v210,v211
M v[76:79],a[168:171],a[180:183],v[76:79]
D a[180:183],v7,2048
R v210,a68
M v[108:111],a[168:171],a[184:187],v[108:111]
D a[184:187],v7,4096
R v211,a69
M v[108:111],a[172:175],a[204:207],v[108:111]
s_add_u32 s72, s79, 14336
D a[204:207],v11,4096
buffer_load_dwordx4 v10, s[36:39], s72 offen offset:2048 lds
M v[140:143],a[172:175],a[208:211],v[140:143]
D a[208:211],v11,6144
R v212,a70
M v[140:143],a[168:171],a[188:191],v[140:143]
D a[188:191],v7,6144
R v213,a71
M v[172:175],a[168:171],a[192:195],v[172:175]
D a[192:195],v7,8192
v_mul_f32 v210, v246, v210
M v[172:175],a[172:175],a[212:215],v[172:175]
D a[212:215],v11,8192
v_mul_f32 v211, v246, v211
s_waitcnt lgkmcnt(9)
M v[16:19],a[144:147],a[176:179],v[16:19]
s_waitcnt lgkmcnt(8)
s_add_u32 s71, s79, 21504
T a[160:161],v6,512
buffer_load_dwordx4 v10, s[36:39], s71 offen offset:3072 lds
M v[16:19],a[148:151],a[196:199],v[16:19]
s_waitcnt lgkmcnt(8)
T a[156:157],v6,8456
M v[48:51],a[148:151],a[200:203],v[48:51]
s_waitcnt lgkmcnt(8)
T a[162:163],v6,768
M v[48:51],a[144:147],a[180:183],v[48:51]
s_waitcnt lgkmcnt(8)
s_add_u32 s72, s79, 32768
T a[158:159],v6,8712
v_mul_f32 v212, v246, v212
M v[80:83],a[144:147],a[184:187],v[80:83]
s_waitcnt lgkmcnt(8)
s_add_u32 m0, s75, 4096
T a[164:165],v6,8960
M v[80:83],a[148:151],a[204:207],v[80:83]
s_waitcnt lgkmcnt(8)
buffer_load_dwordx4 v10, s[36:39], s72 offen lds
v_mul_f32 v213, v246, v213
M v[112:115],a[148:151],a[208:211],v[112:115]
s_waitcnt lgkmcnt(7)
s_add_u32 s79, s79, 128
T a[166:167],v6,9216
C v210,v210,v211
M v[112:115],a[144:147],a[188:191],v[112:115]
s_waitcnt lgkmcnt(7)
s_add_u32 s75, s75, 20480
C v211,v212,v213
R v212,a72
M v[144:147],a[144:147],a[192:195],v[144:147]
s_waitcnt lgkmcnt(6)
s_cmp_eq_u32 s75, s95
s_cselect_b32 s75, s80, s75
R v213,a73
R v214,a74
M v[144:147],a[148:151],a[212:215],v[144:147]
s_add_u32 s70, s74, s64
s_mov_b32 m0, s70
M v[20:23],a[152:155],a[176:179],v[20:23]
s_waitcnt lgkmcnt(2)
T a[168:169],v6,520
buffer_load_dwordx4 v9, s[40:43], s78 offen lds
M v[20:23],a[156:159],a[196:199],v[20:23]
R v215,a75
v_mul_f32 v212, v246, v212
M v[52:55],a[156:159],a[200:203],v[52:55]
T a[170:171],v6,776
v_mul_f32 v213, v246, v213
M v[52:55],a[152:155],a[180:183],v[52:55]
v_mul_f32 v214, v246, v214
v_mul_f32 v215, v246, v215
M v[84:87],a[152:155],a[184:187],v[84:87]
T a[172:173],v6,8968
C v212,v212,v213
M v[84:87],a[156:159],a[204:207],v[84:87]
s_add_u32 s71, s78, 129024
buffer_load_dwordx4 v9, s[40:43], s71 offen offset:2048 lds
C v213,v214,v215
M v[116:119],a[156:159],a[208:211],v[116:119]
T a[174:175],v6,9224
R v214,a76
M v[116:119],a[152:155],a[188:191],v[116:119]
R v215,a77
R v216,a78
M v[148:151],a[152:155],a[192:195],v[148:151]
R v217,a79
v_mul_f32 v214, v246, v214
M v[148:151],a[156:159],a[212:215],v[148:151]
s_add_u32 m0, s70, 896
v_mul_f32 v215, v246, v215
M v[24:27],a[160:163],a[176:179],v[24:27]
s_waitcnt lgkmcnt(4)
T a[144:145],v6,1024
buffer_load_dwordx4 v9, s[40:43], s78 offen offset:128 lds
M v[24:27],a[164:167],a[196:199],v[24:27]
s_cmp_eq_u32 s7, 40960
s_cselect_b32 s67, -40960, s68
v_mul_f32 v216, v246, v216
v_mul_f32 v217, v246, v217
M v[56:59],a[164:167],a[200:203],v[56:59]
T a[146:147],v6,1280
C v214,v214,v215
M v[56:59],a[160:163],a[180:183],v[56:59]
s_add_u32 s7, s7, s67
C v215,v216,v217
R v216,a80
M v[88:91],a[160:163],a[184:187],v[88:91]
T a[148:149],v6,9472
R v217,a81
M v[88:91],a[164:167],a[204:207],v[88:91]
buffer_load_dwordx4 v9, s[40:43], s71 offen offset:2176 lds
v_add_u32 v7, s67, v7
M v[120:123],a[164:167],a[208:211],v[120:123]
T a[150:151],v6,9728
R v218,a82
M v[120:123],a[160:163],a[188:191],v[120:123]
v_add_u32 v11, s67, v11
R v219,a83
M v[152:155],a[160:163],a[192:195],v[152:155]
s_add_u32 s78, s78, 65536
v_mul_f32 v216, v246, v216
v_mul_f32 v217, v246, v217
M v[152:155],a[164:167],a[212:215],v[152:155]
s_waitcnt lgkmcnt(6)
s_add_u32 m0, s70, 4104
v_mul_f32 v218, v246, v218
M v[28:31],a[168:171],a[176:179],v[28:31]
s_waitcnt lgkmcnt(4)
T a[152:153],v6,1032
buffer_load_dwordx4 v9, s[40:43], s78 offen lds
M v[28:31],a[172:175],a[196:199],v[28:31]
v_mul_f32 v219, v246, v219
C v216,v216,v217
M v[60:63],a[172:175],a[200:203],v[60:63]
T a[154:155],v6,1288
C v217,v218,v219
M v[60:63],a[168:171],a[180:183],v[60:63]
)"
R"(R v218,a84
R v219,a85
M v[92:95],a[168:171],a[184:187],v[92:95]
T a[156:157],v6,9480
R v220,a86
M v[92:95],a[172:175],a[204:207],v[92:95]
s_add_u32 s71, s78, 129024
buffer_load_dwordx4 v9, s[40:43], s71 offen offset:2048 lds
R v221,a87
M v[124:127],a[172:175],a[208:211],v[124:127]
T a[158:159],v6,9736
v_mul_f32 v218, v246, v218
M v[124:127],a[168:171],a[188:191],v[124:127]
v_mul_f32 v219, v246, v219
v_mul_f32 v220, v246, v220
M v[156:159],a[168:171],a[192:195],v[156:159]
v_mul_f32 v221, v246, v221
C v218,v218,v219
M v[156:159],a[172:175],a[212:215],v[156:159]
s_waitcnt lgkmcnt(6)
s_add_u32 m0, s70, 5000
C v219,v220,v221
M v[32:35],a[144:147],a[176:179],v[32:35]
s_waitcnt lgkmcnt(4)
T a[160:161],v6,1536
buffer_load_dwordx4 v9, s[40:43], s78 offen offset:128 lds
M v[32:35],a[148:151],a[196:199],v[32:35]
R v220,a88
R v221,a89
M v[64:67],a[148:151],a[200:203],v[64:67]
T a[162:163],v6,1792
R v222,a90
M v[64:67],a[144:147],a[180:183],v[64:67]
R v223,a91
v_mul_f32 v220, v246, v220
M v[96:99],a[144:147],a[184:187],v[96:99]
T a[164:165],v6,9984
v_mul_f32 v221, v246, v221
M v[96:99],a[148:151],a[204:207],v[96:99]
s_add_u32 s78, s78, 458752
buffer_load_dwordx4 v9, s[40:43], s71 offen offset:2176 lds
v_mul_f32 v222, v246, v222
M v[128:131],a[148:151],a[208:211],v[128:131]
T a[166:167],v6,10240
v_mul_f32 v223, v246, v223
M v[128:131],a[144:147],a[188:191],v[128:131]
C v220,v220,v221
C v221,v222,v223
M v[160:163],a[144:147],a[192:195],v[160:163]
R v222,a92
R v223,a93
M v[160:163],a[148:151],a[212:215],v[160:163]
s_waitcnt lgkmcnt(6)
R v224,a94
R v225,a95
M v[36:39],a[152:155],a[176:179],v[36:39]
s_waitcnt lgkmcnt(4)
T a[168:169],v6,1544
v_mul_f32 v222, v246, v222
M v[36:39],a[156:159],a[196:199],v[36:39]
v_mul_f32 v223, v246, v223
v_mul_f32 v224, v246, v224
M v[68:71],a[156:159],a[200:203],v[68:71]
T a[170:171],v6,1800
v_mul_f32 v225, v246, v225
M v[68:71],a[152:155],a[180:183],v[68:71]
C v222,v222,v223
C v223,v224,v225
M v[100:103],a[152:155],a[184:187],v[100:103]
T a[172:173],v6,9992
R v224,a96
M v[100:103],a[156:159],a[204:207],v[100:103]
R v225,a97
R v226,a98
M v[132:135],a[156:159],a[208:211],v[132:135]
T a[174:175],v6,10248
R v227,a99
M v[132:135],a[152:155],a[188:191],v[132:135]
s_mov_b32 s64, s65
v_mul_f32 v224, v247, v224
v_mul_f32 v225, v247, v225
M v[164:167],a[152:155],a[192:195],v[164:167]
s_cmp_eq_u32 s65, 67584
s_cselect_b32 s66, s90, s76
v_mul_f32 v226, v247, v226
M v[164:167],a[156:159],a[212:215],v[164:167]
s_waitcnt lgkmcnt(6)
s_add_u32 s65, s65, s66
v_mul_f32 v227, v247, v227
M v[40:43],a[160:163],a[176:179],v[40:43]
s_waitcnt lgkmcnt(4)
C v224,v224,v225
C v225,v226,v227
M v[40:43],a[164:167],a[196:199],v[40:43]
s_waitcnt vmcnt(13) lgkmcnt(0)
v_add_u32 v6, s66, v6
R v226,a100
M v[72:75],a[164:167],a[200:203],v[72:75]
s_barrier
M v[72:75],a[160:163],a[180:183],v[72:75]
T a[144:145],v6,0
R v227,a101
M v[104:107],a[160:163],a[184:187],v[104:107]
s_mov_b32 m0, s75
T a[146:147],v6,256
M v[104:107],a[164:167],a[204:207],v[104:107]
T a[148:149],v6,8448
buffer_load_dwordx4 v10, s[36:39], s79 offen lds
M v[136:139],a[164:167],a[208:211],v[136:139]
T a[150:151],v6,8704
R v228,a102
M v[136:139],a[160:163],a[188:191],v[136:139]
T a[152:153],v6,8
R v229,a103
M v[168:171],a[160:163],a[192:195],v[168:171]
T a[154:155],v6,264
v_mul_f32 v226, v247, v226
M v[168:171],a[164:167],a[212:215],v[168:171]
v_mul_f32 v227, v247, v227
v_mul_f32 v228, v247, v228
M v[44:47],a[168:171],a[176:179],v[44:47]
s_add_u32 s71, s79, 7168
D a[176:179],v7,0
buffer_load_dwordx4 v10, s[36:39], s71 offen offset:1024 lds
M v[44:47],a[172:175],a[196:199],v[44:47]
D a[196:199],v11,0
v_mul_f32 v229, v247, v229
M v[76:79],a[172:175],a[200:203],v[76:79]
D a[200:203],v11,2048
C v226,v226,v227
M v[76:79],a[168:171],a[180:183],v[76:79]
D a[180:183],v7,2048
C v227,v228,v229
M v[108:111],a[168:171],a[184:187],v[108:111]
D a[184:187],v7,4096
R v228,a104
M v[108:111],a[172:175],a[204:207],v[108:111]
s_add_u32 s72, s79, 14336
D a[204:207],v11,4096
buffer_load_dwordx4 v10, s[36:39], s72 offen offset:2048 lds
M v[140:143],a[172:175],a[208:211],v[140:143]
D a[208:211],v11,6144
R v229,a105
M v[140:143],a[168:171],a[188:191],v[140:143]
D a[188:191],v7,6144
R v230,a106
M v[172:175],a[168:171],a[192:195],v[172:175]
D a[192:195],v7,8192
R v231,a107
M v[172:175],a[172:175],a[212:215],v[172:175]
D a[212:215],v11,8192
v_mul_f32 v228, v247, v228
s_waitcnt lgkmcnt(9)
M v[16:19],a[144:147],a[176:179],v[16:19]
s_waitcnt lgkmcnt(8)
s_add_u32 s71, s79, 21504
T a[160:161],v6,512
buffer_load_dwordx4 v10, s[36:39], s71 offen offset:3072 lds
M v[16:19],a[148:151],a[196:199],v[16:19]
s_waitcnt lgkmcnt(8)
T a[156:157],v6,8456
M v[48:51],a[148:151],a[200:203],v[48:51]
s_waitcnt lgkmcnt(8)
T a[162:163],v6,768
M v[48:51],a[144:147],a[180:183],v[48:51]
s_waitcnt lgkmcnt(8)
s_add_u32 s72, s79, 32768
T a[158:159],v6,8712
v_mul_f32 v229, v247, v229
M v[80:83],a[144:147],a[184:187],v[80:83]
s_waitcnt lgkmcnt(8)
s_add_u32 m0, s75, 4096
T a[164:165],v6,8960
M v[80:83],a[148:151],a[204:207],v[80:83]
s_waitcnt lgkmcnt(8)
buffer_load_dwordx4 v10, s[36:39], s72 offen lds
v_mul_f32 v230, v247, v230
M v[112:115],a[148:151],a[208:211],v[112:115]
s_waitcnt lgkmcnt(7)
s_add_u32 s79, s79, 128
T a[166:167],v6,9216
v_mul_f32 v231, v247, v231
M v[112:115],a[144:147],a[188:191],v[112:115]
s_waitcnt lgkmcnt(7)
s_add_u32 s75, s75, 20480
C v228,v228,v229
C v229,v230,v231
M v[144:147],a[144:147],a[192:195],v[144:147]
s_waitcnt lgkmcnt(6)
s_cmp_eq_u32 s75, s95
s_cselect_b32 s75, s80, s75
R v230,a108
R v231,a109
M v[144:147],a[148:151],a[212:215],v[144:147]
s_add_u32 s70, s74, s64
s_mov_b32 m0, s70
M v[20:23],a[152:155],a[176:179],v[20:23]
s_waitcnt lgkmcnt(2)
T a[168:169],v6,520
buffer_load_dwordx4 v9, s[40:43], s78 offen lds
M v[20:23],a[156:159],a[196:199],v[20:23]
R v232,a110
R v233,a111
M v[52:55],a[156:159],a[200:203],v[52:55]
T a[170:171],v6,776
v_mul_f32 v230, v247, v230
M v[52:55],a[152:155],a[180:183],v[52:55]
v_mul_f32 v231, v247, v231
v_mul_f32 v232, v247, v232
M v[84:87],a[152:155],a[184:187],v[84:87]
T a[172:173],v6,8968
v_mul_f32 v233, v247, v233
M v[84:87],a[156:159],a[204:207],v[84:87]
s_add_u32 s71, s78, 129024
buffer_load_dwordx4 v9, s[40:43], s71 offen offset:2048 lds
C v230,v230,v231
M v[116:119],a[156:159],a[208:211],v[116:119]
T a[174:175],v6,9224
C v231,v232,v233
M v[116:119],a[152:155],a[188:191],v[116:119]
R v232,a112
R v233,a113
M v[148:151],a[152:155],a[192:195],v[148:151]
R v234,a114
R v235,a115
M v[148:151],a[156:159],a[212:215],v[148:151]
s_add_u32 m0, s70, 896
v_mul_f32 v232, v247, v232
M v[24:27],a[160:163],a[176:179],v[24:27]
s_waitcnt lgkmcnt(4)
T a[144:145],v6,1024
buffer_load_dwordx4 v9, s[40:43], s78 offen offset:128 lds
M v[24:27],a[164:167],a[196:199],v[24:27]
s_cmp_eq_u32 s7, 40960
s_cselect_b32 s67, -40960, s68
v_mul_f32 v233, v247, v233
v_mul_f32 v234, v247, v234
M v[56:59],a[164:167],a[200:203],v[56:59]
T a[146:147],v6,1280
v_mul_f32 v235, v247, v235
M v[56:59],a[160:163],a[180:183],v[56:59]
s_add_u32 s7, s7, s67
C v232,v232,v233
C v233,v234,v235
M v[88:91],a[160:163],a[184:187],v[88:91]
T a[148:149],v6,9472
R v234,a116
M v[88:91],a[164:167],a[204:207],v[88:91]
buffer_load_dwordx4 v9, s[40:43], s71 offen offset:2176 lds
v_add_u32 v7, s67, v7
M v[120:123],a[164:167],a[208:211],v[120:123]
T a[150:151],v6,9728
R v235,a117
M v[120:123],a[160:163],a[188:191],v[120:123]
v_add_u32 v11, s67, v11
R v236,a118
M v[152:155],a[160:163],a[192:195],v[152:155]
s_add_u32 s78, s78, 65536
R v237,a119
v_mul_f32 v234, v247, v234
M v[152:155],a[164:167],a[212:215],v[152:155]
s_waitcnt lgkmcnt(6)
s_add_u32 m0, s70, 4104
v_mul_f32 v235, v247, v235
M v[28:31],a[168:171],a[176:179],v[28:31]
s_waitcnt lgkmcnt(4)
T a[152:153],v6,1032
buffer_load_dwordx4 v9, s[40:43], s78 offen lds
M v[28:31],a[172:175],a[196:199],v[28:31]
v_mul_f32 v236, v247, v236
v_mul_f32 v237, v247, v237
M v[60:63],a[172:175],a[200:203],v[60:63]
T a[154:155],v6,1288
C v234,v234,v235
M v[60:63],a[168:171],a[180:183],v[60:63]
C v235,v236,v237
R v236,a120
M v[92:95],a[168:171],a[184:187],v[92:95]
T a[156:157],v6,9480
R v237,a121
M v[92:95],a[172:175],a[204:207],v[92:95]
s_add_u32 s71, s78, 129024
buffer_load_dwordx4 v9, s[40:43], s71 offen offset:2048 lds
R v238,a122
M v[124:127],a[172:175],a[208:211],v[124:127]
T a[158:159],v6,9736
R v239,a123
M v[124:127],a[168:171],a[188:191],v[124:127]
v_mul_f32 v236, v247, v236
v_mul_f32 v237, v247, v237
M v[156:159],a[168:171],a[192:195],v[156:159]
v_mul_f32 v238, v247, v238
v_mul_f32 v239, v247, v239
M v[156:159],a[172:175],a[212:215],v[156:159]
s_waitcnt lgkmcnt(6)
s_add_u32 m0, s70, 5000
C v236,v236,v237
M v[32:35],a[144:147],a[176:179],v[32:35]
s_waitcnt lgkmcnt(4)
T a[160:161],v6,1536
buffer_load_dwordx4 v9, s[40:43], s78 offen offset:128 lds
M v[32:35],a[148:151],a[196:199],v[32:35]
C v237,v238,v239
R v238,a124
M v[64:67],a[148:151],a[200:203],v[64:67]
T a[162:163],v6,1792
R v239,a125
M v[64:67],a[144:147],a[180:183],v[64:67]
R v240,a126
R v241,a127
M v[96:99],a[144:147],a[184:187],v[96:99]
T a[164:165],v6,9984
v_mul_f32 v238, v247, v238
M v[96:99],a[148:151],a[204:207],v[96:99]
s_add_u32 s78, s78, 458752
buffer_load_dwordx4 v9, s[40:43], s71 offen offset:2176 lds
v_mul_f32 v239, v247, v239
M v[128:131],a[148:151],a[208:211],v[128:131]
T a[166:167],v6,10240
v_mul_f32 v240, v247, v240
M v[128:131],a[144:147],a[188:191],v[128:131]
v_mul_f32 v241, v247, v241
C v238,v238,v239
M v[160:163],a[144:147],a[192:195],v[160:163]
C v239,v240,v241
R v240,a128
M v[160:163],a[148:151],a[212:215],v[160:163]
s_waitcnt lgkmcnt(6)
R v241,a129
R v242,a130
M v[36:39],a[152:155],a[176:179],v[36:39]
s_waitcnt lgkmcnt(4)
T a[168:169],v6,1544
R v243,a131
M v[36:39],a[156:159],a[196:199],v[36:39]
v_mul_f32 v240, v15, v240
v_mul_f32 v241, v15, v241
M v[68:71],a[156:159],a[200:203],v[68:71]
T a[170:171],v6,1800
v_mul_f32 v242, v15, v242
M v[68:71],a[152:155],a[180:183],v[68:71]
v_mul_f32 v243, v15, v243
C v240,v240,v241
M v[100:103],a[152:155],a[184:187],v[100:103]
T a[172:173],v6,9992
C v241,v242,v243
M v[100:103],a[156:159],a[204:207],v[100:103]
R v242,a132
R v243,a133
M v[132:135],a[156:159],a[208:211],v[132:135]
T a[174:175],v6,10248
R v244,a134
M v[132:135],a[152:155],a[188:191],v[132:135]
s_mov_b32 s64, s65
R v245,a135
v_mul_f32 v242, v15, v242
M v[164:167],a[152:155],a[192:195],v[164:167]
s_cmp_eq_u32 s65, 67584
s_cselect_b32 s66, s90, s76
v_mul_f32 v243, v15, v243
M v[164:167],a[156:159],a[212:215],v[164:167]
s_waitcnt lgkmcnt(6)
s_add_u32 s65, s65, s66
v_mul_f32 v244, v15, v244
M v[40:43],a[160:163],a[176:179],v[40:43]
s_waitcnt lgkmcnt(4)
v_mul_f32 v245, v15, v245
C v242,v242,v243
M v[40:43],a[164:167],a[196:199],v[40:43]
s_waitcnt vmcnt(13) lgkmcnt(0)
v_add_u32 v6, s66, v6
C v243,v244,v245
M v[72:75],a[164:167],a[200:203],v[72:75]
s_barrier
M v[72:75],a[160:163],a[180:183],v[72:75]
T a[144:145],v6,0
R v244,a136
M v[104:107],a[160:163],a[184:187],v[104:107]
s_mov_b32 m0, s75
T a[146:147],v6,256
M v[104:107],a[164:167],a[204:207],v[104:107]
T a[148:149],v6,8448
buffer_load_dwordx4 v10, s[36:39], s79 offen lds
M v[136:139],a[164:167],a[208:211],v[136:139]
T a[150:151],v6,8704
R v245,a137
M v[136:139],a[160:163],a[188:191],v[136:139]
T a[152:153],v6,8
R v246,a138
M v[168:171],a[160:163],a[192:195],v[168:171]
T a[154:155],v6,264
R v247,a139
M v[168:171],a[164:167],a[212:215],v[168:171]
v_mul_f32 v244, v15, v244
v_mul_f32 v245, v15, v245
M v[44:47],a[168:171],a[176:179],v[44:47]
s_add_u32 s71, s79, 7168
D a[176:179],v7,0
buffer_load_dwordx4 v10, s[36:39], s71 offen offset:1024 lds
M v[44:47],a[172:175],a[196:199],v[44:47]
D a[196:199],v11,0
v_mul_f32 v246, v15, v246
M v[76:79],a[172:175],a[200:203],v[76:79]
D a[200:203],v11,2048
v_mul_f32 v247, v15, v247
M v[76:79],a[168:171],a[180:183],v[76:79]
D a[180:183],v7,2048
C v244,v244,v245
M v[108:111],a[168:171],a[184:187],v[108:111]
D a[184:187],v7,4096
C v245,v246,v247
M v[108:111],a[172:175],a[204:207],v[108:111]
s_add_u32 s72, s79, 14336
D a[204:207],v11,4096
buffer_load_dwordx4 v10, s[36:39], s72 offen offset:2048 lds
M v[140:143],a[172:175],a[208:211],v[140:143]
D a[208:211],v11,6144
R v246,a140
M v[140:143],a[168:171],a[188:191],v[140:143]
D a[188:191],v7,6144
R v247,a141
M v[172:175],a[168:171],a[192:195],v[172:175]
D a[192:195],v7,8192
v_mul_f32 v246, v15, v246
M v[172:175],a[172:175],a[212:215],v[172:175]
D a[212:215],v11,8192
v_mul_f32 v247, v15, v247
s_waitcnt lgkmcnt(9)
M v[16:19],a[144:147],a[176:179],v[16:19]
s_waitcnt lgkmcnt(8)
s_add_u32 s71, s79, 21504
T a[160:161],v6,512
buffer_load_dwordx4 v10, s[36:39], s71 offen offset:3072 lds
M v[16:19],a[148:151],a[196:199],v[16:19]
s_waitcnt lgkmcnt(8)
T a[156:157],v6,8456
M v[48:51],a[148:151],a[200:203],v[48:51]
s_waitcnt lgkmcnt(8)
T a[162:163],v6,768
M v[48:51],a[144:147],a[180:183],v[48:51]
s_waitcnt lgkmcnt(8)
s_add_u32 s72, s79, 32768
T a[158:159],v6,8712
C v246,v246,v247
M v[80:83],a[144:147],a[184:187],v[80:83]
s_waitcnt lgkmcnt(8)
s_add_u32 m0, s75, 4096
T a[164:165],v6,8960
M v[80:83],a[148:151],a[204:207],v[80:83]
s_waitcnt lgkmcnt(8)
buffer_load_dwordx4 v10, s[36:39], s72 offen lds
R v247,a142
M v[112:115],a[148:151],a[208:211],v[112:115]
s_waitcnt lgkmcnt(7)
s_add_u32 s79, s79, 128
T a[166:167],v6,9216
R v12,a143
M v[112:115],a[144:147],a[188:191],v[112:115]
s_waitcnt lgkmcnt(7)
s_add_u32 s75, s75, 20480
v_mul_f32 v247, v15, v247
v_mul_f32 v12, v15, v12
M v[144:147],a[144:147],a[192:195],v[144:147]
s_waitcnt lgkmcnt(6)
s_cmp_eq_u32 s75, s95
s_cselect_b32 s75, s80, s75
C v247,v247,v12
M v[144:147],a[148:151],a[212:215],v[144:147]
s_add_u32 s70, s74, s64
s_mov_b32 m0, s70
M v[20:23],a[152:155],a[176:179],v[20:23]
s_waitcnt lgkmcnt(2)
T a[168:169],v6,520
buffer_load_dwordx4 v9, s[40:43], s78 offen lds
M v[20:23],a[156:159],a[196:199],v[20:23]
M v[52:55],a[156:159],a[200:203],v[52:55]
T a[170:171],v6,776
M v[52:55],a[152:155],a[180:183],v[52:55]
M v[84:87],a[152:155],a[184:187],v[84:87]
T a[172:173],v6,8968
M v[84:87],a[156:159],a[204:207],v[84:87]
s_add_u32 s71, s78, 129024
buffer_load_dwordx4 v9, s[40:43], s71 offen offset:2048 lds
M v[116:119],a[156:159],a[208:211],v[116:119]
T a[174:175],v6,9224
M v[116:119],a[152:155],a[188:191],v[116:119]
M v[148:151],a[152:155],a[192:195],v[148:151]
M v[148:151],a[156:159],a[212:215],v[148:151]
s_add_u32 m0, s70, 896
M v[24:27],a[160:163],a[176:179],v[24:27]
s_waitcnt lgkmcnt(4)
T a[144:145],v6,1024
buffer_load_dwordx4 v9, s[40:43], s78 offen offset:128 lds
M v[24:27],a[164:167],a[196:199],v[24:27]
s_cmp_eq_u32 s7, 40960
s_cselect_b32 s67, -40960, s68
M v[56:59],a[164:167],a[200:203],v[56:59]
T a[146:147],v6,1280
M v[56:59],a[160:163],a[180:183],v[56:59]
s_add_u32 s7, s7, s67
)"
R"(M v[88:91],a[160:163],a[184:187],v[88:91]
T a[148:149],v6,9472
M v[88:91],a[164:167],a[204:207],v[88:91]
buffer_load_dwordx4 v9, s[40:43], s71 offen offset:2176 lds
v_add_u32 v7, s67, v7
M v[120:123],a[164:167],a[208:211],v[120:123]
T a[150:151],v6,9728
M v[120:123],a[160:163],a[188:191],v[120:123]
v_add_u32 v11, s67, v11
M v[152:155],a[160:163],a[192:195],v[152:155]
s_add_u32 s78, s78, 65536
M v[152:155],a[164:167],a[212:215],v[152:155]
s_waitcnt lgkmcnt(6)
s_add_u32 m0, s70, 4104
M v[28:31],a[168:171],a[176:179],v[28:31]
s_waitcnt lgkmcnt(4)
T a[152:153],v6,1032
buffer_load_dwordx4 v9, s[40:43], s78 offen lds
M v[28:31],a[172:175],a[196:199],v[28:31]
M v[60:63],a[172:175],a[200:203],v[60:63]
T a[154:155],v6,1288
M v[60:63],a[168:171],a[180:183],v[60:63]
M v[92:95],a[168:171],a[184:187],v[92:95]
T a[156:157],v6,9480
M v[92:95],a[172:175],a[204:207],v[92:95]
s_add_u32 s71, s78, 129024
buffer_load_dwordx4 v9, s[40:43], s71 offen offset:2048 lds
M v[124:127],a[172:175],a[208:211],v[124:127]
T a[158:159],v6,9736
M v[124:127],a[168:171],a[188:191],v[124:127]
M v[156:159],a[168:171],a[192:195],v[156:159]
M v[156:159],a[172:175],a[212:215],v[156:159]
s_waitcnt lgkmcnt(6)
s_add_u32 m0, s70, 5000
M v[32:35],a[144:147],a[176:179],v[32:35]
s_waitcnt lgkmcnt(4)
T a[160:161],v6,1536
buffer_load_dwordx4 v9, s[40:43], s78 offen offset:128 lds
M v[32:35],a[148:151],a[196:199],v[32:35]
M v[64:67],a[148:151],a[200:203],v[64:67]
T a[162:163],v6,1792
M v[64:67],a[144:147],a[180:183],v[64:67]
M v[96:99],a[144:147],a[184:187],v[96:99]
T a[164:165],v6,9984
M v[96:99],a[148:151],a[204:207],v[96:99]
s_add_u32 s78, s78, 458752
buffer_load_dwordx4 v9, s[40:43], s71 offen offset:2176 lds
M v[128:131],a[148:151],a[208:211],v[128:131]
T a[166:167],v6,10240
M v[128:131],a[144:147],a[188:191],v[128:131]
M v[160:163],a[144:147],a[192:195],v[160:163]
M v[160:163],a[148:151],a[212:215],v[160:163]
s_waitcnt lgkmcnt(6)
M v[36:39],a[152:155],a[176:179],v[36:39]
s_waitcnt lgkmcnt(4)
T a[168:169],v6,1544
M v[36:39],a[156:159],a[196:199],v[36:39]
M v[68:71],a[156:159],a[200:203],v[68:71]
T a[170:171],v6,1800
M v[68:71],a[152:155],a[180:183],v[68:71]
M v[100:103],a[152:155],a[184:187],v[100:103]
T a[172:173],v6,9992
M v[100:103],a[156:159],a[204:207],v[100:103]
M v[132:135],a[156:159],a[208:211],v[132:135]
T a[174:175],v6,10248
M v[132:135],a[152:155],a[188:191],v[132:135]
s_mov_b32 s64, s65
M v[164:167],a[152:155],a[192:195],v[164:167]
s_cmp_eq_u32 s65, 67584
s_cselect_b32 s66, s90, s76
M v[164:167],a[156:159],a[212:215],v[164:167]
s_waitcnt lgkmcnt(6)
s_add_u32 s65, s65, s66
M v[40:43],a[160:163],a[176:179],v[40:43]
s_waitcnt lgkmcnt(4)
M v[40:43],a[164:167],a[196:199],v[40:43]
s_waitcnt vmcnt(13) lgkmcnt(0)
v_add_u32 v6, s66, v6
M v[72:75],a[164:167],a[200:203],v[72:75]
s_barrier
M v[72:75],a[160:163],a[180:183],v[72:75]
T a[144:145],v6,0
M v[104:107],a[160:163],a[184:187],v[104:107]
s_mov_b32 m0, s75
T a[146:147],v6,256
M v[104:107],a[164:167],a[204:207],v[104:107]
T a[148:149],v6,8448
buffer_load_dwordx4 v10, s[36:39], s79 offen lds
M v[136:139],a[164:167],a[208:211],v[136:139]
T a[150:151],v6,8704
M v[136:139],a[160:163],a[188:191],v[136:139]
T a[152:153],v6,8
M v[168:171],a[160:163],a[192:195],v[168:171]
T a[154:155],v6,264
M v[168:171],a[164:167],a[212:215],v[168:171]
M v[44:47],a[168:171],a[176:179],v[44:47]
s_add_u32 s71, s79, 7168
D a[176:179],v7,0
buffer_load_dwordx4 v10, s[36:39], s71 offen offset:1024 lds
M v[44:47],a[172:175],a[196:199],v[44:47]
D a[196:199],v11,0
M v[76:79],a[172:175],a[200:203],v[76:79]
D a[200:203],v11,2048
M v[76:79],a[168:171],a[180:183],v[76:79]
D a[180:183],v7,2048
M v[108:111],a[168:171],a[184:187],v[108:111]
D a[184:187],v7,4096
M v[108:111],a[172:175],a[204:207],v[108:111]
s_add_u32 s72, s79, 14336
D a[204:207],v11,4096
buffer_load_dwordx4 v10, s[36:39], s72 offen offset:2048 lds
M v[140:143],a[172:175],a[208:211],v[140:143]
D a[208:211],v11,6144
M v[140:143],a[168:171],a[188:191],v[140:143]
D a[188:191],v7,6144
M v[172:175],a[168:171],a[192:195],v[172:175]
D a[192:195],v7,8192
M v[172:175],a[172:175],a[212:215],v[172:175]
D a[212:215],v11,8192
s_waitcnt lgkmcnt(9)
M v[16:19],a[144:147],a[176:179],v[16:19]
s_waitcnt lgkmcnt(8)
s_add_u32 s71, s79, 21504
T a[160:161],v6,512
buffer_load_dwordx4 v10, s[36:39], s71 offen offset:3072 lds
M v[16:19],a[148:151],a[196:199],v[16:19]
s_waitcnt lgkmcnt(8)
T a[156:157],v6,8456
M v[48:51],a[148:151],a[200:203],v[48:51]
s_waitcnt lgkmcnt(8)
T a[162:163],v6,768
M v[48:51],a[144:147],a[180:183],v[48:51]
s_waitcnt lgkmcnt(8)
s_add_u32 s72, s79, 32768
T a[158:159],v6,8712
M v[80:83],a[144:147],a[184:187],v[80:83]
s_waitcnt lgkmcnt(8)
s_add_u32 m0, s75, 4096
T a[164:165],v6,8960
M v[80:83],a[148:151],a[204:207],v[80:83]
s_waitcnt lgkmcnt(8)
buffer_load_dwordx4 v10, s[36:39], s72 offen lds
M v[112:115],a[148:151],a[208:211],v[112:115]
s_waitcnt lgkmcnt(7)
s_add_u32 s79, s79, 128
T a[166:167],v6,9216
M v[112:115],a[144:147],a[188:191],v[112:115]
s_waitcnt lgkmcnt(7)
s_add_u32 s75, s75, 20480
M v[144:147],a[144:147],a[192:195],v[144:147]
s_waitcnt lgkmcnt(6)
s_cmp_eq_u32 s75, s95
s_cselect_b32 s75, s80, s75
M v[144:147],a[148:151],a[212:215],v[144:147]
s_add_u32 s70, s74, s64
s_mov_b32 m0, s70
M v[20:23],a[152:155],a[176:179],v[20:23]
s_waitcnt lgkmcnt(2)
T a[168:169],v6,520
buffer_load_dwordx4 v9, s[40:43], s78 offen lds
M v[20:23],a[156:159],a[196:199],v[20:23]
M v[52:55],a[156:159],a[200:203],v[52:55]
T a[170:171],v6,776
M v[52:55],a[152:155],a[180:183],v[52:55]
M v[84:87],a[152:155],a[184:187],v[84:87]
T a[172:173],v6,8968
M v[84:87],a[156:159],a[204:207],v[84:87]
s_add_u32 s71, s78, 129024
buffer_load_dwordx4 v9, s[40:43], s71 offen offset:2048 lds
M v[116:119],a[156:159],a[208:211],v[116:119]
T a[174:175],v6,9224
M v[116:119],a[152:155],a[188:191],v[116:119]
M v[148:151],a[152:155],a[192:195],v[148:151]
M v[148:151],a[156:159],a[212:215],v[148:151]
s_add_u32 m0, s70, 896
M v[24:27],a[160:163],a[176:179],v[24:27]
s_waitcnt lgkmcnt(4)
T a[144:145],v6,1024
buffer_load_dwordx4 v9, s[40:43], s78 offen offset:128 lds
M v[24:27],a[164:167],a[196:199],v[24:27]
s_cmp_eq_u32 s7, 40960
s_cselect_b32 s67, -40960, s68
M v[56:59],a[164:167],a[200:203],v[56:59]
T a[146:147],v6,1280
M v[56:59],a[160:163],a[180:183],v[56:59]
s_add_u32 s7, s7, s67
M v[88:91],a[160:163],a[184:187],v[88:91]
T a[148:149],v6,9472
M v[88:91],a[164:167],a[204:207],v[88:91]
buffer_load_dwordx4 v9, s[40:43], s71 offen offset:2176 lds
v_add_u32 v7, s67, v7
M v[120:123],a[164:167],a[208:211],v[120:123]
T a[150:151],v6,9728
M v[120:123],a[160:163],a[188:191],v[120:123]
v_add_u32 v11, s67, v11
M v[152:155],a[160:163],a[192:195],v[152:155]
s_add_u32 s78, s78, 65536
M v[152:155],a[164:167],a[212:215],v[152:155]
s_waitcnt lgkmcnt(6)
s_add_u32 m0, s70, 4104
M v[28:31],a[168:171],a[176:179],v[28:31]
s_waitcnt lgkmcnt(4)
T a[152:153],v6,1032
buffer_load_dwordx4 v9, s[40:43], s78 offen lds
M v[28:31],a[172:175],a[196:199],v[28:31]
M v[60:63],a[172:175],a[200:203],v[60:63]
T a[154:155],v6,1288
M v[60:63],a[168:171],a[180:183],v[60:63]
M v[92:95],a[168:171],a[184:187],v[92:95]
T a[156:157],v6,9480
M v[92:95],a[172:175],a[204:207],v[92:95]
s_add_u32 s71, s78, 129024
buffer_load_dwordx4 v9, s[40:43], s71 offen offset:2048 lds
M v[124:127],a[172:175],a[208:211],v[124:127]
T a[158:159],v6,9736
M v[124:127],a[168:171],a[188:191],v[124:127]
M v[156:159],a[168:171],a[192:195],v[156:159]
M v[156:159],a[172:175],a[212:215],v[156:159]
s_waitcnt lgkmcnt(6)
s_add_u32 m0, s70, 5000
M v[32:35],a[144:147],a[176:179],v[32:35]
s_waitcnt lgkmcnt(4)
T a[160:161],v6,1536
buffer_load_dwordx4 v9, s[40:43], s78 offen offset:128 lds
M v[32:35],a[148:151],a[196:199],v[32:35]
M v[64:67],a[148:151],a[200:203],v[64:67]
T a[162:163],v6,1792
M v[64:67],a[144:147],a[180:183],v[64:67]
M v[96:99],a[144:147],a[184:187],v[96:99]
T a[164:165],v6,9984
M v[96:99],a[148:151],a[204:207],v[96:99]
s_add_u32 s78, s78, 458752
buffer_load_dwordx4 v9, s[40:43], s71 offen offset:2176 lds
M v[128:131],a[148:151],a[208:211],v[128:131]
T a[166:167],v6,10240
M v[128:131],a[144:147],a[188:191],v[128:131]
M v[160:163],a[144:147],a[192:195],v[160:163]
M v[160:163],a[148:151],a[212:215],v[160:163]
s_waitcnt lgkmcnt(6)
M v[36:39],a[152:155],a[176:179],v[36:39]
s_waitcnt lgkmcnt(4)
T a[168:169],v6,1544
M v[36:39],a[156:159],a[196:199],v[36:39]
M v[68:71],a[156:159],a[200:203],v[68:71]
T a[170:171],v6,1800
M v[68:71],a[152:155],a[180:183],v[68:71]
M v[100:103],a[152:155],a[184:187],v[100:103]
T a[172:173],v6,9992
M v[100:103],a[156:159],a[204:207],v[100:103]
M v[132:135],a[156:159],a[208:211],v[132:135]
T a[174:175],v6,10248
M v[132:135],a[152:155],a[188:191],v[132:135]
s_mov_b32 s64, s65
M v[164:167],a[152:155],a[192:195],v[164:167]
s_cmp_eq_u32 s65, 67584
s_cselect_b32 s66, s90, s76
M v[164:167],a[156:159],a[212:215],v[164:167]
s_waitcnt lgkmcnt(6)
s_add_u32 s65, s65, s66
M v[40:43],a[160:163],a[176:179],v[40:43]
s_waitcnt lgkmcnt(4)
M v[40:43],a[164:167],a[196:199],v[40:43]
s_waitcnt vmcnt(13) lgkmcnt(0)
v_add_u32 v6, s66, v6
M v[72:75],a[164:167],a[200:203],v[72:75]
s_barrier
M v[72:75],a[160:163],a[180:183],v[72:75]
s_add_u32 s70, s73, 0
T a[144:145],v6,0
M v[104:107],a[160:163],a[184:187],v[104:107]
s_mov_b32 m0, s70
T a[146:147],v6,256
M v[104:107],a[164:167],a[204:207],v[104:107]
T a[148:149],v6,8448
buffer_load_dwordx4 v8, s[44:47], s6 offen nt lds
M v[136:139],a[164:167],a[208:211],v[136:139]
T a[150:151],v6,8704
M v[136:139],a[160:163],a[188:191],v[136:139]
T a[152:153],v6,8
M v[168:171],a[160:163],a[192:195],v[168:171]
T a[154:155],v6,264
M v[168:171],a[164:167],a[212:215],v[168:171]
M v[44:47],a[168:171],a[176:179],v[44:47]
D a[176:179],v7,0
M v[44:47],a[172:175],a[196:199],v[44:47]
s_add_u32 s77, s77, 8192
D a[196:199],v11,0
buffer_load_dwordx4 v8, s[44:47], s6 offen offset:2048 nt lds
M v[76:79],a[172:175],a[200:203],v[76:79]
D a[200:203],v11,2048
M v[76:79],a[168:171],a[180:183],v[76:79]
D a[180:183],v7,2048
M v[108:111],a[168:171],a[184:187],v[108:111]
D a[184:187],v7,4096
M v[108:111],a[172:175],a[204:207],v[108:111]
D a[204:207],v11,4096
M v[140:143],a[172:175],a[208:211],v[140:143]
s_add_u32 m0, s70, 896
D a[208:211],v11,6144
M v[140:143],a[168:171],a[188:191],v[140:143]
D a[188:191],v7,6144
buffer_load_dwordx4 v8, s[44:47], s6 offen offset:128 nt lds
M v[172:175],a[168:171],a[192:195],v[172:175]
D a[192:195],v7,8192
M v[172:175],a[172:175],a[212:215],v[172:175]
D a[212:215],v11,8192
s_waitcnt lgkmcnt(9)
M v[16:19],a[144:147],a[176:179],v[16:19]
s_waitcnt lgkmcnt(8)
T a[160:161],v6,512
M v[16:19],a[148:151],a[196:199],v[16:19]
s_waitcnt lgkmcnt(8)
T a[156:157],v6,8456
M v[48:51],a[148:151],a[200:203],v[48:51]
s_waitcnt lgkmcnt(8)
T a[162:163],v6,768
M v[48:51],a[144:147],a[180:183],v[48:51]
s_waitcnt lgkmcnt(8)
s_add_u32 s71, s6, 4096
T a[158:159],v6,8712
buffer_load_dwordx4 v8, s[44:47], s6 offen offset:2176 nt lds
M v[80:83],a[144:147],a[184:187],v[80:83]
s_waitcnt lgkmcnt(8)
T a[164:165],v6,8960
M v[80:83],a[148:151],a[204:207],v[80:83]
s_waitcnt lgkmcnt(8)
M v[112:115],a[148:151],a[208:211],v[112:115]
s_waitcnt lgkmcnt(7)
T a[166:167],v6,9216
M v[112:115],a[144:147],a[188:191],v[112:115]
s_waitcnt lgkmcnt(7)
M v[144:147],a[144:147],a[192:195],v[144:147]
s_waitcnt lgkmcnt(6)
s_add_u32 m0, s70, 4096
M v[144:147],a[148:151],a[212:215],v[144:147]
buffer_load_dwordx4 v8, s[44:47], s71 offen nt lds
M v[20:23],a[152:155],a[176:179],v[20:23]
s_waitcnt lgkmcnt(2)
T a[168:169],v6,520
M v[20:23],a[156:159],a[196:199],v[20:23]
M v[52:55],a[156:159],a[200:203],v[52:55]
T a[170:171],v6,776
M v[52:55],a[152:155],a[180:183],v[52:55]
M v[84:87],a[152:155],a[184:187],v[84:87]
T a[172:173],v6,8968
M v[84:87],a[156:159],a[204:207],v[84:87]
s_and_b32 s77, s77, 131071
buffer_load_dwordx4 v8, s[44:47], s71 offen offset:2048 nt lds
M v[116:119],a[156:159],a[208:211],v[116:119]
T a[174:175],v6,9224
M v[116:119],a[152:155],a[188:191],v[116:119]
M v[148:151],a[152:155],a[192:195],v[148:151]
M v[148:151],a[156:159],a[212:215],v[148:151]
M v[24:27],a[160:163],a[176:179],v[24:27]
s_waitcnt lgkmcnt(4)
s_add_u32 m0, s70, 4992
T a[144:145],v6,1024
M v[24:27],a[164:167],a[196:199],v[24:27]
s_cmp_eq_u32 s7, 40960
s_cselect_b32 s67, -40960, s68
buffer_load_dwordx4 v8, s[44:47], s71 offen offset:128 nt lds
M v[56:59],a[164:167],a[200:203],v[56:59]
T a[146:147],v6,1280
M v[56:59],a[160:163],a[180:183],v[56:59]
s_add_u32 s7, s7, s67
M v[88:91],a[160:163],a[184:187],v[88:91]
T a[148:149],v6,9472
M v[88:91],a[164:167],a[204:207],v[88:91]
v_add_u32 v7, s67, v7
M v[120:123],a[164:167],a[208:211],v[120:123]
T a[150:151],v6,9728
M v[120:123],a[160:163],a[188:191],v[120:123]
s_cmp_eq_u32 s77, s4
s_cselect_b32 s5, 2097152, s5
buffer_load_dwordx4 v8, s[44:47], s71 offen offset:2176 nt lds
v_add_u32 v11, s67, v11
M v[152:155],a[160:163],a[192:195],v[152:155]
s_add_u32 s6, s77, s5
M v[152:155],a[164:167],a[212:215],v[152:155]
s_waitcnt lgkmcnt(6)
M v[28:31],a[168:171],a[176:179],v[28:31]
s_waitcnt lgkmcnt(4)
T a[152:153],v6,1032
M v[28:31],a[172:175],a[196:199],v[28:31]
M v[60:63],a[172:175],a[200:203],v[60:63]
T a[154:155],v6,1288
M v[60:63],a[168:171],a[180:183],v[60:63]
M v[92:95],a[168:171],a[184:187],v[92:95]
T a[156:157],v6,9480
M v[92:95],a[172:175],a[204:207],v[92:95]
M v[124:127],a[172:175],a[208:211],v[124:127]
T a[158:159],v6,9736
M v[124:127],a[168:171],a[188:191],v[124:127]
M v[156:159],a[168:171],a[192:195],v[156:159]
M v[156:159],a[172:175],a[212:215],v[156:159]
s_waitcnt lgkmcnt(6)
M v[32:35],a[144:147],a[176:179],v[32:35]
s_waitcnt lgkmcnt(4)
T a[160:161],v6,1536
M v[32:35],a[148:151],a[196:199],v[32:35]
M v[64:67],a[148:151],a[200:203],v[64:67]
T a[162:163],v6,1792
M v[64:67],a[144:147],a[180:183],v[64:67]
M v[96:99],a[144:147],a[184:187],v[96:99]
T a[164:165],v6,9984
M v[96:99],a[148:151],a[204:207],v[96:99]
M v[128:131],a[148:151],a[208:211],v[128:131]
T a[166:167],v6,10240
M v[128:131],a[144:147],a[188:191],v[128:131]
M v[160:163],a[144:147],a[192:195],v[160:163]
M v[160:163],a[148:151],a[212:215],v[160:163]
s_waitcnt lgkmcnt(6)
M v[36:39],a[152:155],a[176:179],v[36:39]
s_waitcnt lgkmcnt(4)
T a[168:169],v6,1544
M v[36:39],a[156:159],a[196:199],v[36:39]
M v[68:71],a[156:159],a[200:203],v[68:71]
T a[170:171],v6,1800
M v[68:71],a[152:155],a[180:183],v[68:71]
M v[100:103],a[152:155],a[184:187],v[100:103]
T a[172:173],v6,9992
M v[100:103],a[156:159],a[204:207],v[100:103]
M v[132:135],a[156:159],a[208:211],v[132:135]
T a[174:175],v6,10248
M v[132:135],a[152:155],a[188:191],v[132:135]
s_mov_b32 s64, s65
M v[164:167],a[152:155],a[192:195],v[164:167]
s_cmp_eq_u32 s65, 67584
s_cselect_b32 s66, s90, s76
M v[164:167],a[156:159],a[212:215],v[164:167]
s_waitcnt lgkmcnt(6)
s_add_u32 s65, s65, s66
M v[40:43],a[160:163],a[176:179],v[40:43]
s_waitcnt lgkmcnt(4)
M v[40:43],a[164:167],a[196:199],v[40:43]
s_waitcnt vmcnt(8) lgkmcnt(0)
v_add_u32 v6, s66, v6
M v[72:75],a[164:167],a[200:203],v[72:75]
s_barrier
M v[72:75],a[160:163],a[180:183],v[72:75]
s_add_u32 s70, s73, 32768
T a[144:145],v6,0
M v[104:107],a[160:163],a[184:187],v[104:107]
s_mov_b32 m0, s70
T a[146:147],v6,256
M v[104:107],a[164:167],a[204:207],v[104:107]
T a[148:149],v6,8448
buffer_load_dwordx4 v8, s[44:47], s6 offen nt lds
M v[136:139],a[164:167],a[208:211],v[136:139]
T a[150:151],v6,8704
M v[136:139],a[160:163],a[188:191],v[136:139]
T a[152:153],v6,8
M v[168:171],a[160:163],a[192:195],v[168:171]
T a[154:155],v6,264
M v[168:171],a[164:167],a[212:215],v[168:171]
M v[44:47],a[168:171],a[176:179],v[44:47]
D a[176:179],v7,0
M v[44:47],a[172:175],a[196:199],v[44:47]
s_add_u32 s77, s77, 8192
D a[196:199],v11,0
buffer_load_dwordx4 v8, s[44:47], s6 offen offset:2048 nt lds
M v[76:79],a[172:175],a[200:203],v[76:79]
D a[200:203],v11,2048
M v[76:79],a[168:171],a[180:183],v[76:79]
D a[180:183],v7,2048
M v[108:111],a[168:171],a[184:187],v[108:111]
D a[184:187],v7,4096
M v[108:111],a[172:175],a[204:207],v[108:111]
D a[204:207],v11,4096
M v[140:143],a[172:175],a[208:211],v[140:143]
s_add_u32 m0, s70, 896
D a[208:211],v11,6144
M v[140:143],a[168:171],a[188:191],v[140:143]
D a[188:191],v7,6144
buffer_load_dwordx4 v8, s[44:47], s6 offen offset:128 nt lds
M v[172:175],a[168:171],a[192:195],v[172:175]
D a[192:195],v7,8192
M v[172:175],a[172:175],a[212:215],v[172:175]
D a[212:215],v11,8192
s_waitcnt lgkmcnt(9)
M v[16:19],a[144:147],a[176:179],v[16:19]
s_waitcnt lgkmcnt(8)
T a[160:161],v6,512
M v[16:19],a[148:151],a[196:199],v[16:19]
s_waitcnt lgkmcnt(8)
T a[156:157],v6,8456
M v[48:51],a[148:151],a[200:203],v[48:51]
s_waitcnt lgkmcnt(8)
T a[162:163],v6,768
M v[48:51],a[144:147],a[180:183],v[48:51]
s_waitcnt lgkmcnt(8)
s_add_u32 s71, s6, 4096
T a[158:159],v6,8712
buffer_load_dwordx4 v8, s[44:47], s6 offen offset:2176 nt lds
M v[80:83],a[144:147],a[184:187],v[80:83]
s_waitcnt lgkmcnt(8)
T a[164:165],v6,8960
M v[80:83],a[148:151],a[204:207],v[80:83]
s_waitcnt lgkmcnt(8)
M v[112:115],a[148:151],a[208:211],v[112:115]
)"
R"(s_waitcnt lgkmcnt(7)
T a[166:167],v6,9216
M v[112:115],a[144:147],a[188:191],v[112:115]
s_waitcnt lgkmcnt(7)
M v[144:147],a[144:147],a[192:195],v[144:147]
s_waitcnt lgkmcnt(6)
s_add_u32 m0, s70, 4096
M v[144:147],a[148:151],a[212:215],v[144:147]
buffer_load_dwordx4 v8, s[44:47], s71 offen nt lds
M v[20:23],a[152:155],a[176:179],v[20:23]
s_waitcnt lgkmcnt(2)
T a[168:169],v6,520
M v[20:23],a[156:159],a[196:199],v[20:23]
M v[52:55],a[156:159],a[200:203],v[52:55]
T a[170:171],v6,776
M v[52:55],a[152:155],a[180:183],v[52:55]
M v[84:87],a[152:155],a[184:187],v[84:87]
T a[172:173],v6,8968
M v[84:87],a[156:159],a[204:207],v[84:87]
s_and_b32 s77, s77, 131071
buffer_load_dwordx4 v8, s[44:47], s71 offen offset:2048 nt lds
M v[116:119],a[156:159],a[208:211],v[116:119]
T a[174:175],v6,9224
M v[116:119],a[152:155],a[188:191],v[116:119]
M v[148:151],a[152:155],a[192:195],v[148:151]
M v[148:151],a[156:159],a[212:215],v[148:151]
M v[24:27],a[160:163],a[176:179],v[24:27]
s_waitcnt lgkmcnt(4)
s_add_u32 m0, s70, 4992
T a[144:145],v6,1024
M v[24:27],a[164:167],a[196:199],v[24:27]
buffer_load_dwordx4 v8, s[44:47], s71 offen offset:128 nt lds
M v[56:59],a[164:167],a[200:203],v[56:59]
T a[146:147],v6,1280
M v[56:59],a[160:163],a[180:183],v[56:59]
M v[88:91],a[160:163],a[184:187],v[88:91]
T a[148:149],v6,9472
M v[88:91],a[164:167],a[204:207],v[88:91]
M v[120:123],a[164:167],a[208:211],v[120:123]
T a[150:151],v6,9728
M v[120:123],a[160:163],a[188:191],v[120:123]
s_cmp_eq_u32 s77, s4
s_cselect_b32 s5, 2097152, s5
buffer_load_dwordx4 v8, s[44:47], s71 offen offset:2176 nt lds
M v[152:155],a[160:163],a[192:195],v[152:155]
s_add_u32 s6, s77, s5
M v[152:155],a[164:167],a[212:215],v[152:155]
s_waitcnt lgkmcnt(6)
M v[28:31],a[168:171],a[176:179],v[28:31]
s_waitcnt lgkmcnt(4)
T a[152:153],v6,1032
M v[28:31],a[172:175],a[196:199],v[28:31]
M v[60:63],a[172:175],a[200:203],v[60:63]
T a[154:155],v6,1288
M v[60:63],a[168:171],a[180:183],v[60:63]
M v[92:95],a[168:171],a[184:187],v[92:95]
T a[156:157],v6,9480
M v[92:95],a[172:175],a[204:207],v[92:95]
M v[124:127],a[172:175],a[208:211],v[124:127]
T a[158:159],v6,9736
M v[124:127],a[168:171],a[188:191],v[124:127]
M v[156:159],a[168:171],a[192:195],v[156:159]
M v[156:159],a[172:175],a[212:215],v[156:159]
s_waitcnt lgkmcnt(6)
M v[32:35],a[144:147],a[176:179],v[32:35]
s_waitcnt lgkmcnt(4)
T a[160:161],v6,1536
M v[32:35],a[148:151],a[196:199],v[32:35]
M v[64:67],a[148:151],a[200:203],v[64:67]
T a[162:163],v6,1792
M v[64:67],a[144:147],a[180:183],v[64:67]
M v[96:99],a[144:147],a[184:187],v[96:99]
T a[164:165],v6,9984
M v[96:99],a[148:151],a[204:207],v[96:99]
M v[128:131],a[148:151],a[208:211],v[128:131]
T a[166:167],v6,10240
M v[128:131],a[144:147],a[188:191],v[128:131]
M v[160:163],a[144:147],a[192:195],v[160:163]
M v[160:163],a[148:151],a[212:215],v[160:163]
s_waitcnt lgkmcnt(6)
M v[36:39],a[152:155],a[176:179],v[36:39]
s_waitcnt lgkmcnt(4)
T a[168:169],v6,1544
M v[36:39],a[156:159],a[196:199],v[36:39]
M v[68:71],a[156:159],a[200:203],v[68:71]
T a[170:171],v6,1800
M v[68:71],a[152:155],a[180:183],v[68:71]
M v[100:103],a[152:155],a[184:187],v[100:103]
T a[172:173],v6,9992
M v[100:103],a[156:159],a[204:207],v[100:103]
M v[132:135],a[156:159],a[208:211],v[132:135]
T a[174:175],v6,10248
M v[132:135],a[152:155],a[188:191],v[132:135]
M v[164:167],a[152:155],a[192:195],v[164:167]
M v[164:167],a[156:159],a[212:215],v[164:167]
s_waitcnt lgkmcnt(6)
M v[40:43],a[160:163],a[176:179],v[40:43]
s_waitcnt lgkmcnt(4)
M v[40:43],a[164:167],a[196:199],v[40:43]
s_waitcnt vmcnt(8) lgkmcnt(0)
M v[72:75],a[164:167],a[200:203],v[72:75]
s_barrier
M v[72:75],a[160:163],a[180:183],v[72:75]
s_add_u32 s70, s73, 65536
M v[104:107],a[160:163],a[184:187],v[104:107]
s_mov_b32 m0, s70
M v[104:107],a[164:167],a[204:207],v[104:107]
buffer_load_dwordx4 v8, s[44:47], s6 offen nt lds
M v[136:139],a[164:167],a[208:211],v[136:139]
M v[136:139],a[160:163],a[188:191],v[136:139]
M v[168:171],a[160:163],a[192:195],v[168:171]
M v[168:171],a[164:167],a[212:215],v[168:171]
s_add_u32 s77, s77, 8192
buffer_load_dwordx4 v8, s[44:47], s6 offen offset:2048 nt lds
M v[44:47],a[168:171],a[176:179],v[44:47]
M v[44:47],a[172:175],a[196:199],v[44:47]
M v[76:79],a[172:175],a[200:203],v[76:79]
s_add_u32 m0, s70, 896
M v[76:79],a[168:171],a[180:183],v[76:79]
buffer_load_dwordx4 v8, s[44:47], s6 offen offset:128 nt lds
M v[108:111],a[168:171],a[184:187],v[108:111]
M v[108:111],a[172:175],a[204:207],v[108:111]
M v[140:143],a[172:175],a[208:211],v[140:143]
M v[140:143],a[168:171],a[188:191],v[140:143]
s_add_u32 s71, s6, 4096
buffer_load_dwordx4 v8, s[44:47], s6 offen offset:2176 nt lds
M v[172:175],a[168:171],a[192:195],v[172:175]
M v[172:175],a[172:175],a[212:215],v[172:175]
D a[160:163],v2,0
D a[164:167],v3,0
D a[168:171],v2,1024
D a[172:175],v3,1024
D a[176:179],v2,4096
D a[180:183],v3,4096
D a[184:187],v2,5120
D a[188:191],v3,5120
s_nop 3
v_pk_mul_f32 v[16:17], v[16:17], s[92:93] op_sel_hi:[1,0]
v_pk_mul_f32 v[18:19], v[18:19], s[92:93] op_sel_hi:[1,0]
C v16,v16,v17
s_add_u32 m0, s70, 4096
s_nop 0
buffer_load_dwordx4 v8, s[44:47], s71 offen nt lds
C v17,v18,v19
v_pk_mul_f32 v[20:21], v[20:21], s[92:93] op_sel_hi:[1,0]
v_pk_mul_f32 v[22:23], v[22:23], s[92:93] op_sel_hi:[1,0]
C v18,v20,v21
C v19,v22,v23
v_pk_mul_f32 v[24:25], v[24:25], s[92:93] op_sel_hi:[1,0]
v_pk_mul_f32 v[26:27], v[26:27], s[92:93] op_sel_hi:[1,0]
C v20,v24,v25
s_and_b32 s77, s77, 131071
buffer_load_dwordx4 v8, s[44:47], s71 offen offset:2048 nt lds
C v21,v26,v27
v_pk_mul_f32 v[28:29], v[28:29], s[92:93] op_sel_hi:[1,0]
v_pk_mul_f32 v[30:31], v[30:31], s[92:93] op_sel_hi:[1,0]
C v22,v28,v29
C v23,v30,v31
v_pk_mul_f32 v[32:33], v[32:33], s[92:93] op_sel_hi:[1,0]
v_pk_mul_f32 v[34:35], v[34:35], s[92:93] op_sel_hi:[1,0]
C v24,v32,v33
s_add_u32 m0, s70, 4992
s_nop 0
buffer_load_dwordx4 v8, s[44:47], s71 offen offset:128 nt lds
C v25,v34,v35
v_pk_mul_f32 v[36:37], v[36:37], s[92:93] op_sel_hi:[1,0]
v_pk_mul_f32 v[38:39], v[38:39], s[92:93] op_sel_hi:[1,0]
C v26,v36,v37
C v27,v38,v39
v_pk_mul_f32 v[40:41], v[40:41], s[92:93] op_sel_hi:[1,0]
v_pk_mul_f32 v[42:43], v[42:43], s[92:93] op_sel_hi:[1,0]
C v28,v40,v41
s_cmp_eq_u32 s77, s4
s_cselect_b32 s5, 2097152, s5
buffer_load_dwordx4 v8, s[44:47], s71 offen offset:2176 nt lds
C v29,v42,v43
v_pk_mul_f32 v[44:45], v[44:45], s[92:93] op_sel_hi:[1,0]
v_pk_mul_f32 v[46:47], v[46:47], s[92:93] op_sel_hi:[1,0]
C v30,v44,v45
C v31,v46,v47
v_pk_mul_f32 v[48:49], v[48:49], s[92:93] op_sel_hi:[1,0]
v_pk_mul_f32 v[50:51], v[50:51], s[92:93] op_sel_hi:[1,0]
C v48,v48,v49
s_add_u32 s6, s77, s5
C v49,v50,v51
v_pk_mul_f32 v[52:53], v[52:53], s[92:93] op_sel_hi:[1,0]
v_pk_mul_f32 v[54:55], v[54:55], s[92:93] op_sel_hi:[1,0]
C v50,v52,v53
C v51,v54,v55
v_pk_mul_f32 v[56:57], v[56:57], s[92:93] op_sel_hi:[1,0]
v_pk_mul_f32 v[58:59], v[58:59], s[92:93] op_sel_hi:[1,0]
C v52,v56,v57
C v53,v58,v59
v_pk_mul_f32 v[60:61], v[60:61], s[92:93] op_sel_hi:[1,0]
v_pk_mul_f32 v[62:63], v[62:63], s[92:93] op_sel_hi:[1,0]
C v54,v60,v61
C v55,v62,v63
v_pk_mul_f32 v[64:65], v[64:65], s[92:93] op_sel_hi:[1,0]
v_pk_mul_f32 v[66:67], v[66:67], s[92:93] op_sel_hi:[1,0]
C v56,v64,v65
C v57,v66,v67
v_pk_mul_f32 v[68:69], v[68:69], s[92:93] op_sel_hi:[1,0]
v_pk_mul_f32 v[70:71], v[70:71], s[92:93] op_sel_hi:[1,0]
C v58,v68,v69
C v59,v70,v71
v_pk_mul_f32 v[72:73], v[72:73], s[92:93] op_sel_hi:[1,0]
v_pk_mul_f32 v[74:75], v[74:75], s[92:93] op_sel_hi:[1,0]
C v60,v72,v73
C v61,v74,v75
v_pk_mul_f32 v[76:77], v[76:77], s[92:93] op_sel_hi:[1,0]
v_pk_mul_f32 v[78:79], v[78:79], s[92:93] op_sel_hi:[1,0]
C v62,v76,v77
C v63,v78,v79
v_pk_mul_f32 v[80:81], v[80:81], s[92:93] op_sel_hi:[1,0]
v_pk_mul_f32 v[82:83], v[82:83], s[92:93] op_sel_hi:[1,0]
C v80,v80,v81
C v81,v82,v83
v_pk_mul_f32 v[84:85], v[84:85], s[92:93] op_sel_hi:[1,0]
v_pk_mul_f32 v[86:87], v[86:87], s[92:93] op_sel_hi:[1,0]
C v82,v84,v85
C v83,v86,v87
v_pk_mul_f32 v[88:89], v[88:89], s[92:93] op_sel_hi:[1,0]
v_pk_mul_f32 v[90:91], v[90:91], s[92:93] op_sel_hi:[1,0]
C v84,v88,v89
C v85,v90,v91
v_pk_mul_f32 v[92:93], v[92:93], s[92:93] op_sel_hi:[1,0]
v_pk_mul_f32 v[94:95], v[94:95], s[92:93] op_sel_hi:[1,0]
C v86,v92,v93
C v87,v94,v95
v_pk_mul_f32 v[96:97], v[96:97], s[92:93] op_sel_hi:[1,0]
v_pk_mul_f32 v[98:99], v[98:99], s[92:93] op_sel_hi:[1,0]
C v88,v96,v97
C v89,v98,v99
v_pk_mul_f32 v[100:101], v[100:101], s[92:93] op_sel_hi:[1,0]
v_pk_mul_f32 v[102:103], v[102:103], s[92:93] op_sel_hi:[1,0]
C v90,v100,v101
C v91,v102,v103
v_pk_mul_f32 v[104:105], v[104:105], s[92:93] op_sel_hi:[1,0]
v_pk_mul_f32 v[106:107], v[106:107], s[92:93] op_sel_hi:[1,0]
C v92,v104,v105
C v93,v106,v107
v_pk_mul_f32 v[108:109], v[108:109], s[92:93] op_sel_hi:[1,0]
v_pk_mul_f32 v[110:111], v[110:111], s[92:93] op_sel_hi:[1,0]
C v94,v108,v109
C v95,v110,v111
v_pk_mul_f32 v[112:113], v[112:113], s[92:93] op_sel_hi:[1,0]
v_pk_mul_f32 v[114:115], v[114:115], s[92:93] op_sel_hi:[1,0]
C v112,v112,v113
C v113,v114,v115
v_pk_mul_f32 v[116:117], v[116:117], s[92:93] op_sel_hi:[1,0]
v_pk_mul_f32 v[118:119], v[118:119], s[92:93] op_sel_hi:[1,0]
C v114,v116,v117
C v115,v118,v119
v_pk_mul_f32 v[120:121], v[120:121], s[92:93] op_sel_hi:[1,0]
v_pk_mul_f32 v[122:123], v[122:123], s[92:93] op_sel_hi:[1,0]
C v116,v120,v121
C v117,v122,v123
v_pk_mul_f32 v[124:125], v[124:125], s[92:93] op_sel_hi:[1,0]
v_pk_mul_f32 v[126:127], v[126:127], s[92:93] op_sel_hi:[1,0]
C v118,v124,v125
C v119,v126,v127
v_pk_mul_f32 v[128:129], v[128:129], s[92:93] op_sel_hi:[1,0]
v_pk_mul_f32 v[130:131], v[130:131], s[92:93] op_sel_hi:[1,0]
C v120,v128,v129
C v121,v130,v131
v_pk_mul_f32 v[132:133], v[132:133], s[92:93] op_sel_hi:[1,0]
v_pk_mul_f32 v[134:135], v[134:135], s[92:93] op_sel_hi:[1,0]
C v122,v132,v133
C v123,v134,v135
v_pk_mul_f32 v[136:137], v[136:137], s[92:93] op_sel_hi:[1,0]
v_pk_mul_f32 v[138:139], v[138:139], s[92:93] op_sel_hi:[1,0]
C v124,v136,v137
C v125,v138,v139
v_pk_mul_f32 v[140:141], v[140:141], s[92:93] op_sel_hi:[1,0]
v_pk_mul_f32 v[142:143], v[142:143], s[92:93] op_sel_hi:[1,0]
C v126,v140,v141
C v127,v142,v143
v_pk_mul_f32 v[144:145], v[144:145], s[92:93] op_sel_hi:[1,0]
v_pk_mul_f32 v[146:147], v[146:147], s[92:93] op_sel_hi:[1,0]
C v144,v144,v145
C v145,v146,v147
v_pk_mul_f32 v[148:149], v[148:149], s[92:93] op_sel_hi:[1,0]
v_pk_mul_f32 v[150:151], v[150:151], s[92:93] op_sel_hi:[1,0]
C v146,v148,v149
C v147,v150,v151
v_pk_mul_f32 v[152:153], v[152:153], s[92:93] op_sel_hi:[1,0]
v_pk_mul_f32 v[154:155], v[154:155], s[92:93] op_sel_hi:[1,0]
C v148,v152,v153
C v149,v154,v155
v_pk_mul_f32 v[156:157], v[156:157], s[92:93] op_sel_hi:[1,0]
v_pk_mul_f32 v[158:159], v[158:159], s[92:93] op_sel_hi:[1,0]
C v150,v156,v157
C v151,v158,v159
v_pk_mul_f32 v[160:161], v[160:161], s[92:93] op_sel_hi:[1,0]
v_pk_mul_f32 v[162:163], v[162:163], s[92:93] op_sel_hi:[1,0]
C v152,v160,v161
C v153,v162,v163
v_pk_mul_f32 v[164:165], v[164:165], s[92:93] op_sel_hi:[1,0]
v_pk_mul_f32 v[166:167], v[166:167], s[92:93] op_sel_hi:[1,0]
C v154,v164,v165
C v155,v166,v167
v_pk_mul_f32 v[168:169], v[168:169], s[92:93] op_sel_hi:[1,0]
v_pk_mul_f32 v[170:171], v[170:171], s[92:93] op_sel_hi:[1,0]
C v156,v168,v169
C v157,v170,v171
v_pk_mul_f32 v[172:173], v[172:173], s[92:93] op_sel_hi:[1,0]
v_pk_mul_f32 v[174:175], v[174:175], s[92:93] op_sel_hi:[1,0]
C v158,v172,v173
C v159,v174,v175
s_mov_b32 s65, 0
s_mov_b32 s76, 32768
s_mov_b32 s90, -131072
s_mov_b32 s95, 32768
s_mov_b32 s80, 98304
s_mov_b32 s7, 0
s_waitcnt lgkmcnt(0)
L_shared:
L_ph_at1S:
s_memrealtime s[18:19]
s_waitcnt lgkmcnt(0)
s_add_u32 s3, s18, 44
M v[36:39],a[160:163],v[16:19],0
T a[192:193],v4,0
M v[36:39],a[164:167],v[20:23],v[36:39]
T a[194:195],v4,4096
M v[36:39],a[168:171],v[24:27],v[36:39]
T a[196:197],v5,0
M v[36:39],a[172:175],v[28:31],v[36:39]
T a[198:199],v5,4096
M v[40:43],a[176:179],v[16:19],0
T a[200:201],v4,512
M v[40:43],a[180:183],v[20:23],v[40:43]
T a[202:203],v4,4608
M v[40:43],a[184:187],v[24:27],v[40:43]
T a[204:205],v5,512
M v[40:43],a[188:191],v[28:31],v[40:43]
T a[206:207],v5,4608
v_max3_f32 v32, v36, v37, v38
T a[208:209],v4,1024
T a[210:211],v4,5120
s_nop 3
v_max3_f32 v33, v40, v41, v42
v_max3_f32 v32, v32, v39, v43
v_max_f32 v32, v32, v33
v_mov_b32 v33, v32
s_nop 1
v_permlane32_swap_b32 v32, v33
v_max_f32 v32, v32, v33
v_mov_b32 v33, v32
s_nop 1
v_permlane16_swap_b32 v32, v33
M v[68:71],a[160:163],v[48:51],0
v_max_f32 v32, v32, v33
v_sub_f32 v33, 0, v32
M v[68:71],a[164:167],v[52:55],v[68:71]
v_add_f32 v36, v33, v36
v_add_f32 v37, v33, v37
M v[68:71],a[168:171],v[56:59],v[68:71]
v_add_f32 v38, v33, v38
v_add_f32 v39, v33, v39
M v[68:71],a[172:175],v[60:63],v[68:71]
E v36,v36
M v[72:75],a[176:179],v[48:51],0
v_add_f32 v40, v33, v40
v_add_f32 v41, v33, v41
M v[72:75],a[180:183],v[52:55],v[72:75]
E v37,v37
M v[72:75],a[184:187],v[56:59],v[72:75]
E v38,v38
M v[72:75],a[188:191],v[60:63],v[72:75]
v_add_f32 v42, v33, v42
v_add_f32 v43, v33, v43
T a[212:213],v5,1024
E v39,v39
v_max3_f32 v64, v68, v69, v70
E v40,v40
v_mov_b32 v32, v33
E v43,v43
v_max3_f32 v65, v72, v73, v74
v_max3_f32 v64, v64, v71, v75
v_max_f32 v64, v64, v65
E v41,v41
E v42,v42
v_mov_b32 v65, v64
s_nop 1
v_permlane32_swap_b32 v64, v65
v_mov_b32 v34, v33
v_mov_b32 v35, v33
C v44,v36,v37
C v45,v38,v39
v_max_f32 v64, v64, v65
v_mov_b32 v65, v64
M v[36:39],a[160:163],v[80:83],0
s_nop 0
v_permlane16_swap_b32 v64, v65
M v[36:39],a[164:167],v[84:87],v[36:39]
v_max_f32 v64, v64, v65
v_sub_f32 v65, 0, v64
M v[36:39],a[168:171],v[88:91],v[36:39]
C v46,v40,v41
C v47,v42,v43
M v[36:39],a[172:175],v[92:95],v[36:39]
T a[222:223],v5,5632
v_add_f32 v68, v65, v68
v_add_f32 v69, v65, v69
M v[40:43],a[176:179],v[80:83],0
T a[214:215],v5,5120
v_add_f32 v70, v65, v70
M v[40:43],a[180:183],v[84:87],v[40:43]
T a[216:217],v4,1536
v_add_f32 v71, v65, v71
M v[40:43],a[184:187],v[88:91],v[40:43]
T a[218:219],v4,5632
v_add_f32 v72, v65, v72
M v[40:43],a[188:191],v[92:95],v[40:43]
s_waitcnt lgkmcnt(13)
T a[220:221],v5,1536
E v68,v68
M a[0:3],a[192:195],v[44:47],0
s_waitcnt lgkmcnt(12)
E v69,v69
M a[4:7],a[196:199],v[44:47],0
s_waitcnt lgkmcnt(10)
v_add_f32 v73, v65, v73
E v70,v70
M a[8:11],a[200:203],v[44:47],0
s_waitcnt lgkmcnt(8)
v_add_f32 v74, v65, v74
E v71,v71
M a[12:15],a[204:207],v[44:47],0
v_add_f32 v75, v65, v75
E v72,v72
v_max3_f32 v96, v36, v37, v38
v_max3_f32 v97, v40, v41, v42
M a[228:231],a[224:227],v[44:47],0
s_waitcnt lgkmcnt(6)
M a[16:19],a[208:211],v[44:47],0
s_waitcnt lgkmcnt(3)
v_mov_b32 v66, v65
E v73,v73
E v75,v75
v_max3_f32 v96, v96, v39, v43
v_max_f32 v96, v96, v97
M a[20:23],a[212:215],v[44:47],0
s_waitcnt lgkmcnt(1)
E v74,v74
v_mov_b32 v97, v96
M a[24:27],a[216:219],v[44:47],0
s_waitcnt lgkmcnt(0)
v_mov_b32 v64, v65
v_permlane32_swap_b32 v96, v97
M a[28:31],a[220:223],v[44:47],0
s_memrealtime s[18:19]
v_mov_b32 v67, v65
C v76,v68,v69
C v77,v70,v71
v_max_f32 v96, v96, v97
v_mov_b32 v97, v96
M v[68:71],a[160:163],v[112:115],0
s_nop 0
v_permlane16_swap_b32 v96, v97
M v[68:71],a[164:167],v[116:119],v[68:71]
v_max_f32 v96, v96, v97
v_sub_f32 v97, 0, v96
M v[68:71],a[168:171],v[120:123],v[68:71]
s_mov_b32 s64, s65
C v78,v72,v73
C v79,v74,v75
M v[68:71],a[172:175],v[124:127],v[68:71]
s_cmp_eq_u32 s65, 131072
s_cselect_b32 s66, s90, s76
v_add_f32 v36, v97, v36
M v[72:75],a[176:179],v[112:115],0
s_add_u32 s65, s65, s66
v_add_f32 v37, v97, v37
v_add_f32 v38, v97, v38
v_mov_b32 v96, v97
M v[72:75],a[180:183],v[116:119],v[72:75]
v_add_f32 v39, v97, v39
v_add_f32 v40, v97, v40
M v[72:75],a[184:187],v[120:123],v[72:75]
s_waitcnt lgkmcnt(0)
E v36,v36
M v[72:75],a[188:191],v[124:127],v[72:75]
s_sub_u32 s19, s3, s18
E v37,v37
M a[32:35],a[192:195],v[76:79],0
s_cmp_gt_i32 s19, 14
s_cbranch_scc1 L_pc1S0
L_pe1S0:
v_add_f32 v41, v97, v41
v_add_f32 v42, v97, v42
v_mov_b32 v99, v97
C v44,v36,v37
M a[36:39],a[196:199],v[76:79],0
s_add_u32 s3, s18, 110
E v38,v38
M a[40:43],a[200:203],v[76:79],0
s_waitcnt vmcnt(8) lgkmcnt(0)
E v39,v39
)"
R"(M a[44:47],a[204:207],v[76:79],0
s_barrier
v_add_f32 v43, v97, v43
E v40,v40
M a[232:235],a[224:227],v[76:79],0
M a[48:51],a[208:211],v[76:79],0
E v41,v41
M a[52:55],a[212:215],v[76:79],0
E v42,v42
M a[56:59],a[216:219],v[76:79],0
E v43,v43
M a[60:63],a[220:223],v[76:79],0
v_mov_b32 v98, v97
C v45,v38,v39
C v46,v40,v41
C v47,v42,v43
v_add_u32 v2, s66, v2
v_add_u32 v3, s66, v3
M v[100:103],a[160:163],v[144:147],0
v_max3_f32 v128, v68, v69, v70
v_max3_f32 v129, v72, v73, v74
v_max3_f32 v128, v128, v71, v75
M v[100:103],a[164:167],v[148:151],v[100:103]
v_max_f32 v128, v128, v129
v_mov_b32 v129, v128
M v[100:103],a[168:171],v[152:155],v[100:103]
s_nop 0
v_permlane32_swap_b32 v128, v129
M v[100:103],a[172:175],v[156:159],v[100:103]
v_max_f32 v128, v128, v129
v_mov_b32 v129, v128
M v[104:107],a[176:179],v[144:147],0
D a[160:163],v2,0
v_permlane16_swap_b32 v128, v129
M v[104:107],a[180:183],v[148:151],v[104:107]
D a[176:179],v2,4096
v_max_f32 v128, v128, v129
v_sub_f32 v129, 0, v128
v_add_f32 v68, v129, v68
v_add_f32 v69, v129, v69
v_add_f32 v70, v129, v70
E v68,v68
M v[104:107],a[184:187],v[152:155],v[104:107]
D a[172:175],v3,1024
v_add_f32 v72, v129, v72
v_add_f32 v73, v129, v73
E v69,v69
M v[104:107],a[188:191],v[156:159],v[104:107]
D a[184:187],v2,5120
v_add_f32 v71, v129, v71
v_add_f32 v74, v129, v74
v_mov_b32 v130, v129
v_add_u32 v5, s66, v5
M a[64:67],a[192:195],v[44:47],0
E v73,v73
E v74,v74
M a[68:71],a[196:199],v[44:47],0
D a[164:167],v3,0
E v70,v70
E v71,v71
E v72,v72
v_max3_f32 v160, v100, v101, v102
M a[72:75],a[200:203],v[44:47],0
D a[168:171],v2,1024
C v76,v68,v69
M a[76:79],a[204:207],v[44:47],0
v_add_f32 v75, v129, v75
E v75,v75
v_max3_f32 v161, v104, v105, v106
v_max3_f32 v160, v160, v103, v107
v_max_f32 v160, v160, v161
v_mov_b32 v161, v160
M a[236:239],a[224:227],v[44:47],0
M a[80:83],a[208:211],v[44:47],0
v_mov_b32 v131, v129
v_permlane32_swap_b32 v160, v161
M a[84:87],a[212:215],v[44:47],0
D a[180:183],v3,4096
C v77,v70,v71
v_max_f32 v160, v160, v161
v_mov_b32 v161, v160
M a[88:91],a[216:219],v[44:47],0
s_nop 0
v_permlane16_swap_b32 v160, v161
M a[92:95],a[220:223],v[44:47],0
s_waitcnt lgkmcnt(6)
D a[188:191],v3,5120
v_mov_b32 v128, v129
v_max_f32 v160, v160, v161
v_sub_f32 v161, 0, v160
M v[36:39],a[160:163],v[16:19],v[32:35]
s_waitcnt lgkmcnt(3)
v_add_f32 v100, v161, v100
v_add_f32 v105, v161, v105
M v[36:39],a[164:167],v[20:23],v[36:39]
s_waitcnt lgkmcnt(2)
C v79,v74,v75
v_add_f32 v102, v161, v102
v_add_f32 v104, v161, v104
E v100,v100
M v[36:39],a[168:171],v[24:27],v[36:39]
v_add_f32 v101, v161, v101
E v101,v101
M v[36:39],a[172:175],v[28:31],v[36:39]
s_add_u32 s64, s65, -98304
C v78,v72,v73
M v[40:43],a[176:179],v[16:19],v[32:35]
s_waitcnt lgkmcnt(1)
s_cmp_lt_i32 s64, 0
s_cselect_b32 s67, 163840, 0
E v102,v102
M v[40:43],a[180:183],v[20:23],v[40:43]
s_add_u32 s64, s64, s67
v_add_f32 v107, v161, v107
M v[40:43],a[184:187],v[24:27],v[40:43]
s_waitcnt lgkmcnt(0)
s_add_u32 s70, s73, s64
v_add_f32 v103, v161, v103
M v[40:43],a[188:191],v[28:31],v[40:43]
s_mov_b32 m0, s70
E v104,v104
M a[96:99],a[192:195],v[76:79],0
buffer_load_dwordx4 v8, s[44:47], s6 offen nt lds
v_mov_b32 v160, v161
M a[100:103],a[196:199],v[76:79],0
v_add_f32 v106, v161, v106
v_mov_b32 v163, v161
M a[104:107],a[200:203],v[76:79],0
E v103,v103
M a[108:111],a[204:207],v[76:79],0
E v105,v105
M a[240:243],a[224:227],v[76:79],0
M a[112:115],a[208:211],v[76:79],0
s_add_u32 s77, s77, 8192
buffer_load_dwordx4 v8, s[44:47], s6 offen offset:2048 nt lds
E v106,v106
M a[116:119],a[212:215],v[76:79],0
E v107,v107
M a[120:123],a[216:219],v[76:79],0
v_mov_b32 v162, v161
C v108,v100,v101
M a[124:127],a[220:223],v[76:79],0
s_add_u32 m0, s70, 896
C v109,v102,v103
C v111,v106,v107
M v[68:71],a[160:163],v[48:51],v[64:67]
buffer_load_dwordx4 v8, s[44:47], s6 offen offset:128 nt lds
C v110,v104,v105
M v[68:71],a[164:167],v[52:55],v[68:71]
E v42,v42
M v[68:71],a[168:171],v[56:59],v[68:71]
E v40,v40
M v[68:71],a[172:175],v[60:63],v[68:71]
E v38,v38
M v[72:75],a[176:179],v[48:51],v[64:67]
s_add_u32 s71, s6, 4096
buffer_load_dwordx4 v8, s[44:47], s6 offen offset:2176 nt lds
v_add_u32 v4, s66, v4
M v[72:75],a[180:183],v[52:55],v[72:75]
E v39,v39
M v[72:75],a[184:187],v[56:59],v[72:75]
E v43,v43
M v[72:75],a[188:191],v[60:63],v[72:75]
s_add_u32 m0, s70, 4096
E v41,v41
M a[128:131],a[192:195],v[108:111],0
buffer_load_dwordx4 v8, s[44:47], s71 offen nt lds
T a[192:193],v4,0
M a[132:135],a[196:199],v[108:111],0
T a[198:199],v5,4096
C v45,v38,v39
M a[136:139],a[200:203],v[108:111],0
E v36,v36
M a[140:143],a[204:207],v[108:111],0
T a[196:197],v5,0
C v46,v40,v41
M a[244:247],a[224:227],v[108:111],0
M a[144:147],a[208:211],v[108:111],0
s_and_b32 s77, s77, 131071
buffer_load_dwordx4 v8, s[44:47], s71 offen offset:2048 nt lds
T a[208:209],v4,1024
M a[148:151],a[212:215],v[108:111],0
T a[194:195],v4,4096
E v37,v37
M a[152:155],a[216:219],v[108:111],0
T a[218:219],v4,5632
C v47,v42,v43
M a[156:159],a[220:223],v[108:111],0
s_add_u32 m0, s70, 4992
T a[204:205],v5,512
C v44,v36,v37
M v[36:39],a[160:163],v[80:83],v[96:99]
buffer_load_dwordx4 v8, s[44:47], s71 offen offset:128 nt lds
T a[220:221],v5,1536
M v[36:39],a[164:167],v[84:87],v[36:39]
T a[200:201],v4,512
T a[202:203],v4,4608
M v[36:39],a[168:171],v[88:91],v[36:39]
T a[210:211],v4,5120
M v[36:39],a[172:175],v[92:95],v[36:39]
T a[206:207],v5,4608
T a[214:215],v5,5120
M v[40:43],a[176:179],v[80:83],v[96:99]
s_cmp_eq_u32 s77, s4
s_cselect_b32 s5, 2097152, s5
buffer_load_dwordx4 v8, s[44:47], s71 offen offset:2176 nt lds
T a[222:223],v5,5632
M v[40:43],a[180:183],v[84:87],v[40:43]
s_add_u32 s6, s77, s5
T a[212:213],v5,1024
T a[216:217],v4,1536
M v[40:43],a[184:187],v[88:91],v[40:43]
E v69,v69
M v[40:43],a[188:191],v[92:95],v[40:43]
s_waitcnt lgkmcnt(11)
E v68,v68
M a[0:3],a[192:195],v[44:47],a[0:3]
E v70,v70
M a[4:7],a[196:199],v[44:47],a[4:7]
s_waitcnt lgkmcnt(6)
E v71,v71
M a[8:11],a[200:203],v[44:47],a[8:11]
s_waitcnt lgkmcnt(4)
E v74,v74
M a[12:15],a[204:207],v[44:47],a[12:15]
E v72,v72
M a[228:231],a[224:227],v[44:47],a[228:231]
M a[16:19],a[208:211],v[44:47],a[16:19]
s_waitcnt lgkmcnt(1)
E v75,v75
M a[20:23],a[212:215],v[44:47],a[20:23]
s_waitcnt lgkmcnt(0)
E v38,v38
M a[24:27],a[216:219],v[44:47],a[24:27]
E v73,v73
M a[28:31],a[220:223],v[44:47],a[28:31]
s_memrealtime s[18:19]
C v76,v68,v69
C v77,v70,v71
M v[68:71],a[160:163],v[112:115],v[128:131]
C v79,v74,v75
M v[68:71],a[164:167],v[116:119],v[68:71]
E v37,v37
M v[68:71],a[168:171],v[120:123],v[68:71]
s_mov_b32 s64, s65
M v[68:71],a[172:175],v[124:127],v[68:71]
s_cmp_eq_u32 s65, 131072
s_cselect_b32 s66, s90, s76
C v78,v72,v73
M v[72:75],a[176:179],v[112:115],v[128:131]
s_add_u32 s65, s65, s66
E v36,v36
M v[72:75],a[180:183],v[116:119],v[72:75]
E v41,v41
M v[72:75],a[184:187],v[120:123],v[72:75]
s_waitcnt lgkmcnt(0)
E v39,v39
M v[72:75],a[188:191],v[124:127],v[72:75]
s_sub_u32 s19, s3, s18
E v43,v43
M a[32:35],a[192:195],v[76:79],a[32:35]
s_cmp_gt_i32 s19, 14
s_cbranch_scc1 L_pc1S1
L_pe1S1:
v_add_u32 v2, s66, v2
M a[36:39],a[196:199],v[76:79],a[36:39]
s_add_u32 s3, s18, 110
E v42,v42
M a[40:43],a[200:203],v[76:79],a[40:43]
s_waitcnt vmcnt(8) lgkmcnt(0)
E v40,v40
M a[44:47],a[204:207],v[76:79],a[44:47]
s_barrier
M a[232:235],a[224:227],v[76:79],a[232:235]
M a[48:51],a[208:211],v[76:79],a[48:51]
C v47,v42,v43
M a[52:55],a[212:215],v[76:79],a[52:55]
C v44,v36,v37
C v46,v40,v41
M a[56:59],a[216:219],v[76:79],a[56:59]
v_add_u32 v3, s66, v3
M a[60:63],a[220:223],v[76:79],a[60:63]
C v45,v38,v39
s_mov_b32 s69, 12
.p2align 3
L_atS_1001:
M v[100:103],a[160:163],v[144:147],v[160:163]
E v72,v72
M v[100:103],a[164:167],v[148:151],v[100:103]
E v71,v71
M v[100:103],a[168:171],v[152:155],v[100:103]
D a[168:171],v2,1024
E v69,v69
M v[100:103],a[172:175],v[156:159],v[100:103]
E v68,v68
M v[104:107],a[176:179],v[144:147],v[160:163]
D a[164:167],v3,0
M v[104:107],a[180:183],v[148:151],v[104:107]
E v70,v70
M v[104:107],a[184:187],v[152:155],v[104:107]
D a[172:175],v3,1024
M v[104:107],a[188:191],v[156:159],v[104:107]
D a[160:163],v2,0
C v76,v68,v69
M a[64:67],a[192:195],v[44:47],a[64:67]
E v73,v73
M a[68:71],a[196:199],v[44:47],a[68:71]
D a[176:179],v2,4096
E v75,v75
M a[72:75],a[200:203],v[44:47],a[72:75]
D a[184:187],v2,5120
C v77,v70,v71
M a[76:79],a[204:207],v[44:47],a[76:79]
E v106,v106
M a[236:239],a[224:227],v[44:47],a[236:239]
M a[80:83],a[208:211],v[44:47],a[80:83]
E v74,v74
M a[84:87],a[212:215],v[44:47],a[84:87]
D a[180:183],v3,4096
C v79,v74,v75
M a[88:91],a[216:219],v[44:47],a[88:91]
D a[188:191],v3,5120
M a[92:95],a[220:223],v[44:47],a[92:95]
s_waitcnt lgkmcnt(4)
E v101,v101
M v[36:39],a[160:163],v[16:19],v[32:35]
C v78,v72,v73
M v[36:39],a[164:167],v[20:23],v[36:39]
E v105,v105
M v[36:39],a[168:171],v[24:27],v[36:39]
E v104,v104
M v[36:39],a[172:175],v[28:31],v[36:39]
s_waitcnt lgkmcnt(3)
s_add_u32 s64, s65, -98304
M v[40:43],a[176:179],v[16:19],v[32:35]
s_waitcnt lgkmcnt(1)
s_cmp_lt_i32 s64, 0
s_cselect_b32 s67, 163840, 0
M v[40:43],a[180:183],v[20:23],v[40:43]
s_add_u32 s64, s64, s67
v_add_u32 v5, s66, v5
M v[40:43],a[184:187],v[24:27],v[40:43]
s_waitcnt lgkmcnt(0)
s_add_u32 s70, s73, s64
v_add_u32 v4, s66, v4
M v[40:43],a[188:191],v[28:31],v[40:43]
s_mov_b32 m0, s70
E v102,v102
M a[96:99],a[192:195],v[76:79],a[96:99]
buffer_load_dwordx4 v8, s[44:47], s6 offen nt lds
M a[100:103],a[196:199],v[76:79],a[100:103]
E v107,v107
M a[104:107],a[200:203],v[76:79],a[104:107]
E v100,v100
M a[108:111],a[204:207],v[76:79],a[108:111]
E v103,v103
M a[240:243],a[224:227],v[76:79],a[240:243]
M a[112:115],a[208:211],v[76:79],a[112:115]
s_add_u32 s77, s77, 8192
buffer_load_dwordx4 v8, s[44:47], s6 offen offset:2048 nt lds
C v110,v104,v105
M a[116:119],a[212:215],v[76:79],a[116:119]
E v42,v42
M a[120:123],a[216:219],v[76:79],a[120:123]
C v108,v100,v101
C v109,v102,v103
M a[124:127],a[220:223],v[76:79],a[124:127]
s_add_u32 m0, s70, 896
E v39,v39
M v[68:71],a[160:163],v[48:51],v[64:67]
buffer_load_dwordx4 v8, s[44:47], s6 offen offset:128 nt lds
C v111,v106,v107
M v[68:71],a[164:167],v[52:55],v[68:71]
E v43,v43
M v[68:71],a[168:171],v[56:59],v[68:71]
E v40,v40
M v[68:71],a[172:175],v[60:63],v[68:71]
E v37,v37
M v[72:75],a[176:179],v[48:51],v[64:67]
s_add_u32 s71, s6, 4096
buffer_load_dwordx4 v8, s[44:47], s6 offen offset:2176 nt lds
E v41,v41
M v[72:75],a[180:183],v[52:55],v[72:75]
E v36,v36
M v[72:75],a[184:187],v[56:59],v[72:75]
E v38,v38
M v[72:75],a[188:191],v[60:63],v[72:75]
s_add_u32 m0, s70, 4096
E v70,v70
M a[128:131],a[192:195],v[108:111],a[128:131]
buffer_load_dwordx4 v8, s[44:47], s71 offen nt lds
T a[192:193],v4,0
M a[132:135],a[196:199],v[108:111],a[132:135]
T a[194:195],v4,4096
M a[136:139],a[200:203],v[108:111],a[136:139]
T a[200:201],v4,512
M a[140:143],a[204:207],v[108:111],a[140:143]
T a[204:205],v5,512
M a[244:247],a[224:227],v[108:111],a[244:247]
M a[144:147],a[208:211],v[108:111],a[144:147]
s_and_b32 s77, s77, 131071
buffer_load_dwordx4 v8, s[44:47], s71 offen offset:2048 nt lds
T a[206:207],v5,4608
M a[148:151],a[212:215],v[108:111],a[148:151]
T a[196:197],v5,0
C v44,v36,v37
M a[152:155],a[216:219],v[108:111],a[152:155]
T a[212:213],v5,1024
C v45,v38,v39
M a[156:159],a[220:223],v[108:111],a[156:159]
s_add_u32 m0, s70, 4992
T a[208:209],v4,1024
C v46,v40,v41
M v[36:39],a[160:163],v[80:83],v[96:99]
buffer_load_dwordx4 v8, s[44:47], s71 offen offset:128 nt lds
T a[210:211],v4,5120
M v[36:39],a[164:167],v[84:87],v[36:39]
T a[198:199],v5,4096
M v[36:39],a[168:171],v[88:91],v[36:39]
T a[202:203],v4,4608
M v[36:39],a[172:175],v[92:95],v[36:39]
T a[216:217],v4,1536
C v47,v42,v43
M v[40:43],a[176:179],v[80:83],v[96:99]
s_cmp_eq_u32 s77, s4
s_cselect_b32 s5, 2097152, s5
buffer_load_dwordx4 v8, s[44:47], s71 offen offset:2176 nt lds
T a[218:219],v4,5632
M v[40:43],a[180:183],v[84:87],v[40:43]
s_add_u32 s6, s77, s5
T a[214:215],v5,5120
M v[40:43],a[184:187],v[88:91],v[40:43]
T a[222:223],v5,5632
M v[40:43],a[188:191],v[92:95],v[40:43]
s_waitcnt lgkmcnt(13)
T a[220:221],v5,1536
M a[0:3],a[192:195],v[44:47],a[0:3]
s_waitcnt lgkmcnt(6)
E v68,v68
M a[4:7],a[196:199],v[44:47],a[4:7]
s_waitcnt lgkmcnt(5)
E v71,v71
M a[8:11],a[200:203],v[44:47],a[8:11]
E v73,v73
M a[12:15],a[204:207],v[44:47],a[12:15]
E v69,v69
M a[228:231],a[224:227],v[44:47],a[228:231]
M a[16:19],a[208:211],v[44:47],a[16:19]
s_waitcnt lgkmcnt(2)
E v72,v72
M a[20:23],a[212:215],v[44:47],a[20:23]
E v74,v74
M a[24:27],a[216:219],v[44:47],a[24:27]
s_waitcnt lgkmcnt(0)
E v75,v75
M a[28:31],a[220:223],v[44:47],a[28:31]
s_memrealtime s[18:19]
C v76,v68,v69
C v77,v70,v71
M v[68:71],a[160:163],v[112:115],v[128:131]
E v41,v41
M v[68:71],a[164:167],v[116:119],v[68:71]
E v38,v38
M v[68:71],a[168:171],v[120:123],v[68:71]
s_mov_b32 s64, s65
C v79,v74,v75
M v[68:71],a[172:175],v[124:127],v[68:71]
s_cmp_eq_u32 s65, 131072
s_cselect_b32 s66, s90, s76
C v78,v72,v73
M v[72:75],a[176:179],v[112:115],v[128:131]
s_add_u32 s65, s65, s66
E v40,v40
M v[72:75],a[180:183],v[116:119],v[72:75]
E v36,v36
M v[72:75],a[184:187],v[120:123],v[72:75]
s_waitcnt lgkmcnt(0)
E v43,v43
M v[72:75],a[188:191],v[124:127],v[72:75]
s_sub_u32 s19, s3, s18
v_add_u32 v2, s66, v2
M a[32:35],a[192:195],v[76:79],a[32:35]
s_cmp_gt_i32 s19, 14
s_cbranch_scc1 L_pc1SL
L_pe1SL:
E v42,v42
M a[36:39],a[196:199],v[76:79],a[36:39]
s_add_u32 s3, s18, 110
v_add_u32 v3, s66, v3
M a[40:43],a[200:203],v[76:79],a[40:43]
s_waitcnt vmcnt(8) lgkmcnt(0)
E v37,v37
M a[44:47],a[204:207],v[76:79],a[44:47]
s_barrier
M a[232:235],a[224:227],v[76:79],a[232:235]
M a[48:51],a[208:211],v[76:79],a[48:51]
C v47,v42,v43
M a[52:55],a[212:215],v[76:79],a[52:55]
s_sub_u32 s69, s69, 1
E v39,v39
)"
R"(M a[56:59],a[216:219],v[76:79],a[56:59]
s_cmp_lg_u32 s69, 0
C v44,v36,v37
C v45,v38,v39
M a[60:63],a[220:223],v[76:79],a[60:63]
C v46,v40,v41
s_cbranch_scc1 L_atS_1001
v_lshlrev_b32 v12, 3, v1
v_bfe_u32 v164, v1, 8, 1
v_lshlrev_b32 v164, 5, v164
v_and_b32 v165, 48, v1
v_xor_b32 v11, v164, v165
v_lshrrev_b32 v164, 9, v1
v_lshl_add_u32 v11, v164, 6, v11
v_bfe_u32 v164, v1, 6, 3
v_lshl_add_u32 v11, v164, 10, v11
s_add_u32 s4, s95, 98304
s_cmp_eq_u32 s4, 163840
s_cselect_b32 s4, 0, s4
s_add_u32 s5, s4, 32768
s_cmp_eq_u32 s5, 163840
s_cselect_b32 s5, 0, s5
s_add_u32 s6, s95, 32768
M v[100:103],a[160:163],v[144:147],v[160:163]
E v71,v71
M v[100:103],a[164:167],v[148:151],v[100:103]
E v68,v68
M v[100:103],a[168:171],v[152:155],v[100:103]
E v74,v74
M v[100:103],a[172:175],v[156:159],v[100:103]
E v69,v69
M v[104:107],a[176:179],v[144:147],v[160:163]
D a[176:179],v2,4096
v_add_u32 v5, s66, v5
M v[104:107],a[180:183],v[148:151],v[104:107]
E v70,v70
M v[104:107],a[184:187],v[152:155],v[104:107]
E v73,v73
M v[104:107],a[188:191],v[156:159],v[104:107]
s_mov_b32 s23, L_ph_oe1S-L_code0
buffer_load_dword v14, v13, s[8:11], s23 offen
D a[172:175],v3,1024
M a[64:67],a[192:195],v[44:47],a[64:67]
s_lshl_b32 s23, s24, 3
s_add_u32 s23, s23, s27
D a[160:163],v2,0
M a[68:71],a[196:199],v[44:47],a[68:71]
s_lshl_b32 s23, s23, 4
D a[164:167],v3,0
C v77,v70,v71
M a[72:75],a[200:203],v[44:47],a[72:75]
s_add_u32 s23, s23, s28
D a[168:171],v2,1024
C v76,v68,v69
M a[76:79],a[204:207],v[44:47],a[76:79]
s_lshl_b32 s23, s23, 13
buffer_load_dword v14, v12, s[48:51], s23 offen
D a[184:187],v2,5120
M a[236:239],a[224:227],v[44:47],a[236:239]
M a[80:83],a[208:211],v[44:47],a[80:83]
s_lshl_b32 s81, s91, 7
D a[180:183],v3,4096
v_add_u32 v4, s66, v4
M a[84:87],a[212:215],v[44:47],a[84:87]
s_add_u32 s81, s81, s93
E v72,v72
M a[88:91],a[216:219],v[44:47],a[88:91]
s_add_u32 m0, s73, s95
E v75,v75
M a[92:95],a[220:223],v[44:47],a[92:95]
s_waitcnt lgkmcnt(4)
buffer_load_dwordx4 v11, s[48:51], s81 offen lds
D a[188:191],v3,5120
M v[36:39],a[160:163],v[16:19],v[32:35]
s_waitcnt lgkmcnt(4)
C v78,v72,v73
C v79,v74,v75
M v[36:39],a[164:167],v[20:23],v[36:39]
s_waitcnt lgkmcnt(3)
E v106,v106
M v[36:39],a[168:171],v[24:27],v[36:39]
E v104,v104
M v[36:39],a[172:175],v[28:31],v[36:39]
s_add_u32 s71, s81, 7168
buffer_load_dwordx4 v11, s[48:51], s71 offen offset:1024 lds
M v[40:43],a[176:179],v[16:19],v[32:35]
s_waitcnt lgkmcnt(1)
E v103,v103
M v[40:43],a[180:183],v[20:23],v[40:43]
E v100,v100
M v[40:43],a[184:187],v[24:27],v[40:43]
s_waitcnt lgkmcnt(0)
E v107,v107
M v[40:43],a[188:191],v[28:31],v[40:43]
s_add_u32 s72, s81, 14336
buffer_load_dwordx4 v11, s[48:51], s72 offen offset:2048 lds
M a[96:99],a[192:195],v[76:79],a[96:99]
E v101,v101
M a[100:103],a[196:199],v[76:79],a[100:103]
E v102,v102
M a[104:107],a[200:203],v[76:79],a[104:107]
E v105,v105
M a[108:111],a[204:207],v[76:79],a[108:111]
s_add_u32 s71, s81, 21504
buffer_load_dwordx4 v11, s[48:51], s71 offen offset:3072 lds
C v111,v106,v107
M a[240:243],a[224:227],v[76:79],a[240:243]
M a[112:115],a[208:211],v[76:79],a[112:115]
E v36,v36
M a[116:119],a[212:215],v[76:79],a[116:119]
s_add_u32 s72, s81, 32768
C v108,v100,v101
C v109,v102,v103
M a[120:123],a[216:219],v[76:79],a[120:123]
s_add_u32 m0, m0, 4096
E v40,v40
M a[124:127],a[220:223],v[76:79],a[124:127]
buffer_load_dwordx4 v11, s[48:51], s72 offen lds
C v110,v104,v105
M v[68:71],a[160:163],v[48:51],v[64:67]
E v39,v39
M v[68:71],a[164:167],v[52:55],v[68:71]
E v37,v37
M v[68:71],a[168:171],v[56:59],v[68:71]
E v41,v41
M v[68:71],a[172:175],v[60:63],v[68:71]
s_add_u32 s71, s81, 39936
buffer_load_dwordx4 v11, s[48:51], s71 offen offset:1024 lds
M v[72:75],a[176:179],v[48:51],v[64:67]
E v42,v42
M v[72:75],a[180:183],v[52:55],v[72:75]
E v43,v43
M v[72:75],a[184:187],v[56:59],v[72:75]
E v38,v38
M v[72:75],a[188:191],v[60:63],v[72:75]
s_add_u32 s72, s81, 47104
buffer_load_dwordx4 v11, s[48:51], s72 offen offset:2048 lds
M a[128:131],a[192:195],v[108:111],a[128:131]
T a[194:195],v4,4096
M a[132:135],a[196:199],v[108:111],a[132:135]
T a[198:199],v5,4096
C v44,v36,v37
M a[136:139],a[200:203],v[108:111],a[136:139]
T a[202:203],v4,4608
M a[140:143],a[204:207],v[108:111],a[140:143]
s_add_u32 s71, s81, 54272
buffer_load_dwordx4 v11, s[48:51], s71 offen offset:3072 lds
T a[200:201],v4,512
M a[244:247],a[224:227],v[108:111],a[244:247]
M a[144:147],a[208:211],v[108:111],a[144:147]
T a[210:211],v4,5120
M a[148:151],a[212:215],v[108:111],a[148:151]
T a[212:213],v5,1024
C v45,v38,v39
M a[152:155],a[216:219],v[108:111],a[152:155]
T a[196:197],v5,0
C v46,v40,v41
M a[156:159],a[220:223],v[108:111],a[156:159]
T a[192:193],v4,0
M v[36:39],a[160:163],v[80:83],v[96:99]
T a[216:217],v4,1536
M v[36:39],a[164:167],v[84:87],v[36:39]
T a[204:205],v5,512
M v[36:39],a[168:171],v[88:91],v[36:39]
T a[206:207],v5,4608
M v[36:39],a[172:175],v[92:95],v[36:39]
T a[214:215],v5,5120
C v47,v42,v43
M v[40:43],a[176:179],v[80:83],v[96:99]
T a[208:209],v4,1024
T a[222:223],v5,5632
M v[40:43],a[180:183],v[84:87],v[40:43]
T a[220:221],v5,1536
M v[40:43],a[184:187],v[88:91],v[40:43]
T a[218:219],v4,5632
M v[40:43],a[188:191],v[92:95],v[40:43]
s_waitcnt lgkmcnt(8)
E v68,v68
M a[0:3],a[192:195],v[44:47],a[0:3]
E v72,v72
M a[4:7],a[196:199],v[44:47],a[4:7]
E v71,v71
M a[8:11],a[200:203],v[44:47],a[8:11]
s_waitcnt lgkmcnt(5)
E v69,v69
M a[12:15],a[204:207],v[44:47],a[12:15]
E v73,v73
M a[228:231],a[224:227],v[44:47],a[228:231]
s_waitcnt lgkmcnt(3)
M a[16:19],a[208:211],v[44:47],a[16:19]
E v75,v75
M a[20:23],a[212:215],v[44:47],a[20:23]
s_waitcnt lgkmcnt(0)
E v74,v74
M a[24:27],a[216:219],v[44:47],a[24:27]
E v70,v70
M a[28:31],a[220:223],v[44:47],a[28:31]
C v76,v68,v69
C v77,v70,v71
M v[68:71],a[160:163],v[112:115],v[128:131]
E v40,v40
M v[68:71],a[164:167],v[116:119],v[68:71]
C v78,v72,v73
C v79,v74,v75
M v[68:71],a[168:171],v[120:123],v[68:71]
s_mov_b32 s64, s65
E v39,v39
M v[68:71],a[172:175],v[124:127],v[68:71]
s_cmp_eq_u32 s65, 131072
s_cselect_b32 s66, s90, s76
M v[72:75],a[176:179],v[112:115],v[128:131]
s_add_u32 s65, s65, s66
E v38,v38
M v[72:75],a[180:183],v[116:119],v[72:75]
E v41,v41
M v[72:75],a[184:187],v[120:123],v[72:75]
E v36,v36
M v[72:75],a[188:191],v[124:127],v[72:75]
E v43,v43
M a[32:35],a[192:195],v[76:79],a[32:35]
M a[36:39],a[196:199],v[76:79],a[36:39]
E v37,v37
M a[40:43],a[200:203],v[76:79],a[40:43]
s_waitcnt vmcnt(10) lgkmcnt(0)
M a[44:47],a[204:207],v[76:79],a[44:47]
s_barrier
M a[232:235],a[224:227],v[76:79],a[232:235]
M a[48:51],a[208:211],v[76:79],a[48:51]
C v44,v36,v37
v_add_u32 v3, s66, v3
M a[52:55],a[212:215],v[76:79],a[52:55]
E v42,v42
M a[56:59],a[216:219],v[76:79],a[56:59]
C v45,v38,v39
C v47,v42,v43
M a[60:63],a[220:223],v[76:79],a[60:63]
C v46,v40,v41
v_add_u32 v2, s66, v2
M v[100:103],a[160:163],v[144:147],v[160:163]
D a[160:163],v2,0
E v72,v72
M v[100:103],a[164:167],v[148:151],v[100:103]
E v70,v70
M v[100:103],a[168:171],v[152:155],v[100:103]
E v75,v75
M v[100:103],a[172:175],v[156:159],v[100:103]
E v69,v69
M v[104:107],a[176:179],v[144:147],v[160:163]
E v68,v68
M v[104:107],a[180:183],v[148:151],v[104:107]
E v71,v71
M v[104:107],a[184:187],v[152:155],v[104:107]
D a[168:171],v2,1024
v_add_u32 v4, s66, v4
M v[104:107],a[188:191],v[156:159],v[104:107]
E v73,v73
M a[64:67],a[192:195],v[44:47],a[64:67]
E v101,v101
M a[68:71],a[196:199],v[44:47],a[68:71]
D a[164:167],v3,0
M a[72:75],a[200:203],v[44:47],a[72:75]
D a[184:187],v2,5120
C v78,v72,v73
M a[76:79],a[204:207],v[44:47],a[76:79]
D a[172:175],v3,1024
C v76,v68,v69
M a[236:239],a[224:227],v[44:47],a[236:239]
M a[80:83],a[208:211],v[44:47],a[80:83]
D a[176:179],v2,4096
v_add_u32 v5, s66, v5
M a[84:87],a[212:215],v[44:47],a[84:87]
D a[180:183],v3,4096
C v77,v70,v71
M a[88:91],a[216:219],v[44:47],a[88:91]
E v74,v74
M a[92:95],a[220:223],v[44:47],a[92:95]
s_waitcnt lgkmcnt(6)
D a[188:191],v3,5120
C v79,v74,v75
M v[36:39],a[160:163],v[16:19],v[32:35]
s_waitcnt lgkmcnt(5)
M v[36:39],a[164:167],v[20:23],v[36:39]
E v105,v105
M v[36:39],a[168:171],v[24:27],v[36:39]
s_waitcnt lgkmcnt(3)
E v102,v102
M v[36:39],a[172:175],v[28:31],v[36:39]
s_waitcnt lgkmcnt(2)
E v103,v103
M v[40:43],a[176:179],v[16:19],v[32:35]
s_waitcnt lgkmcnt(1)
E v106,v106
M v[40:43],a[180:183],v[20:23],v[40:43]
s_add_u32 s81, s81, 2097152
s_add_u32 s91, s91, 1
M v[40:43],a[184:187],v[24:27],v[40:43]
s_waitcnt lgkmcnt(0)
s_and_b32 s91, s91, 7
M v[40:43],a[188:191],v[28:31],v[40:43]
s_add_u32 m0, s73, s6
E v100,v100
M a[96:99],a[192:195],v[76:79],a[96:99]
buffer_load_dwordx4 v11, s[48:51], s81 offen lds
C v108,v100,v101
M a[100:103],a[196:199],v[76:79],a[100:103]
E v104,v104
M a[104:107],a[200:203],v[76:79],a[104:107]
E v107,v107
M a[108:111],a[204:207],v[76:79],a[108:111]
E v37,v37
M a[240:243],a[224:227],v[76:79],a[240:243]
M a[112:115],a[208:211],v[76:79],a[112:115]
s_add_u32 s71, s81, 7168
buffer_load_dwordx4 v11, s[48:51], s71 offen offset:1024 lds
C v109,v102,v103
M a[116:119],a[212:215],v[76:79],a[116:119]
E v39,v39
M a[120:123],a[216:219],v[76:79],a[120:123]
E v43,v43
M a[124:127],a[220:223],v[76:79],a[124:127]
C v110,v104,v105
C v111,v106,v107
M v[68:71],a[160:163],v[48:51],v[64:67]
s_add_u32 s72, s81, 14336
buffer_load_dwordx4 v11, s[48:51], s72 offen offset:2048 lds
M v[68:71],a[164:167],v[52:55],v[68:71]
E v42,v42
M v[68:71],a[168:171],v[56:59],v[68:71]
E v38,v38
M v[68:71],a[172:175],v[60:63],v[68:71]
E v36,v36
M v[72:75],a[176:179],v[48:51],v[64:67]
s_add_u32 s71, s81, 21504
buffer_load_dwordx4 v11, s[48:51], s71 offen offset:3072 lds
M v[72:75],a[180:183],v[52:55],v[72:75]
E v40,v40
M v[72:75],a[184:187],v[56:59],v[72:75]
s_add_u32 s72, s81, 32768
E v41,v41
M v[72:75],a[188:191],v[60:63],v[72:75]
s_add_u32 m0, m0, 4096
E v71,v71
M a[128:131],a[192:195],v[108:111],a[128:131]
buffer_load_dwordx4 v11, s[48:51], s72 offen lds
T a[192:193],v4,0
M a[132:135],a[196:199],v[108:111],a[132:135]
E v70,v70
M a[136:139],a[200:203],v[108:111],a[136:139]
T a[196:197],v5,0
T a[202:203],v4,4608
M a[140:143],a[204:207],v[108:111],a[140:143]
T a[204:205],v5,512
M a[244:247],a[224:227],v[108:111],a[244:247]
M a[144:147],a[208:211],v[108:111],a[144:147]
s_add_u32 s71, s81, 39936
buffer_load_dwordx4 v11, s[48:51], s71 offen offset:1024 lds
T a[206:207],v5,4608
M a[148:151],a[212:215],v[108:111],a[148:151]
T a[208:209],v4,1024
M a[152:155],a[216:219],v[108:111],a[152:155]
T a[218:219],v4,5632
C v45,v38,v39
M a[156:159],a[220:223],v[108:111],a[156:159]
T a[198:199],v5,4096
C v44,v36,v37
M v[36:39],a[160:163],v[80:83],v[96:99]
s_add_u32 s72, s81, 47104
buffer_load_dwordx4 v11, s[48:51], s72 offen offset:2048 lds
T a[194:195],v4,4096
M v[36:39],a[164:167],v[84:87],v[36:39]
T a[200:201],v4,512
M v[36:39],a[168:171],v[88:91],v[36:39]
T a[212:213],v5,1024
C v47,v42,v43
M v[36:39],a[172:175],v[92:95],v[36:39]
T a[216:217],v4,1536
C v46,v40,v41
M v[40:43],a[176:179],v[80:83],v[96:99]
s_add_u32 s71, s81, 54272
buffer_load_dwordx4 v11, s[48:51], s71 offen offset:3072 lds
T a[210:211],v4,5120
M v[40:43],a[180:183],v[84:87],v[40:43]
T a[214:215],v5,5120
M v[40:43],a[184:187],v[88:91],v[40:43]
T a[220:221],v5,1536
M v[40:43],a[188:191],v[92:95],v[40:43]
s_waitcnt lgkmcnt(6)
T a[222:223],v5,5632
M a[0:3],a[192:195],v[44:47],a[0:3]
E v72,v72
M a[4:7],a[196:199],v[44:47],a[4:7]
s_waitcnt lgkmcnt(6)
E v75,v75
M a[8:11],a[200:203],v[44:47],a[8:11]
E v74,v74
M a[12:15],a[204:207],v[44:47],a[12:15]
E v73,v73
M a[228:231],a[224:227],v[44:47],a[228:231]
s_waitcnt lgkmcnt(3)
M a[16:19],a[208:211],v[44:47],a[16:19]
s_waitcnt lgkmcnt(2)
C v77,v70,v71
C v78,v72,v73
M a[20:23],a[212:215],v[44:47],a[20:23]
E v69,v69
M a[24:27],a[216:219],v[44:47],a[24:27]
s_waitcnt lgkmcnt(0)
E v68,v68
M a[28:31],a[220:223],v[44:47],a[28:31]
C v76,v68,v69
M v[68:71],a[160:163],v[112:115],v[128:131]
E v41,v41
M v[68:71],a[164:167],v[116:119],v[68:71]
C v79,v74,v75
M v[68:71],a[168:171],v[120:123],v[68:71]
E v42,v42
M v[68:71],a[172:175],v[124:127],v[68:71]
E v40,v40
M v[72:75],a[176:179],v[112:115],v[128:131]
M v[72:75],a[180:183],v[116:119],v[72:75]
M v[72:75],a[184:187],v[120:123],v[72:75]
C v46,v40,v41
M v[72:75],a[188:191],v[124:127],v[72:75]
M a[32:35],a[192:195],v[76:79],a[32:35]
E v38,v38
M a[36:39],a[196:199],v[76:79],a[36:39]
E v37,v37
M a[40:43],a[200:203],v[76:79],a[40:43]
s_waitcnt lgkmcnt(0)
E v36,v36
M a[44:47],a[204:207],v[76:79],a[44:47]
s_barrier
M a[232:235],a[224:227],v[76:79],a[232:235]
M a[48:51],a[208:211],v[76:79],a[48:51]
E v39,v39
M a[52:55],a[212:215],v[76:79],a[52:55]
C v45,v38,v39
M a[56:59],a[216:219],v[76:79],a[56:59]
E v43,v43
M a[60:63],a[220:223],v[76:79],a[60:63]
C v44,v36,v37
C v47,v42,v43
s_add_u32 s6, s95, 65536
s_mul_i32 s31, s24, 20480
s_add_u32 s31, s31, s80
s_cmp_eq_u32 s24, 3
s_cselect_b32 s31, s7, s31
v_add_u32 v12, s31, v1
s_xor_b32 s23, s24, 2
s_mul_i32 s31, s23, 20480
s_add_u32 s31, s31, s80
s_cmp_eq_u32 s23, 3
s_cselect_b32 s31, s7, s31
v_add_u32 v9, s31, v1
M v[100:103],a[160:163],v[144:147],v[160:163]
ds_write_b128 v12, v[176:179] offset:0
M v[100:103],a[164:167],v[148:151],v[100:103]
E v68,v68
M v[100:103],a[168:171],v[152:155],v[100:103]
E v74,v74
M v[100:103],a[172:175],v[156:159],v[100:103]
E v71,v71
E v73,v73
M v[104:107],a[176:179],v[144:147],v[160:163]
E v72,v72
E v75,v75
M v[104:107],a[180:183],v[148:151],v[104:107]
E v69,v69
E v70,v70
M v[104:107],a[184:187],v[152:155],v[104:107]
ds_write_b128 v12, v[180:183] offset:1024
M v[104:107],a[188:191],v[156:159],v[104:107]
E v102,v102
M a[64:67],a[192:195],v[44:47],a[64:67]
C v76,v68,v69
C v77,v70,v71
M a[68:71],a[196:199],v[44:47],a[68:71]
ds_write_b128 v12, v[184:187] offset:2048
M a[72:75],a[200:203],v[44:47],a[72:75]
ds_write_b128 v12, v[192:195] offset:4096
C v79,v74,v75
M a[76:79],a[204:207],v[44:47],a[76:79]
ds_write_b128 v12, v[188:191] offset:3072
M a[236:239],a[224:227],v[44:47],a[236:239]
M a[80:83],a[208:211],v[44:47],a[80:83]
E v100,v100
M a[84:87],a[212:215],v[44:47],a[84:87]
ds_write_b128 v12, v[196:199] offset:5120
M a[88:91],a[216:219],v[44:47],a[88:91]
ds_write_b128 v12, v[200:203] offset:6144
M a[92:95],a[220:223],v[44:47],a[92:95]
ds_write_b128 v12, v[204:207] offset:7168
E v107,v107
ds_write_b128 v12, v[212:215] offset:9216
C v78,v72,v73
E v103,v103
E v104,v104
ds_write_b128 v12, v[208:211] offset:8192
ds_write_b128 v12, v[216:219] offset:10240
M a[96:99],a[192:195],v[76:79],a[96:99]
)"
R"(E v105,v105
C v109,v102,v103
M a[100:103],a[196:199],v[76:79],a[100:103]
ds_write_b128 v12, v[220:223] offset:11264
M a[104:107],a[200:203],v[76:79],a[104:107]
E v101,v101
M a[108:111],a[204:207],v[76:79],a[108:111]
E v106,v106
M a[240:243],a[224:227],v[76:79],a[240:243]
M a[112:115],a[208:211],v[76:79],a[112:115]
ds_write_b128 v12, v[224:227] offset:12288
M a[116:119],a[212:215],v[76:79],a[116:119]
ds_write_b128 v12, v[228:231] offset:13312
M a[120:123],a[216:219],v[76:79],a[120:123]
ds_write_b128 v12, v[232:235] offset:14336
M a[124:127],a[220:223],v[76:79],a[124:127]
ds_write_b128 v12, v[236:239] offset:15360
C v108,v100,v101
C v111,v106,v107
C v110,v104,v105
s_nop 1
M a[128:131],a[192:195],v[108:111],a[128:131]
ds_write_b128 v12, v[240:243] offset:16384
M a[132:135],a[196:199],v[108:111],a[132:135]
ds_write_b128 v12, v[244:247] offset:17408
M a[136:139],a[200:203],v[108:111],a[136:139]
ds_write_b128 v12, v[248:251] offset:18432
M a[140:143],a[204:207],v[108:111],a[140:143]
M a[244:247],a[224:227],v[108:111],a[244:247]
M a[144:147],a[208:211],v[108:111],a[144:147]
M a[148:151],a[212:215],v[108:111],a[148:151]
ds_write_b128 v12, v[252:255] offset:19456
M a[152:155],a[216:219],v[108:111],a[152:155]
M a[156:159],a[220:223],v[108:111],a[156:159]
L_ph_oe1S:
s_mov_b64 s[98:99], 0
s_mov_b32 s31, 0x5d800000
R v132,a228
R v133,a232
R v134,a236
R v135,a240
R v136,a244
v_max3_f32 v137, v132, v133, v134
v_max3_f32 v137, v137, v135, v136
v_cmp_ngt_f32 vcc, s31, v137
v_cmp_u_f32 s[98:99], v132, v133
s_nop 1
s_or_b64 vcc, vcc, s[98:99]
v_cmp_u_f32 s[98:99], v134, v135
s_nop 1
s_or_b64 vcc, vcc, s[98:99]
v_cmp_u_f32 s[98:99], v136, v136
s_nop 1
s_or_b64 vcc, vcc, s[98:99]
s_cbranch_scc0 L_ret1S
s_mov_b32 s97, 2097152
s_add_u32 s31, s8, L_slow0-L_code0-4
s_addc_u32 s23, s9, 0
s_mov_b32 s18, s31
s_mov_b32 s19, s23
s_swappc_b64 s[98:99], s[18:19]
L_ret1S:
s_waitcnt lgkmcnt(0)
s_barrier
D a[176:179],v9,0
D a[180:183],v9,1024
D a[184:187],v9,2048
D a[188:191],v9,3072
D a[192:195],v9,4096
D a[196:199],v9,5120
D a[200:203],v9,6144
D a[204:207],v9,7168
D a[208:211],v9,8192
D a[212:215],v9,9216
D a[216:219],v9,10240
D a[220:223],v9,11264
D a[224:227],v9,12288
D a[228:231],v9,13312
D a[232:235],v9,14336
D a[236:239],v9,15360
D a[240:243],v9,16384
D a[244:247],v9,17408
D a[248:251],v9,18432
D a[252:255],v9,19456
v_rcp_f32 v32, v132
v_rcp_f32 v64, v133
v_rcp_f32 v96, v134
v_rcp_f32 v128, v135
v_rcp_f32 v160, v136
R v164,a0
R v165,a1
R v166,a2
R v167,a3
v_pk_mul_f32 v[164:165], v[164:165], v[32:33] op_sel_hi:[1,0]
v_pk_mul_f32 v[166:167], v[166:167], v[32:33] op_sel_hi:[1,0]
C v16,v164,v165
C v17,v166,v167
R v168,a4
R v169,a5
R v170,a6
R v171,a7
v_pk_mul_f32 v[168:169], v[168:169], v[32:33] op_sel_hi:[1,0]
v_pk_mul_f32 v[170:171], v[170:171], v[32:33] op_sel_hi:[1,0]
C v18,v168,v169
C v19,v170,v171
R v172,a8
R v173,a9
R v174,a10
R v175,a11
v_pk_mul_f32 v[172:173], v[172:173], v[32:33] op_sel_hi:[1,0]
v_pk_mul_f32 v[174:175], v[174:175], v[32:33] op_sel_hi:[1,0]
C v20,v172,v173
C v21,v174,v175
R v164,a12
R v165,a13
R v166,a14
R v167,a15
v_pk_mul_f32 v[164:165], v[164:165], v[32:33] op_sel_hi:[1,0]
v_pk_mul_f32 v[166:167], v[166:167], v[32:33] op_sel_hi:[1,0]
C v22,v164,v165
C v23,v166,v167
R v168,a16
R v169,a17
R v170,a18
R v171,a19
v_pk_mul_f32 v[168:169], v[168:169], v[32:33] op_sel_hi:[1,0]
v_pk_mul_f32 v[170:171], v[170:171], v[32:33] op_sel_hi:[1,0]
C v24,v168,v169
C v25,v170,v171
R v172,a20
R v173,a21
R v174,a22
R v175,a23
v_pk_mul_f32 v[172:173], v[172:173], v[32:33] op_sel_hi:[1,0]
v_pk_mul_f32 v[174:175], v[174:175], v[32:33] op_sel_hi:[1,0]
C v26,v172,v173
C v27,v174,v175
s_waitcnt lgkmcnt(0)
s_barrier
ds_write_b128 v12, v[16:19] offset:0
ds_write_b128 v12, v[20:23] offset:1024
ds_write_b128 v12, v[24:27] offset:2048
R v164,a24
R v165,a25
R v166,a26
R v167,a27
v_pk_mul_f32 v[164:165], v[164:165], v[32:33] op_sel_hi:[1,0]
v_pk_mul_f32 v[166:167], v[166:167], v[32:33] op_sel_hi:[1,0]
C v28,v164,v165
C v29,v166,v167
R v168,a28
R v169,a29
R v170,a30
R v171,a31
v_pk_mul_f32 v[168:169], v[168:169], v[32:33] op_sel_hi:[1,0]
v_pk_mul_f32 v[170:171], v[170:171], v[32:33] op_sel_hi:[1,0]
C v30,v168,v169
C v31,v170,v171
ds_write_b128 v12, v[28:31] offset:3072
R v172,a32
R v173,a33
R v174,a34
R v175,a35
v_pk_mul_f32 v[172:173], v[172:173], v[64:65] op_sel_hi:[1,0]
v_pk_mul_f32 v[174:175], v[174:175], v[64:65] op_sel_hi:[1,0]
C v48,v172,v173
C v49,v174,v175
R v164,a36
R v165,a37
R v166,a38
R v167,a39
v_pk_mul_f32 v[164:165], v[164:165], v[64:65] op_sel_hi:[1,0]
v_pk_mul_f32 v[166:167], v[166:167], v[64:65] op_sel_hi:[1,0]
C v50,v164,v165
C v51,v166,v167
ds_write_b128 v12, v[48:51] offset:4096
R v168,a40
R v169,a41
R v170,a42
R v171,a43
v_pk_mul_f32 v[168:169], v[168:169], v[64:65] op_sel_hi:[1,0]
v_pk_mul_f32 v[170:171], v[170:171], v[64:65] op_sel_hi:[1,0]
C v52,v168,v169
C v53,v170,v171
R v172,a44
R v173,a45
R v174,a46
R v175,a47
v_pk_mul_f32 v[172:173], v[172:173], v[64:65] op_sel_hi:[1,0]
v_pk_mul_f32 v[174:175], v[174:175], v[64:65] op_sel_hi:[1,0]
C v54,v172,v173
C v55,v174,v175
ds_write_b128 v12, v[52:55] offset:5120
R v164,a48
R v165,a49
R v166,a50
R v167,a51
v_pk_mul_f32 v[164:165], v[164:165], v[64:65] op_sel_hi:[1,0]
v_pk_mul_f32 v[166:167], v[166:167], v[64:65] op_sel_hi:[1,0]
C v56,v164,v165
C v57,v166,v167
R v168,a52
R v169,a53
R v170,a54
R v171,a55
v_pk_mul_f32 v[168:169], v[168:169], v[64:65] op_sel_hi:[1,0]
v_pk_mul_f32 v[170:171], v[170:171], v[64:65] op_sel_hi:[1,0]
C v58,v168,v169
C v59,v170,v171
ds_write_b128 v12, v[56:59] offset:6144
R v172,a56
R v173,a57
R v174,a58
R v175,a59
v_pk_mul_f32 v[172:173], v[172:173], v[64:65] op_sel_hi:[1,0]
v_pk_mul_f32 v[174:175], v[174:175], v[64:65] op_sel_hi:[1,0]
C v60,v172,v173
C v61,v174,v175
R v164,a60
R v165,a61
R v166,a62
R v167,a63
v_pk_mul_f32 v[164:165], v[164:165], v[64:65] op_sel_hi:[1,0]
v_pk_mul_f32 v[166:167], v[166:167], v[64:65] op_sel_hi:[1,0]
C v62,v164,v165
C v63,v166,v167
ds_write_b128 v12, v[60:63] offset:7168
R v168,a64
R v169,a65
R v170,a66
R v171,a67
v_pk_mul_f32 v[168:169], v[168:169], v[96:97] op_sel_hi:[1,0]
v_pk_mul_f32 v[170:171], v[170:171], v[96:97] op_sel_hi:[1,0]
C v80,v168,v169
C v81,v170,v171
R v172,a68
R v173,a69
R v174,a70
R v175,a71
v_pk_mul_f32 v[172:173], v[172:173], v[96:97] op_sel_hi:[1,0]
v_pk_mul_f32 v[174:175], v[174:175], v[96:97] op_sel_hi:[1,0]
C v82,v172,v173
C v83,v174,v175
ds_write_b128 v12, v[80:83] offset:8192
R v164,a72
R v165,a73
R v166,a74
R v167,a75
v_pk_mul_f32 v[164:165], v[164:165], v[96:97] op_sel_hi:[1,0]
v_pk_mul_f32 v[166:167], v[166:167], v[96:97] op_sel_hi:[1,0]
C v84,v164,v165
C v85,v166,v167
R v168,a76
R v169,a77
R v170,a78
R v171,a79
v_pk_mul_f32 v[168:169], v[168:169], v[96:97] op_sel_hi:[1,0]
v_pk_mul_f32 v[170:171], v[170:171], v[96:97] op_sel_hi:[1,0]
C v86,v168,v169
C v87,v170,v171
ds_write_b128 v12, v[84:87] offset:9216
R v172,a80
R v173,a81
R v174,a82
R v175,a83
v_pk_mul_f32 v[172:173], v[172:173], v[96:97] op_sel_hi:[1,0]
v_pk_mul_f32 v[174:175], v[174:175], v[96:97] op_sel_hi:[1,0]
C v88,v172,v173
C v89,v174,v175
R v164,a84
R v165,a85
R v166,a86
R v167,a87
v_pk_mul_f32 v[164:165], v[164:165], v[96:97] op_sel_hi:[1,0]
v_pk_mul_f32 v[166:167], v[166:167], v[96:97] op_sel_hi:[1,0]
C v90,v164,v165
C v91,v166,v167
ds_write_b128 v12, v[88:91] offset:10240
R v168,a88
R v169,a89
R v170,a90
R v171,a91
v_pk_mul_f32 v[168:169], v[168:169], v[96:97] op_sel_hi:[1,0]
v_pk_mul_f32 v[170:171], v[170:171], v[96:97] op_sel_hi:[1,0]
C v92,v168,v169
C v93,v170,v171
R v172,a92
R v173,a93
R v174,a94
R v175,a95
v_pk_mul_f32 v[172:173], v[172:173], v[96:97] op_sel_hi:[1,0]
v_pk_mul_f32 v[174:175], v[174:175], v[96:97] op_sel_hi:[1,0]
C v94,v172,v173
C v95,v174,v175
ds_write_b128 v12, v[92:95] offset:11264
R v164,a96
R v165,a97
R v166,a98
R v167,a99
v_pk_mul_f32 v[164:165], v[164:165], v[128:129] op_sel_hi:[1,0]
v_pk_mul_f32 v[166:167], v[166:167], v[128:129] op_sel_hi:[1,0]
C v112,v164,v165
C v113,v166,v167
R v168,a100
R v169,a101
R v170,a102
R v171,a103
v_pk_mul_f32 v[168:169], v[168:169], v[128:129] op_sel_hi:[1,0]
v_pk_mul_f32 v[170:171], v[170:171], v[128:129] op_sel_hi:[1,0]
C v114,v168,v169
C v115,v170,v171
ds_write_b128 v12, v[112:115] offset:12288
R v172,a104
R v173,a105
R v174,a106
R v175,a107
v_pk_mul_f32 v[172:173], v[172:173], v[128:129] op_sel_hi:[1,0]
v_pk_mul_f32 v[174:175], v[174:175], v[128:129] op_sel_hi:[1,0]
C v116,v172,v173
C v117,v174,v175
R v164,a108
R v165,a109
R v166,a110
R v167,a111
v_pk_mul_f32 v[164:165], v[164:165], v[128:129] op_sel_hi:[1,0]
v_pk_mul_f32 v[166:167], v[166:167], v[128:129] op_sel_hi:[1,0]
C v118,v164,v165
C v119,v166,v167
ds_write_b128 v12, v[116:119] offset:13312
R v168,a112
R v169,a113
R v170,a114
R v171,a115
v_pk_mul_f32 v[168:169], v[168:169], v[128:129] op_sel_hi:[1,0]
v_pk_mul_f32 v[170:171], v[170:171], v[128:129] op_sel_hi:[1,0]
C v120,v168,v169
C v121,v170,v171
R v172,a116
R v173,a117
R v174,a118
R v175,a119
v_pk_mul_f32 v[172:173], v[172:173], v[128:129] op_sel_hi:[1,0]
v_pk_mul_f32 v[174:175], v[174:175], v[128:129] op_sel_hi:[1,0]
C v122,v172,v173
C v123,v174,v175
ds_write_b128 v12, v[120:123] offset:14336
R v164,a120
R v165,a121
R v166,a122
R v167,a123
v_pk_mul_f32 v[164:165], v[164:165], v[128:129] op_sel_hi:[1,0]
v_pk_mul_f32 v[166:167], v[166:167], v[128:129] op_sel_hi:[1,0]
C v124,v164,v165
C v125,v166,v167
R v168,a124
R v169,a125
R v170,a126
R v171,a127
v_pk_mul_f32 v[168:169], v[168:169], v[128:129] op_sel_hi:[1,0]
v_pk_mul_f32 v[170:171], v[170:171], v[128:129] op_sel_hi:[1,0]
C v126,v168,v169
C v127,v170,v171
ds_write_b128 v12, v[124:127] offset:15360
R v172,a128
R v173,a129
R v174,a130
R v175,a131
v_pk_mul_f32 v[172:173], v[172:173], v[160:161] op_sel_hi:[1,0]
v_pk_mul_f32 v[174:175], v[174:175], v[160:161] op_sel_hi:[1,0]
C v144,v172,v173
C v145,v174,v175
R v164,a132
R v165,a133
R v166,a134
R v167,a135
v_pk_mul_f32 v[164:165], v[164:165], v[160:161] op_sel_hi:[1,0]
v_pk_mul_f32 v[166:167], v[166:167], v[160:161] op_sel_hi:[1,0]
C v146,v164,v165
C v147,v166,v167
ds_write_b128 v12, v[144:147] offset:16384
R v168,a136
R v169,a137
R v170,a138
R v171,a139
v_pk_mul_f32 v[168:169], v[168:169], v[160:161] op_sel_hi:[1,0]
v_pk_mul_f32 v[170:171], v[170:171], v[160:161] op_sel_hi:[1,0]
C v148,v168,v169
C v149,v170,v171
R v172,a140
R v173,a141
R v174,a142
R v175,a143
v_pk_mul_f32 v[172:173], v[172:173], v[160:161] op_sel_hi:[1,0]
v_pk_mul_f32 v[174:175], v[174:175], v[160:161] op_sel_hi:[1,0]
C v150,v172,v173
C v151,v174,v175
ds_write_b128 v12, v[148:151] offset:17408
R v164,a144
R v165,a145
R v166,a146
R v167,a147
v_pk_mul_f32 v[164:165], v[164:165], v[160:161] op_sel_hi:[1,0]
v_pk_mul_f32 v[166:167], v[166:167], v[160:161] op_sel_hi:[1,0]
C v152,v164,v165
C v153,v166,v167
R v168,a148
R v169,a149
R v170,a150
R v171,a151
v_pk_mul_f32 v[168:169], v[168:169], v[160:161] op_sel_hi:[1,0]
v_pk_mul_f32 v[170:171], v[170:171], v[160:161] op_sel_hi:[1,0]
C v154,v168,v169
C v155,v170,v171
ds_write_b128 v12, v[152:155] offset:18432
R v172,a152
R v173,a153
R v174,a154
R v175,a155
v_pk_mul_f32 v[172:173], v[172:173], v[160:161] op_sel_hi:[1,0]
v_pk_mul_f32 v[174:175], v[174:175], v[160:161] op_sel_hi:[1,0]
C v156,v172,v173
C v157,v174,v175
R v164,a156
R v165,a157
R v166,a158
R v167,a159
v_pk_mul_f32 v[164:165], v[164:165], v[160:161] op_sel_hi:[1,0]
v_pk_mul_f32 v[166:167], v[166:167], v[160:161] op_sel_hi:[1,0]
C v158,v164,v165
C v159,v166,v167
ds_write_b128 v12, v[156:159] offset:19456
L_ph_opS:
v_lshrrev_b32 v164, 4, v1
v_lshrrev_b32 v165, 4, v164
v_bfe_u32 v166, v164, 2, 2
v_and_b32 v167, 3, v164
s_mul_i32 s31, s25, 16896
s_add_u32 s31, s31, s95
s_mov_b32 s65, s95
s_mov_b32 s76, 32768
s_mov_b32 s90, -131072
v_and_b32 v164, 1, v165
v_mul_u32_u24 v2, 288, v164
v_lshl_add_u32 v2, v166, 6, v2
v_lshl_add_u32 v2, v167, 3, v2
v_lshrrev_b32 v164, 1, v165
v_lshl_add_u32 v2, v164, 10, v2
v_add_u32 v2, s31, v2
v_xor_b32 v3, 32, v2
v_xor_b32 v4, 16384, v2
v_xor_b32 v5, 16416, v2
s_lshl_b32 s31, s30, 6
s_add_u32 s31, s31, s27
s_mul_i32 s94, s31, 20480
s_mul_i32 s23, s24, 5120
s_add_u32 s94, s94, s23
s_lshl_b32 s31, s30, 3
s_add_u32 s31, s31, s27
s_mul_i32 s68, s31, 163840
s_add_u32 s68, s68, s23
v_lshrrev_b32 v164, 4, v1
v_cmp_eq_u32 vcc, v166, v165
v_and_b32 v168, 1, v164
v_lshlrev_b32 v168, 4, v168
v_mov_b32 v169, 0x3f80
v_lshlrev_b32 v169, v168, v169
v_cndmask_b32 v169, 0, v169, vcc
v_and_b32 v168, 2, v164
v_cmp_eq_u32 vcc, 0, v168
v_mov_b32 v170, 0
v_accvgpr_write_b32 a142, 0
v_accvgpr_write_b32 a143, 0
v_accvgpr_write_b32 a144, 0
v_accvgpr_write_b32 a145, 0
v_cndmask_b32 v168, 0, v169, vcc
v_cndmask_b32 v170, v169, v170, vcc
v_accvgpr_write_b32 a140, v168
v_accvgpr_write_b32 a146, v168
v_accvgpr_write_b32 a141, v170
v_accvgpr_write_b32 a147, v170
s_lshl_b32 s77, s30, 3
s_lshl_b32 s31, s24, 16
s_lshl_b32 s23, s27, 2
s_add_u32 s31, s31, s23
v_lshlrev_b32 v6, 12, v1
v_add_u32 v6, s31, v6
s_waitcnt vmcnt(8) lgkmcnt(0)
s_barrier
T a[148:149],v2,0
T a[150:151],v2,2048
T a[152:153],v2,4096
T a[154:155],v2,6144
T a[156:157],v3,0
T a[158:159],v3,2048
T a[160:161],v3,4096
T a[162:163],v3,6144
s_waitcnt lgkmcnt(6)
M v[32:35],a[148:151],v[176:179],0
s_waitcnt lgkmcnt(4)
T a[164:165],v2,8192
)"
R"(M v[32:35],a[152:155],v[180:183],v[32:35]
D a[0:3],v9,0
M v[64:67],a[148:151],v[192:195],0
T a[166:167],v2,10240
M v[64:67],a[152:155],v[196:199],v[64:67]
D a[4:7],v9,1024
M v[96:99],a[148:151],v[208:211],0
T a[168:169],v2,12288
M v[96:99],a[152:155],v[212:215],v[96:99]
D a[8:11],v9,2048
M v[128:131],a[148:151],v[224:227],0
T a[170:171],v2,14336
M v[128:131],a[152:155],v[228:231],v[128:131]
D a[12:15],v9,3072
M v[160:163],a[148:151],v[240:243],0
D a[16:19],v9,4096
M v[160:163],a[152:155],v[244:247],v[160:163]
s_waitcnt lgkmcnt(11)
D a[20:23],v9,5120
M v[36:39],a[156:159],v[176:179],0
s_waitcnt lgkmcnt(10)
T a[172:173],v3,8192
M v[36:39],a[160:163],v[180:183],v[36:39]
D a[24:27],v9,6144
M v[68:71],a[156:159],v[192:195],0
T a[174:175],v3,10240
M v[68:71],a[160:163],v[196:199],v[68:71]
D a[28:31],v9,7168
M v[100:103],a[156:159],v[208:211],0
T a[148:149],v3,12288
M v[100:103],a[160:163],v[212:215],v[100:103]
D a[32:35],v9,8192
M v[132:135],a[156:159],v[224:227],0
T a[150:151],v3,14336
M v[132:135],a[160:163],v[228:231],v[132:135]
D a[36:39],v9,9216
M v[164:167],a[156:159],v[240:243],0
D a[40:43],v9,10240
M v[164:167],a[160:163],v[244:247],v[164:167]
s_waitcnt lgkmcnt(15)
D a[44:47],v9,11264
M v[32:35],a[164:167],v[184:187],v[32:35]
s_waitcnt lgkmcnt(13)
T a[152:153],v4,0
M v[32:35],a[168:171],v[188:191],v[32:35]
D a[48:51],v9,12288
M v[64:67],a[168:171],v[204:207],v[64:67]
T a[154:155],v4,2048
M v[64:67],a[164:167],v[200:203],v[64:67]
D a[52:55],v9,13312
M v[96:99],a[164:167],v[216:219],v[96:99]
T a[156:157],v4,4096
M v[96:99],a[168:171],v[220:223],v[96:99]
D a[56:59],v9,14336
M v[128:131],a[168:171],v[236:239],v[128:131]
T a[158:159],v4,6144
D a[60:63],v9,15360
M v[128:131],a[164:167],v[232:235],v[128:131]
D a[64:67],v9,16384
M v[160:163],a[164:167],v[248:251],v[160:163]
D a[68:71],v9,17408
M v[160:163],a[168:171],v[252:255],v[160:163]
s_waitcnt lgkmcnt(15)
D a[72:75],v9,18432
M v[36:39],a[172:175],v[184:187],v[36:39]
s_waitcnt lgkmcnt(14)
T a[160:161],v5,0
M v[36:39],a[148:151],v[188:191],v[36:39]
D a[76:79],v9,19456
M v[68:71],a[148:151],v[204:207],v[68:71]
T a[162:163],v5,2048
M v[68:71],a[172:175],v[200:203],v[68:71]
M v[100:103],a[172:175],v[216:219],v[100:103]
T a[164:165],v5,4096
M v[100:103],a[148:151],v[220:223],v[100:103]
M v[132:135],a[148:151],v[236:239],v[132:135]
T a[166:167],v5,6144
M v[132:135],a[172:175],v[232:235],v[132:135]
s_waitcnt lgkmcnt(0)
s_barrier
M v[164:167],a[172:175],v[248:251],v[164:167]
s_lshl_b32 s81, s91, 7
M v[164:167],a[148:151],v[252:255],v[164:167]
s_add_u32 s81, s81, s93
M v[32:35],a[152:155],a[176:179],v[32:35]
s_add_u32 m0, s73, s6
T a[168:169],v4,8192
M v[32:35],a[156:159],a[180:183],v[32:35]
buffer_load_dwordx4 v11, s[48:51], s81 offen lds
M v[64:67],a[156:159],a[196:199],v[64:67]
T a[170:171],v4,10240
M v[64:67],a[152:155],a[192:195],v[64:67]
M v[96:99],a[152:155],a[208:211],v[96:99]
s_add_u32 s71, s81, 7168
T a[172:173],v4,12288
buffer_load_dwordx4 v11, s[48:51], s71 offen offset:1024 lds
M v[96:99],a[156:159],a[212:215],v[96:99]
M v[128:131],a[156:159],a[228:231],v[128:131]
T a[174:175],v4,14336
M v[128:131],a[152:155],a[224:227],v[128:131]
s_add_u32 s72, s81, 14336
buffer_load_dwordx4 v11, s[48:51], s72 offen offset:2048 lds
M v[160:163],a[152:155],a[240:243],v[160:163]
M v[160:163],a[156:159],a[244:247],v[160:163]
M v[36:39],a[160:163],a[176:179],v[36:39]
s_add_u32 s71, s81, 21504
T a[148:149],v5,8192
buffer_load_dwordx4 v11, s[48:51], s71 offen offset:3072 lds
M v[36:39],a[164:167],a[180:183],v[36:39]
s_add_u32 s72, s81, 32768
M v[68:71],a[164:167],a[196:199],v[68:71]
s_add_u32 m0, m0, 4096
T a[150:151],v5,10240
M v[68:71],a[160:163],a[192:195],v[68:71]
buffer_load_dwordx4 v11, s[48:51], s72 offen lds
M v[100:103],a[160:163],a[208:211],v[100:103]
T a[152:153],v5,12288
M v[100:103],a[164:167],a[212:215],v[100:103]
M v[132:135],a[164:167],a[228:231],v[132:135]
s_add_u32 s71, s81, 39936
T a[154:155],v5,14336
buffer_load_dwordx4 v11, s[48:51], s71 offen offset:1024 lds
M v[132:135],a[160:163],a[224:227],v[132:135]
s_mov_b32 s64, s65
M v[164:167],a[160:163],a[240:243],v[164:167]
s_cmp_eq_u32 s65, 131072
s_cselect_b32 s66, s90, s76
M v[164:167],a[164:167],a[244:247],v[164:167]
s_waitcnt lgkmcnt(6)
s_add_u32 s72, s81, 47104
s_add_u32 s65, s65, s66
buffer_load_dwordx4 v11, s[48:51], s72 offen offset:2048 lds
M v[32:35],a[168:171],a[184:187],v[32:35]
s_waitcnt lgkmcnt(4)
v_add_u32 v2, s66, v2
v_add_u32 v3, s66, v3
M v[32:35],a[172:175],a[188:191],v[32:35]
v_add_u32 v4, s66, v4
v_add_u32 v5, s66, v5
M v[64:67],a[172:175],a[204:207],v[64:67]
s_add_u32 s71, s81, 54272
buffer_load_dwordx4 v11, s[48:51], s71 offen offset:3072 lds
M v[64:67],a[168:171],a[200:203],v[64:67]
M v[96:99],a[168:171],a[216:219],v[96:99]
M v[96:99],a[172:175],a[220:223],v[96:99]
s_waitcnt vmcnt(8) lgkmcnt(0)
M v[128:131],a[172:175],a[236:239],v[128:131]
s_barrier
M v[128:131],a[168:171],a[232:235],v[128:131]
T a[156:157],v2,0
M v[160:163],a[168:171],a[248:251],v[160:163]
s_add_u32 s81, s81, 2097152
s_add_u32 s91, s91, 1
T a[158:159],v2,2048
M v[160:163],a[172:175],a[252:255],v[160:163]
s_and_b32 s91, s91, 7
T a[160:161],v2,4096
M v[36:39],a[148:151],a[184:187],v[36:39]
s_add_u32 m0, s73, s4
T a[162:163],v2,6144
M v[36:39],a[152:155],a[188:191],v[36:39]
buffer_load_dwordx4 v11, s[48:51], s81 offen lds
M v[68:71],a[152:155],a[204:207],v[68:71]
T a[164:165],v3,0
M v[68:71],a[148:151],a[200:203],v[68:71]
M v[100:103],a[148:151],a[216:219],v[100:103]
s_add_u32 s71, s81, 7168
T a[166:167],v3,2048
buffer_load_dwordx4 v11, s[48:51], s71 offen offset:1024 lds
M v[100:103],a[152:155],a[220:223],v[100:103]
M v[132:135],a[152:155],a[236:239],v[132:135]
T a[168:169],v3,4096
M v[132:135],a[148:151],a[232:235],v[132:135]
s_add_u32 s72, s81, 14336
buffer_load_dwordx4 v11, s[48:51], s72 offen offset:2048 lds
M v[164:167],a[148:151],a[248:251],v[164:167]
T a[170:171],v3,6144
M v[164:167],a[152:155],a[252:255],v[164:167]
s_waitcnt lgkmcnt(6)
M v[32:35],a[156:159],v[16:19],v[32:35]
s_waitcnt lgkmcnt(4)
s_add_u32 s71, s81, 21504
T a[172:173],v2,8192
buffer_load_dwordx4 v11, s[48:51], s71 offen offset:3072 lds
M v[32:35],a[160:163],v[20:23],v[32:35]
s_add_u32 s72, s81, 32768
M v[64:67],a[156:159],v[48:51],v[64:67]
s_add_u32 m0, m0, 4096
T a[174:175],v2,10240
M v[64:67],a[160:163],v[52:55],v[64:67]
buffer_load_dwordx4 v11, s[48:51], s72 offen lds
M v[96:99],a[156:159],v[80:83],v[96:99]
T a[148:149],v2,12288
M v[96:99],a[160:163],v[84:87],v[96:99]
M v[128:131],a[156:159],v[112:115],v[128:131]
s_add_u32 s71, s81, 39936
T a[150:151],v2,14336
buffer_load_dwordx4 v11, s[48:51], s71 offen offset:1024 lds
M v[128:131],a[160:163],v[116:119],v[128:131]
M v[160:163],a[156:159],v[144:147],v[160:163]
M v[160:163],a[160:163],v[148:151],v[160:163]
s_waitcnt lgkmcnt(6)
s_add_u32 s72, s81, 47104
buffer_load_dwordx4 v11, s[48:51], s72 offen offset:2048 lds
M v[36:39],a[164:167],v[16:19],v[36:39]
s_waitcnt lgkmcnt(4)
T a[152:153],v3,8192
M v[36:39],a[168:171],v[20:23],v[36:39]
M v[68:71],a[164:167],v[48:51],v[68:71]
s_add_u32 s71, s81, 54272
T a[154:155],v3,10240
buffer_load_dwordx4 v11, s[48:51], s71 offen offset:3072 lds
M v[68:71],a[168:171],v[52:55],v[68:71]
s_lshl_b32 s81, s91, 7
s_add_u32 s81, s81, s93
M v[100:103],a[164:167],v[80:83],v[100:103]
s_add_u32 m0, s73, s5
T a[156:157],v3,12288
M v[100:103],a[168:171],v[84:87],v[100:103]
buffer_load_dwordx4 v11, s[48:51], s81 offen lds
M v[132:135],a[164:167],v[112:115],v[132:135]
T a[158:159],v3,14336
M v[132:135],a[168:171],v[116:119],v[132:135]
M v[164:167],a[164:167],v[144:147],v[164:167]
s_add_u32 s71, s81, 7168
buffer_load_dwordx4 v11, s[48:51], s71 offen offset:1024 lds
M v[164:167],a[168:171],v[148:151],v[164:167]
s_waitcnt lgkmcnt(6)
M v[32:35],a[172:175],v[24:27],v[32:35]
s_waitcnt lgkmcnt(4)
T a[160:161],v4,0
M v[32:35],a[148:151],v[28:31],v[32:35]
s_add_u32 s72, s81, 14336
buffer_load_dwordx4 v11, s[48:51], s72 offen offset:2048 lds
M v[64:67],a[148:151],v[60:63],v[64:67]
T a[162:163],v4,2048
M v[64:67],a[172:175],v[56:59],v[64:67]
M v[96:99],a[172:175],v[88:91],v[96:99]
s_add_u32 s71, s81, 21504
T a[164:165],v4,4096
buffer_load_dwordx4 v11, s[48:51], s71 offen offset:3072 lds
M v[96:99],a[148:151],v[92:95],v[96:99]
s_add_u32 s72, s81, 32768
M v[128:131],a[148:151],v[124:127],v[128:131]
s_add_u32 m0, m0, 4096
T a[166:167],v4,6144
M v[128:131],a[172:175],v[120:123],v[128:131]
buffer_load_dwordx4 v11, s[48:51], s72 offen lds
M v[160:163],a[172:175],v[152:155],v[160:163]
M v[160:163],a[148:151],v[156:159],v[160:163]
s_waitcnt lgkmcnt(6)
M v[36:39],a[152:155],v[24:27],v[36:39]
s_waitcnt lgkmcnt(4)
s_add_u32 s71, s81, 39936
T a[168:169],v5,0
buffer_load_dwordx4 v11, s[48:51], s71 offen offset:1024 lds
M v[36:39],a[156:159],v[28:31],v[36:39]
M v[68:71],a[156:159],v[60:63],v[68:71]
T a[170:171],v5,2048
M v[68:71],a[152:155],v[56:59],v[68:71]
s_add_u32 s72, s81, 47104
buffer_load_dwordx4 v11, s[48:51], s72 offen offset:2048 lds
M v[100:103],a[152:155],v[88:91],v[100:103]
T a[172:173],v5,4096
M v[100:103],a[156:159],v[92:95],v[100:103]
M v[132:135],a[156:159],v[124:127],v[132:135]
s_add_u32 s71, s81, 54272
T a[174:175],v5,6144
buffer_load_dwordx4 v11, s[48:51], s71 offen offset:3072 lds
M v[132:135],a[152:155],v[120:123],v[132:135]
s_add_u32 s81, s81, 2097152
s_add_u32 s91, s91, 1
s_and_b32 s91, s91, 7
M v[164:167],a[152:155],v[152:155],v[164:167]
s_add_u32 m0, s73, s64
M v[164:167],a[156:159],v[156:159],v[164:167]
s_waitcnt lgkmcnt(6)
buffer_load_dwordx4 v11, s[48:51], s81 offen lds
M v[32:35],a[160:163],a[0:3],v[32:35]
s_waitcnt lgkmcnt(4)
T a[148:149],v4,8192
M v[32:35],a[164:167],a[4:7],v[32:35]
M v[64:67],a[164:167],a[20:23],v[64:67]
s_add_u32 s71, s81, 7168
T a[150:151],v4,10240
buffer_load_dwordx4 v11, s[48:51], s71 offen offset:1024 lds
M v[64:67],a[160:163],a[16:19],v[64:67]
M v[96:99],a[160:163],a[32:35],v[96:99]
T a[152:153],v4,12288
M v[96:99],a[164:167],a[36:39],v[96:99]
s_add_u32 s72, s81, 14336
buffer_load_dwordx4 v11, s[48:51], s72 offen offset:2048 lds
M v[128:131],a[164:167],a[52:55],v[128:131]
T a[154:155],v4,14336
M v[128:131],a[160:163],a[48:51],v[128:131]
M v[160:163],a[160:163],a[64:67],v[160:163]
s_add_u32 s71, s81, 21504
buffer_load_dwordx4 v11, s[48:51], s71 offen offset:3072 lds
M v[160:163],a[164:167],a[68:71],v[160:163]
s_waitcnt lgkmcnt(6)
s_add_u32 s72, s81, 32768
M v[36:39],a[168:171],a[0:3],v[36:39]
s_waitcnt lgkmcnt(4)
s_add_u32 m0, m0, 4096
T a[156:157],v5,8192
M v[36:39],a[172:175],a[4:7],v[36:39]
buffer_load_dwordx4 v11, s[48:51], s72 offen lds
M v[68:71],a[172:175],a[20:23],v[68:71]
T a[158:159],v5,10240
M v[68:71],a[168:171],a[16:19],v[68:71]
M v[100:103],a[168:171],a[32:35],v[100:103]
s_add_u32 s71, s81, 39936
T a[160:161],v5,12288
buffer_load_dwordx4 v11, s[48:51], s71 offen offset:1024 lds
M v[100:103],a[172:175],a[36:39],v[100:103]
M v[132:135],a[172:175],a[52:55],v[132:135]
T a[162:163],v5,14336
M v[132:135],a[168:171],a[48:51],v[132:135]
s_mov_b32 s64, s65
s_add_u32 s72, s81, 47104
buffer_load_dwordx4 v11, s[48:51], s72 offen offset:2048 lds
M v[164:167],a[168:171],a[64:67],v[164:167]
s_cmp_eq_u32 s65, 131072
s_cselect_b32 s66, s90, s76
M v[164:167],a[172:175],a[68:71],v[164:167]
s_waitcnt lgkmcnt(6)
s_add_u32 s65, s65, s66
M v[32:35],a[148:151],a[8:11],v[32:35]
s_waitcnt lgkmcnt(4)
s_add_u32 s71, s81, 54272
buffer_load_dwordx4 v11, s[48:51], s71 offen offset:3072 lds
v_add_u32 v2, s66, v2
M v[32:35],a[152:155],a[12:15],v[32:35]
v_add_u32 v3, s66, v3
v_add_u32 v4, s66, v4
M v[64:67],a[152:155],a[28:31],v[64:67]
v_add_u32 v5, s66, v5
M v[64:67],a[148:151],a[24:27],v[64:67]
M v[96:99],a[148:151],a[40:43],v[96:99]
M v[96:99],a[152:155],a[44:47],v[96:99]
s_waitcnt vmcnt(24) lgkmcnt(0)
M v[128:131],a[152:155],a[60:63],v[128:131]
s_barrier
M v[128:131],a[148:151],a[56:59],v[128:131]
T a[164:165],v2,0
M v[160:163],a[148:151],a[72:75],v[160:163]
s_lshl_b32 s81, s91, 7
T a[166:167],v2,2048
M v[160:163],a[152:155],a[76:79],v[160:163]
s_add_u32 s81, s81, s93
T a[168:169],v2,4096
M v[36:39],a[156:159],a[8:11],v[36:39]
s_add_u32 m0, s73, s64
T a[170:171],v2,6144
M v[36:39],a[160:163],a[12:15],v[36:39]
buffer_load_dwordx4 v11, s[48:51], s81 offen lds
M v[68:71],a[160:163],a[28:31],v[68:71]
T a[172:173],v3,0
M v[68:71],a[156:159],a[24:27],v[68:71]
M v[100:103],a[156:159],a[40:43],v[100:103]
T a[174:175],v3,2048
M v[100:103],a[160:163],a[44:47],v[100:103]
M v[132:135],a[160:163],a[60:63],v[132:135]
T a[148:149],v3,4096
M v[132:135],a[156:159],a[56:59],v[132:135]
s_add_u32 s71, s81, 7168
buffer_load_dwordx4 v11, s[48:51], s71 offen offset:1024 lds
M v[164:167],a[156:159],a[72:75],v[164:167]
T a[150:151],v3,6144
M v[164:167],a[160:163],a[76:79],v[164:167]
s_waitcnt lgkmcnt(6)
M v[40:43],a[164:167],v[176:179],0
s_waitcnt lgkmcnt(4)
s_add_u32 s97, s27, 1
T a[152:153],v2,8192
M v[40:43],a[168:171],v[180:183],v[40:43]
s_and_b32 s97, s97, 7
M v[72:75],a[164:167],v[192:195],0
s_mul_i32 s97, s97, 163840
T a[154:155],v2,10240
M v[72:75],a[168:171],v[196:199],v[72:75]
s_add_u32 s72, s81, 14336
s_add_u32 s97, s97, s94
buffer_load_dwordx4 v11, s[48:51], s72 offen offset:2048 lds
M v[104:107],a[164:167],v[208:211],0
s_add_u32 s80, s97, 4096
T a[156:157],v2,12288
C v32,v32,v33
M v[104:107],a[168:171],v[212:215],v[104:107]
C v33,v34,v35
C v34,v36,v37
M v[136:139],a[164:167],v[224:227],0
T a[158:159],v2,14336
C v35,v38,v39
M v[136:139],a[168:171],v[228:231],v[136:139]
buffer_store_dwordx4 v[32:35], v1, s[52:55], s97 offen sc1
M v[168:171],a[164:167],v[240:243],0
C v64,v64,v65
C v65,v66,v67
M v[168:171],a[168:171],v[244:247],v[168:171]
s_waitcnt lgkmcnt(6)
s_add_u32 s71, s81, 21504
buffer_load_dwordx4 v11, s[48:51], s71 offen offset:3072 lds
C v66,v68,v69
M v[44:47],a[172:175],v[176:179],0
s_waitcnt lgkmcnt(4)
T a[160:161],v3,8192
C v67,v70,v71
M v[44:47],a[148:151],v[180:183],v[44:47]
C v96,v96,v97
C v97,v98,v99
M v[76:79],a[172:175],v[192:195],0
T a[162:163],v3,10240
C v98,v100,v101
M v[76:79],a[148:151],v[196:199],v[76:79]
s_add_u32 s72, s81, 32768
buffer_store_dwordx4 v[64:67], v1, s[52:55], s97 offen offset:1024 sc1
M v[108:111],a[172:175],v[208:211],0
s_add_u32 m0, m0, 4096
T a[164:165],v3,12288
C v99,v102,v103
M v[108:111],a[148:151],v[212:215],v[108:111]
buffer_load_dwordx4 v11, s[48:51], s72 offen lds
C v128,v128,v129
M v[140:143],a[172:175],v[224:227],0
T a[166:167],v3,14336
C v129,v130,v131
M v[140:143],a[148:151],v[228:231],v[140:143]
C v130,v132,v133
C v131,v134,v135
M v[172:175],a[172:175],v[240:243],0
buffer_store_dwordx4 v[96:99], v1, s[52:55], s97 offen offset:2048 sc1
M v[172:175],a[148:151],v[244:247],v[172:175]
s_waitcnt lgkmcnt(6)
C v160,v160,v161
C v161,v162,v163
M v[40:43],a[152:155],v[184:187],v[40:43]
s_waitcnt lgkmcnt(4)
T a[168:169],v4,0
C v162,v164,v165
M v[40:43],a[156:159],v[188:191],v[40:43]
s_add_u32 s71, s81, 39936
buffer_load_dwordx4 v11, s[48:51], s71 offen offset:1024 lds
C v163,v166,v167
M v[72:75],a[156:159],v[204:207],v[72:75]
T a[170:171],v4,2048
M v[72:75],a[152:155],v[200:203],v[72:75]
buffer_store_dwordx4 v[128:131], v1, s[52:55], s97 offen offset:3072 sc1
M v[104:107],a[152:155],v[216:219],v[104:107]
T a[172:173],v4,4096
M v[104:107],a[156:159],v[220:223],v[104:107]
M v[136:139],a[156:159],v[236:239],v[136:139]
T a[174:175],v4,6144
M v[136:139],a[152:155],v[232:235],v[136:139]
s_add_u32 s72, s81, 47104
buffer_load_dwordx4 v11, s[48:51], s72 offen offset:2048 lds
M v[168:171],a[152:155],v[248:251],v[168:171]
buffer_store_dwordx4 v[160:163], v1, s[52:55], s80 offen sc1
M v[168:171],a[156:159],v[252:255],v[168:171]
s_waitcnt lgkmcnt(6)
M v[44:47],a[160:163],v[184:187],v[44:47]
s_waitcnt lgkmcnt(4)
T a[148:149],v5,0
M v[44:47],a[164:167],v[188:191],v[44:47]
M v[76:79],a[164:167],v[204:207],v[76:79]
T a[150:151],v5,2048
M v[76:79],a[160:163],v[200:203],v[76:79]
s_add_u32 s71, s81, 54272
buffer_load_dwordx4 v11, s[48:51], s71 offen offset:3072 lds
M v[108:111],a[160:163],v[216:219],v[108:111]
T a[152:153],v5,4096
M v[108:111],a[164:167],v[220:223],v[108:111]
M v[140:143],a[164:167],v[236:239],v[140:143]
T a[154:155],v5,6144
M v[140:143],a[160:163],v[232:235],v[140:143]
M v[172:175],a[160:163],v[248:251],v[172:175]
M v[172:175],a[164:167],v[252:255],v[172:175]
s_waitcnt lgkmcnt(6)
M v[40:43],a[168:171],a[176:179],v[40:43]
s_waitcnt lgkmcnt(4)
T a[156:157],v4,8192
M v[40:43],a[172:175],a[180:183],v[40:43]
M v[72:75],a[172:175],a[196:199],v[72:75]
T a[158:159],v4,10240
M v[72:75],a[168:171],a[192:195],v[72:75]
M v[104:107],a[168:171],a[208:211],v[104:107]
T a[160:161],v4,12288
M v[104:107],a[172:175],a[212:215],v[104:107]
M v[136:139],a[172:175],a[228:231],v[136:139]
T a[162:163],v4,14336
M v[136:139],a[168:171],a[224:227],v[136:139]
M v[168:171],a[168:171],a[240:243],v[168:171]
M v[168:171],a[172:175],a[244:247],v[168:171]
s_waitcnt lgkmcnt(6)
M v[44:47],a[148:151],a[176:179],v[44:47]
s_waitcnt lgkmcnt(4)
T a[164:165],v5,8192
M v[44:47],a[152:155],a[180:183],v[44:47]
M v[76:79],a[152:155],a[196:199],v[76:79]
T a[166:167],v5,10240
)"
R"(M v[76:79],a[148:151],a[192:195],v[76:79]
M v[108:111],a[148:151],a[208:211],v[108:111]
T a[168:169],v5,12288
M v[108:111],a[152:155],a[212:215],v[108:111]
M v[140:143],a[152:155],a[228:231],v[140:143]
T a[170:171],v5,14336
M v[140:143],a[148:151],a[224:227],v[140:143]
s_mov_b32 s64, s65
M v[172:175],a[148:151],a[240:243],v[172:175]
s_cmp_eq_u32 s65, 131072
s_cselect_b32 s66, s90, s76
M v[172:175],a[152:155],a[244:247],v[172:175]
s_waitcnt lgkmcnt(6)
s_add_u32 s65, s65, s66
M v[40:43],a[156:159],a[184:187],v[40:43]
s_waitcnt lgkmcnt(4)
v_add_u32 v2, s66, v2
v_add_u32 v3, s66, v3
M v[40:43],a[160:163],a[188:191],v[40:43]
v_add_u32 v4, s66, v4
v_add_u32 v5, s66, v5
M v[72:75],a[160:163],a[204:207],v[72:75]
M v[72:75],a[156:159],a[200:203],v[72:75]
M v[104:107],a[156:159],a[216:219],v[104:107]
M v[104:107],a[160:163],a[220:223],v[104:107]
s_waitcnt vmcnt(29) lgkmcnt(0)
M v[136:139],a[160:163],a[236:239],v[136:139]
s_barrier
M v[136:139],a[156:159],a[232:235],v[136:139]
T a[172:173],v2,0
M v[168:171],a[156:159],a[248:251],v[168:171]
s_add_u32 s81, s81, 2097152
s_add_u32 s91, s91, 1
T a[174:175],v2,2048
M v[168:171],a[160:163],a[252:255],v[168:171]
s_and_b32 s91, s91, 7
T a[148:149],v2,4096
M v[44:47],a[164:167],a[184:187],v[44:47]
s_add_u32 m0, s73, s64
T a[150:151],v2,6144
M v[44:47],a[168:171],a[188:191],v[44:47]
buffer_load_dwordx4 v11, s[48:51], s81 offen lds
M v[76:79],a[168:171],a[204:207],v[76:79]
T a[152:153],v3,0
M v[76:79],a[164:167],a[200:203],v[76:79]
M v[108:111],a[164:167],a[216:219],v[108:111]
T a[154:155],v3,2048
M v[108:111],a[168:171],a[220:223],v[108:111]
M v[140:143],a[168:171],a[236:239],v[140:143]
T a[156:157],v3,4096
M v[140:143],a[164:167],a[232:235],v[140:143]
s_add_u32 s71, s81, 7168
buffer_load_dwordx4 v11, s[48:51], s71 offen offset:1024 lds
M v[172:175],a[164:167],a[248:251],v[172:175]
T a[158:159],v3,6144
M v[172:175],a[168:171],a[252:255],v[172:175]
s_waitcnt lgkmcnt(6)
M v[40:43],a[172:175],v[16:19],v[40:43]
s_waitcnt lgkmcnt(4)
T a[160:161],v2,8192
M v[40:43],a[148:151],v[20:23],v[40:43]
M v[72:75],a[172:175],v[48:51],v[72:75]
T a[162:163],v2,10240
M v[72:75],a[148:151],v[52:55],v[72:75]
s_add_u32 s72, s81, 14336
buffer_load_dwordx4 v11, s[48:51], s72 offen offset:2048 lds
M v[104:107],a[172:175],v[80:83],v[104:107]
T a[164:165],v2,12288
M v[104:107],a[148:151],v[84:87],v[104:107]
M v[136:139],a[172:175],v[112:115],v[136:139]
T a[166:167],v2,14336
M v[136:139],a[148:151],v[116:119],v[136:139]
M v[168:171],a[172:175],v[144:147],v[168:171]
M v[168:171],a[148:151],v[148:151],v[168:171]
s_waitcnt lgkmcnt(6)
s_add_u32 s71, s81, 21504
buffer_load_dwordx4 v11, s[48:51], s71 offen offset:3072 lds
M v[44:47],a[152:155],v[16:19],v[44:47]
s_waitcnt lgkmcnt(4)
T a[168:169],v3,8192
M v[44:47],a[156:159],v[20:23],v[44:47]
M v[76:79],a[152:155],v[48:51],v[76:79]
T a[170:171],v3,10240
M v[76:79],a[156:159],v[52:55],v[76:79]
s_add_u32 s72, s81, 32768
M v[108:111],a[152:155],v[80:83],v[108:111]
s_add_u32 m0, m0, 4096
T a[172:173],v3,12288
M v[108:111],a[156:159],v[84:87],v[108:111]
buffer_load_dwordx4 v11, s[48:51], s72 offen lds
M v[140:143],a[152:155],v[112:115],v[140:143]
T a[174:175],v3,14336
M v[140:143],a[156:159],v[116:119],v[140:143]
M v[172:175],a[152:155],v[144:147],v[172:175]
M v[172:175],a[156:159],v[148:151],v[172:175]
s_waitcnt lgkmcnt(6)
M v[40:43],a[160:163],v[24:27],v[40:43]
s_waitcnt lgkmcnt(4)
T a[148:149],v4,0
M v[40:43],a[164:167],v[28:31],v[40:43]
s_add_u32 s71, s81, 39936
buffer_load_dwordx4 v11, s[48:51], s71 offen offset:1024 lds
M v[72:75],a[164:167],v[60:63],v[72:75]
T a[150:151],v4,2048
M v[72:75],a[160:163],v[56:59],v[72:75]
M v[104:107],a[160:163],v[88:91],v[104:107]
T a[152:153],v4,4096
M v[104:107],a[164:167],v[92:95],v[104:107]
M v[136:139],a[164:167],v[124:127],v[136:139]
T a[154:155],v4,6144
M v[136:139],a[160:163],v[120:123],v[136:139]
s_add_u32 s72, s81, 47104
buffer_load_dwordx4 v11, s[48:51], s72 offen offset:2048 lds
M v[168:171],a[160:163],v[152:155],v[168:171]
M v[168:171],a[164:167],v[156:159],v[168:171]
s_waitcnt lgkmcnt(6)
M v[44:47],a[168:171],v[24:27],v[44:47]
s_waitcnt lgkmcnt(4)
T a[156:157],v5,0
M v[44:47],a[172:175],v[28:31],v[44:47]
M v[76:79],a[172:175],v[60:63],v[76:79]
T a[158:159],v5,2048
M v[76:79],a[168:171],v[56:59],v[76:79]
s_add_u32 s71, s81, 54272
buffer_load_dwordx4 v11, s[48:51], s71 offen offset:3072 lds
M v[108:111],a[168:171],v[88:91],v[108:111]
T a[160:161],v5,4096
M v[108:111],a[172:175],v[92:95],v[108:111]
M v[140:143],a[172:175],v[124:127],v[140:143]
T a[162:163],v5,6144
M v[140:143],a[168:171],v[120:123],v[140:143]
M v[172:175],a[168:171],v[152:155],v[172:175]
M v[172:175],a[172:175],v[156:159],v[172:175]
s_waitcnt lgkmcnt(6)
M v[40:43],a[148:151],a[0:3],v[40:43]
s_waitcnt lgkmcnt(4)
T a[164:165],v4,8192
M v[40:43],a[152:155],a[4:7],v[40:43]
M v[72:75],a[152:155],a[20:23],v[72:75]
T a[166:167],v4,10240
M v[72:75],a[148:151],a[16:19],v[72:75]
M v[104:107],a[148:151],a[32:35],v[104:107]
T a[168:169],v4,12288
M v[104:107],a[152:155],a[36:39],v[104:107]
M v[136:139],a[152:155],a[52:55],v[136:139]
T a[170:171],v4,14336
M v[136:139],a[148:151],a[48:51],v[136:139]
M v[168:171],a[148:151],a[64:67],v[168:171]
M v[168:171],a[152:155],a[68:71],v[168:171]
s_waitcnt lgkmcnt(6)
M v[44:47],a[156:159],a[0:3],v[44:47]
s_waitcnt lgkmcnt(4)
T a[172:173],v5,8192
M v[44:47],a[160:163],a[4:7],v[44:47]
M v[76:79],a[160:163],a[20:23],v[76:79]
T a[174:175],v5,10240
M v[76:79],a[156:159],a[16:19],v[76:79]
M v[108:111],a[156:159],a[32:35],v[108:111]
T a[148:149],v5,12288
M v[108:111],a[160:163],a[36:39],v[108:111]
M v[140:143],a[160:163],a[52:55],v[140:143]
T a[150:151],v5,14336
M v[140:143],a[156:159],a[48:51],v[140:143]
s_mov_b32 s64, s65
M v[172:175],a[156:159],a[64:67],v[172:175]
s_cmp_eq_u32 s65, 131072
s_cselect_b32 s66, s90, s76
M v[172:175],a[160:163],a[68:71],v[172:175]
s_waitcnt lgkmcnt(6)
s_add_u32 s65, s65, s66
M v[40:43],a[164:167],a[8:11],v[40:43]
s_waitcnt lgkmcnt(4)
v_add_u32 v2, s66, v2
v_add_u32 v3, s66, v3
M v[40:43],a[168:171],a[12:15],v[40:43]
s_add_u32 s31, s27, 1
v_add_u32 v4, s66, v4
v_add_u32 v5, s66, v5
M v[72:75],a[168:171],a[28:31],v[72:75]
s_and_b32 s31, s31, 7
v_mov_b32 v15, s82
M v[72:75],a[164:167],a[24:27],v[72:75]
s_add_u32 s31, s31, s77
M v[104:107],a[164:167],a[40:43],v[104:107]
s_lshl_b32 s31, s31, 7
M v[104:107],a[168:171],a[44:47],v[104:107]
s_waitcnt vmcnt(9) lgkmcnt(0)
M v[136:139],a[168:171],a[60:63],v[136:139]
s_barrier
M v[136:139],a[164:167],a[56:59],v[136:139]
T a[152:153],v2,0
buffer_store_dword v15, v6, s[60:63], s31 offen sc1
M v[168:171],a[164:167],a[72:75],v[168:171]
s_lshl_b32 s81, s91, 7
T a[154:155],v2,2048
M v[168:171],a[168:171],a[76:79],v[168:171]
s_add_u32 s81, s81, s93
T a[156:157],v2,4096
M v[44:47],a[172:175],a[8:11],v[44:47]
s_add_u32 m0, s73, s64
T a[158:159],v2,6144
M v[44:47],a[148:151],a[12:15],v[44:47]
buffer_load_dwordx4 v11, s[48:51], s81 offen lds
M v[76:79],a[148:151],a[28:31],v[76:79]
T a[160:161],v3,0
M v[76:79],a[172:175],a[24:27],v[76:79]
M v[108:111],a[172:175],a[40:43],v[108:111]
T a[162:163],v3,2048
M v[108:111],a[148:151],a[44:47],v[108:111]
M v[140:143],a[148:151],a[60:63],v[140:143]
T a[164:165],v3,4096
M v[140:143],a[172:175],a[56:59],v[140:143]
s_add_u32 s71, s81, 7168
buffer_load_dwordx4 v11, s[48:51], s71 offen offset:1024 lds
M v[172:175],a[172:175],a[72:75],v[172:175]
T a[166:167],v3,6144
M v[172:175],a[148:151],a[76:79],v[172:175]
s_waitcnt lgkmcnt(6)
M v[32:35],a[152:155],v[176:179],0
s_waitcnt lgkmcnt(4)
s_add_u32 s97, s27, 2
T a[168:169],v2,8192
M v[32:35],a[156:159],v[180:183],v[32:35]
s_and_b32 s97, s97, 7
M v[64:67],a[152:155],v[192:195],0
s_mul_i32 s97, s97, 163840
T a[170:171],v2,10240
M v[64:67],a[156:159],v[196:199],v[64:67]
s_add_u32 s72, s81, 14336
s_add_u32 s97, s97, s94
buffer_load_dwordx4 v11, s[48:51], s72 offen offset:2048 lds
M v[96:99],a[152:155],v[208:211],0
s_add_u32 s80, s97, 4096
T a[172:173],v2,12288
C v40,v40,v41
M v[96:99],a[156:159],v[212:215],v[96:99]
C v41,v42,v43
C v42,v44,v45
M v[128:131],a[152:155],v[224:227],0
T a[174:175],v2,14336
C v43,v46,v47
M v[128:131],a[156:159],v[228:231],v[128:131]
buffer_store_dwordx4 v[40:43], v1, s[52:55], s97 offen sc1
M v[160:163],a[152:155],v[240:243],0
C v72,v72,v73
C v73,v74,v75
M v[160:163],a[156:159],v[244:247],v[160:163]
s_waitcnt lgkmcnt(6)
s_add_u32 s71, s81, 21504
buffer_load_dwordx4 v11, s[48:51], s71 offen offset:3072 lds
C v74,v76,v77
M v[36:39],a[160:163],v[176:179],0
s_waitcnt lgkmcnt(4)
T a[148:149],v3,8192
C v75,v78,v79
M v[36:39],a[164:167],v[180:183],v[36:39]
C v104,v104,v105
C v105,v106,v107
M v[68:71],a[160:163],v[192:195],0
T a[150:151],v3,10240
C v106,v108,v109
M v[68:71],a[164:167],v[196:199],v[68:71]
s_add_u32 s72, s81, 32768
buffer_store_dwordx4 v[72:75], v1, s[52:55], s97 offen offset:1024 sc1
M v[100:103],a[160:163],v[208:211],0
s_add_u32 m0, m0, 4096
T a[152:153],v3,12288
C v107,v110,v111
M v[100:103],a[164:167],v[212:215],v[100:103]
buffer_load_dwordx4 v11, s[48:51], s72 offen lds
C v136,v136,v137
M v[132:135],a[160:163],v[224:227],0
T a[154:155],v3,14336
C v137,v138,v139
M v[132:135],a[164:167],v[228:231],v[132:135]
C v138,v140,v141
C v139,v142,v143
M v[164:167],a[160:163],v[240:243],0
buffer_store_dwordx4 v[104:107], v1, s[52:55], s97 offen offset:2048 sc1
M v[164:167],a[164:167],v[244:247],v[164:167]
s_waitcnt lgkmcnt(6)
C v168,v168,v169
C v169,v170,v171
M v[32:35],a[168:171],v[184:187],v[32:35]
s_waitcnt lgkmcnt(4)
T a[156:157],v4,0
C v170,v172,v173
M v[32:35],a[172:175],v[188:191],v[32:35]
s_add_u32 s71, s81, 39936
buffer_load_dwordx4 v11, s[48:51], s71 offen offset:1024 lds
C v171,v174,v175
M v[64:67],a[172:175],v[204:207],v[64:67]
T a[158:159],v4,2048
M v[64:67],a[168:171],v[200:203],v[64:67]
buffer_store_dwordx4 v[136:139], v1, s[52:55], s97 offen offset:3072 sc1
M v[96:99],a[168:171],v[216:219],v[96:99]
T a[160:161],v4,4096
M v[96:99],a[172:175],v[220:223],v[96:99]
M v[128:131],a[172:175],v[236:239],v[128:131]
T a[162:163],v4,6144
M v[128:131],a[168:171],v[232:235],v[128:131]
s_add_u32 s72, s81, 47104
buffer_load_dwordx4 v11, s[48:51], s72 offen offset:2048 lds
M v[160:163],a[168:171],v[248:251],v[160:163]
buffer_store_dwordx4 v[168:171], v1, s[52:55], s80 offen sc1
M v[160:163],a[172:175],v[252:255],v[160:163]
s_waitcnt lgkmcnt(6)
M v[36:39],a[148:151],v[184:187],v[36:39]
s_waitcnt lgkmcnt(4)
T a[164:165],v5,0
M v[36:39],a[152:155],v[188:191],v[36:39]
M v[68:71],a[152:155],v[204:207],v[68:71]
T a[166:167],v5,2048
M v[68:71],a[148:151],v[200:203],v[68:71]
s_add_u32 s71, s81, 54272
buffer_load_dwordx4 v11, s[48:51], s71 offen offset:3072 lds
M v[100:103],a[148:151],v[216:219],v[100:103]
T a[168:169],v5,4096
M v[100:103],a[152:155],v[220:223],v[100:103]
M v[132:135],a[152:155],v[236:239],v[132:135]
T a[170:171],v5,6144
M v[132:135],a[148:151],v[232:235],v[132:135]
M v[164:167],a[148:151],v[248:251],v[164:167]
M v[164:167],a[152:155],v[252:255],v[164:167]
s_waitcnt lgkmcnt(6)
M v[32:35],a[156:159],a[176:179],v[32:35]
s_waitcnt lgkmcnt(4)
T a[172:173],v4,8192
M v[32:35],a[160:163],a[180:183],v[32:35]
M v[64:67],a[160:163],a[196:199],v[64:67]
T a[174:175],v4,10240
M v[64:67],a[156:159],a[192:195],v[64:67]
M v[96:99],a[156:159],a[208:211],v[96:99]
T a[148:149],v4,12288
M v[96:99],a[160:163],a[212:215],v[96:99]
M v[128:131],a[160:163],a[228:231],v[128:131]
T a[150:151],v4,14336
M v[128:131],a[156:159],a[224:227],v[128:131]
M v[160:163],a[156:159],a[240:243],v[160:163]
M v[160:163],a[160:163],a[244:247],v[160:163]
s_waitcnt lgkmcnt(6)
M v[36:39],a[164:167],a[176:179],v[36:39]
s_waitcnt lgkmcnt(4)
T a[152:153],v5,8192
M v[36:39],a[168:171],a[180:183],v[36:39]
M v[68:71],a[168:171],a[196:199],v[68:71]
T a[154:155],v5,10240
M v[68:71],a[164:167],a[192:195],v[68:71]
M v[100:103],a[164:167],a[208:211],v[100:103]
T a[156:157],v5,12288
M v[100:103],a[168:171],a[212:215],v[100:103]
M v[132:135],a[168:171],a[228:231],v[132:135]
T a[158:159],v5,14336
M v[132:135],a[164:167],a[224:227],v[132:135]
s_mov_b32 s64, s65
M v[164:167],a[164:167],a[240:243],v[164:167]
s_cmp_eq_u32 s65, 131072
s_cselect_b32 s66, s90, s76
M v[164:167],a[168:171],a[244:247],v[164:167]
s_waitcnt lgkmcnt(6)
s_add_u32 s65, s65, s66
M v[32:35],a[172:175],a[184:187],v[32:35]
s_waitcnt lgkmcnt(4)
v_add_u32 v2, s66, v2
v_add_u32 v3, s66, v3
M v[32:35],a[148:151],a[188:191],v[32:35]
v_add_u32 v4, s66, v4
v_add_u32 v5, s66, v5
M v[64:67],a[148:151],a[204:207],v[64:67]
M v[64:67],a[172:175],a[200:203],v[64:67]
M v[96:99],a[172:175],a[216:219],v[96:99]
M v[96:99],a[148:151],a[220:223],v[96:99]
s_waitcnt lgkmcnt(0)
M v[128:131],a[148:151],a[236:239],v[128:131]
s_barrier
M v[128:131],a[172:175],a[232:235],v[128:131]
T a[160:161],v2,0
M v[160:163],a[172:175],a[248:251],v[160:163]
s_add_u32 s81, s81, 2097152
s_add_u32 s91, s91, 1
T a[162:163],v2,2048
M v[160:163],a[148:151],a[252:255],v[160:163]
s_and_b32 s91, s91, 7
T a[164:165],v2,4096
M v[36:39],a[152:155],a[184:187],v[36:39]
s_add_u32 m0, s73, s64
T a[166:167],v2,6144
M v[36:39],a[156:159],a[188:191],v[36:39]
buffer_load_dwordx4 v11, s[48:51], s81 offen lds
M v[68:71],a[156:159],a[204:207],v[68:71]
T a[168:169],v3,0
M v[68:71],a[152:155],a[200:203],v[68:71]
M v[100:103],a[152:155],a[216:219],v[100:103]
T a[170:171],v3,2048
M v[100:103],a[156:159],a[220:223],v[100:103]
M v[132:135],a[156:159],a[236:239],v[132:135]
T a[172:173],v3,4096
M v[132:135],a[152:155],a[232:235],v[132:135]
s_add_u32 s71, s81, 7168
buffer_load_dwordx4 v11, s[48:51], s71 offen offset:1024 lds
M v[164:167],a[152:155],a[248:251],v[164:167]
T a[174:175],v3,6144
M v[164:167],a[156:159],a[252:255],v[164:167]
s_waitcnt lgkmcnt(6)
M v[32:35],a[160:163],v[16:19],v[32:35]
s_waitcnt lgkmcnt(4)
T a[148:149],v2,8192
M v[32:35],a[164:167],v[20:23],v[32:35]
M v[64:67],a[160:163],v[48:51],v[64:67]
T a[150:151],v2,10240
M v[64:67],a[164:167],v[52:55],v[64:67]
s_add_u32 s72, s81, 14336
buffer_load_dwordx4 v11, s[48:51], s72 offen offset:2048 lds
M v[96:99],a[160:163],v[80:83],v[96:99]
T a[152:153],v2,12288
M v[96:99],a[164:167],v[84:87],v[96:99]
M v[128:131],a[160:163],v[112:115],v[128:131]
T a[154:155],v2,14336
M v[128:131],a[164:167],v[116:119],v[128:131]
M v[160:163],a[160:163],v[144:147],v[160:163]
M v[160:163],a[164:167],v[148:151],v[160:163]
s_waitcnt lgkmcnt(6)
s_add_u32 s71, s81, 21504
buffer_load_dwordx4 v11, s[48:51], s71 offen offset:3072 lds
M v[36:39],a[168:171],v[16:19],v[36:39]
s_waitcnt lgkmcnt(4)
T a[156:157],v3,8192
M v[36:39],a[172:175],v[20:23],v[36:39]
M v[68:71],a[168:171],v[48:51],v[68:71]
T a[158:159],v3,10240
M v[68:71],a[172:175],v[52:55],v[68:71]
s_add_u32 s72, s81, 32768
M v[100:103],a[168:171],v[80:83],v[100:103]
s_add_u32 m0, m0, 4096
T a[160:161],v3,12288
M v[100:103],a[172:175],v[84:87],v[100:103]
buffer_load_dwordx4 v11, s[48:51], s72 offen lds
M v[132:135],a[168:171],v[112:115],v[132:135]
T a[162:163],v3,14336
M v[132:135],a[172:175],v[116:119],v[132:135]
M v[164:167],a[168:171],v[144:147],v[164:167]
M v[164:167],a[172:175],v[148:151],v[164:167]
s_waitcnt lgkmcnt(6)
M v[32:35],a[148:151],v[24:27],v[32:35]
s_waitcnt lgkmcnt(4)
T a[164:165],v4,0
M v[32:35],a[152:155],v[28:31],v[32:35]
s_add_u32 s71, s81, 39936
buffer_load_dwordx4 v11, s[48:51], s71 offen offset:1024 lds
M v[64:67],a[152:155],v[60:63],v[64:67]
T a[166:167],v4,2048
M v[64:67],a[148:151],v[56:59],v[64:67]
M v[96:99],a[148:151],v[88:91],v[96:99]
T a[168:169],v4,4096
M v[96:99],a[152:155],v[92:95],v[96:99]
M v[128:131],a[152:155],v[124:127],v[128:131]
T a[170:171],v4,6144
M v[128:131],a[148:151],v[120:123],v[128:131]
s_add_u32 s72, s81, 47104
buffer_load_dwordx4 v11, s[48:51], s72 offen offset:2048 lds
M v[160:163],a[148:151],v[152:155],v[160:163]
M v[160:163],a[152:155],v[156:159],v[160:163]
s_waitcnt lgkmcnt(6)
M v[36:39],a[156:159],v[24:27],v[36:39]
s_waitcnt lgkmcnt(4)
T a[172:173],v5,0
M v[36:39],a[160:163],v[28:31],v[36:39]
M v[68:71],a[160:163],v[60:63],v[68:71]
T a[174:175],v5,2048
M v[68:71],a[156:159],v[56:59],v[68:71]
s_add_u32 s71, s81, 54272
buffer_load_dwordx4 v11, s[48:51], s71 offen offset:3072 lds
M v[100:103],a[156:159],v[88:91],v[100:103]
T a[148:149],v5,4096
M v[100:103],a[160:163],v[92:95],v[100:103]
M v[132:135],a[160:163],v[124:127],v[132:135]
T a[150:151],v5,6144
M v[132:135],a[156:159],v[120:123],v[132:135]
M v[164:167],a[156:159],v[152:155],v[164:167]
s_lshl_b32 s67, s30, 3
M v[164:167],a[160:163],v[156:159],v[164:167]
s_waitcnt lgkmcnt(6)
s_add_u32 s67, s67, s27
M v[32:35],a[164:167],a[0:3],v[32:35]
s_waitcnt lgkmcnt(4)
s_lshl_b32 s67, s67, 7
T a[152:153],v4,8192
M v[32:35],a[168:171],a[4:7],v[32:35]
s_add_u32 s69, s27, 7
M v[64:67],a[168:171],a[20:23],v[64:67]
T a[154:155],v4,10240
v_lshrrev_b32 v8, 4, v1
M v[64:67],a[164:167],a[16:19],v[64:67]
v_min_u32 v8, 0, v8
v_max_u32 v8, 0, v8
M v[96:99],a[164:167],a[32:35],v[96:99]
T a[156:157],v4,12288
v_sub_u32 v8, s69, v8
M v[96:99],a[168:171],a[36:39],v[96:99]
v_and_b32 v8, 7, v8
)"
R"(v_lshlrev_b32 v8, 2, v8
M v[128:131],a[168:171],a[52:55],v[128:131]
T a[158:159],v4,14336
buffer_load_dword v10, v8, s[60:63], s67 offen sc1
M v[128:131],a[164:167],a[48:51],v[128:131]
M v[160:163],a[164:167],a[64:67],v[160:163]
M v[160:163],a[168:171],a[68:71],v[160:163]
s_waitcnt lgkmcnt(6)
M v[36:39],a[172:175],a[0:3],v[36:39]
s_waitcnt lgkmcnt(4)
T a[160:161],v5,8192
M v[36:39],a[148:151],a[4:7],v[36:39]
M v[68:71],a[148:151],a[20:23],v[68:71]
T a[162:163],v5,10240
M v[68:71],a[172:175],a[16:19],v[68:71]
M v[100:103],a[172:175],a[32:35],v[100:103]
T a[164:165],v5,12288
M v[100:103],a[148:151],a[36:39],v[100:103]
M v[132:135],a[148:151],a[52:55],v[132:135]
T a[166:167],v5,14336
M v[132:135],a[172:175],a[48:51],v[132:135]
s_mov_b32 s64, s65
M v[164:167],a[172:175],a[64:67],v[164:167]
s_cmp_eq_u32 s65, 131072
s_cselect_b32 s66, s90, s76
M v[164:167],a[148:151],a[68:71],v[164:167]
s_waitcnt lgkmcnt(6)
s_add_u32 s65, s65, s66
M v[32:35],a[152:155],a[8:11],v[32:35]
s_waitcnt lgkmcnt(4)
v_add_u32 v2, s66, v2
v_add_u32 v3, s66, v3
M v[32:35],a[156:159],a[12:15],v[32:35]
s_add_u32 s31, s27, 2
v_add_u32 v4, s66, v4
v_add_u32 v5, s66, v5
M v[64:67],a[156:159],a[28:31],v[64:67]
s_and_b32 s31, s31, 7
v_mov_b32 v15, s83
M v[64:67],a[152:155],a[24:27],v[64:67]
s_add_u32 s31, s31, s77
M v[96:99],a[152:155],a[40:43],v[96:99]
s_lshl_b32 s31, s31, 7
M v[96:99],a[156:159],a[44:47],v[96:99]
s_waitcnt vmcnt(10) lgkmcnt(0)
M v[128:131],a[156:159],a[60:63],v[128:131]
s_barrier
M v[128:131],a[152:155],a[56:59],v[128:131]
T a[168:169],v2,0
buffer_store_dword v15, v6, s[60:63], s31 offen sc1
M v[160:163],a[152:155],a[72:75],v[160:163]
s_lshl_b32 s81, s91, 7
T a[170:171],v2,2048
M v[160:163],a[156:159],a[76:79],v[160:163]
s_add_u32 s81, s81, s93
T a[172:173],v2,4096
M v[36:39],a[160:163],a[8:11],v[36:39]
s_add_u32 m0, s73, s64
T a[174:175],v2,6144
M v[36:39],a[164:167],a[12:15],v[36:39]
buffer_load_dwordx4 v11, s[48:51], s81 offen lds
M v[68:71],a[164:167],a[28:31],v[68:71]
T a[148:149],v3,0
M v[68:71],a[160:163],a[24:27],v[68:71]
M v[100:103],a[160:163],a[40:43],v[100:103]
T a[150:151],v3,2048
M v[100:103],a[164:167],a[44:47],v[100:103]
M v[132:135],a[164:167],a[60:63],v[132:135]
T a[152:153],v3,4096
M v[132:135],a[160:163],a[56:59],v[132:135]
s_add_u32 s71, s81, 7168
buffer_load_dwordx4 v11, s[48:51], s71 offen offset:1024 lds
M v[164:167],a[160:163],a[72:75],v[164:167]
T a[154:155],v3,6144
M v[164:167],a[164:167],a[76:79],v[164:167]
s_waitcnt lgkmcnt(6)
M v[40:43],a[168:171],v[176:179],0
s_waitcnt lgkmcnt(4)
s_add_u32 s97, s27, 3
T a[156:157],v2,8192
M v[40:43],a[172:175],v[180:183],v[40:43]
s_and_b32 s97, s97, 7
M v[72:75],a[168:171],v[192:195],0
s_mul_i32 s97, s97, 163840
T a[158:159],v2,10240
M v[72:75],a[172:175],v[196:199],v[72:75]
s_add_u32 s72, s81, 14336
s_add_u32 s97, s97, s94
buffer_load_dwordx4 v11, s[48:51], s72 offen offset:2048 lds
M v[104:107],a[168:171],v[208:211],0
s_add_u32 s80, s97, 4096
T a[160:161],v2,12288
C v32,v32,v33
M v[104:107],a[172:175],v[212:215],v[104:107]
C v33,v34,v35
C v34,v36,v37
M v[136:139],a[168:171],v[224:227],0
T a[162:163],v2,14336
C v35,v38,v39
M v[136:139],a[172:175],v[228:231],v[136:139]
buffer_store_dwordx4 v[32:35], v1, s[52:55], s97 offen sc1
M v[168:171],a[168:171],v[240:243],0
C v64,v64,v65
C v65,v66,v67
M v[168:171],a[172:175],v[244:247],v[168:171]
s_waitcnt lgkmcnt(6)
s_add_u32 s71, s81, 21504
buffer_load_dwordx4 v11, s[48:51], s71 offen offset:3072 lds
C v66,v68,v69
M v[44:47],a[148:151],v[176:179],0
s_waitcnt lgkmcnt(4)
T a[164:165],v3,8192
C v67,v70,v71
M v[44:47],a[152:155],v[180:183],v[44:47]
C v96,v96,v97
C v97,v98,v99
M v[76:79],a[148:151],v[192:195],0
T a[166:167],v3,10240
C v98,v100,v101
M v[76:79],a[152:155],v[196:199],v[76:79]
s_add_u32 s72, s81, 32768
buffer_store_dwordx4 v[64:67], v1, s[52:55], s97 offen offset:1024 sc1
M v[108:111],a[148:151],v[208:211],0
s_add_u32 m0, m0, 4096
T a[168:169],v3,12288
C v99,v102,v103
M v[108:111],a[152:155],v[212:215],v[108:111]
buffer_load_dwordx4 v11, s[48:51], s72 offen lds
C v128,v128,v129
M v[140:143],a[148:151],v[224:227],0
T a[170:171],v3,14336
C v129,v130,v131
M v[140:143],a[152:155],v[228:231],v[140:143]
C v130,v132,v133
C v131,v134,v135
M v[172:175],a[148:151],v[240:243],0
buffer_store_dwordx4 v[96:99], v1, s[52:55], s97 offen offset:2048 sc1
M v[172:175],a[152:155],v[244:247],v[172:175]
s_waitcnt lgkmcnt(6)
C v160,v160,v161
C v161,v162,v163
M v[40:43],a[156:159],v[184:187],v[40:43]
s_waitcnt lgkmcnt(4)
T a[172:173],v4,0
C v162,v164,v165
M v[40:43],a[160:163],v[188:191],v[40:43]
s_add_u32 s71, s81, 39936
buffer_load_dwordx4 v11, s[48:51], s71 offen offset:1024 lds
C v163,v166,v167
M v[72:75],a[160:163],v[204:207],v[72:75]
T a[174:175],v4,2048
M v[72:75],a[156:159],v[200:203],v[72:75]
buffer_store_dwordx4 v[128:131], v1, s[52:55], s97 offen offset:3072 sc1
M v[104:107],a[156:159],v[216:219],v[104:107]
T a[148:149],v4,4096
M v[104:107],a[160:163],v[220:223],v[104:107]
M v[136:139],a[160:163],v[236:239],v[136:139]
T a[150:151],v4,6144
M v[136:139],a[156:159],v[232:235],v[136:139]
s_add_u32 s72, s81, 47104
buffer_load_dwordx4 v11, s[48:51], s72 offen offset:2048 lds
M v[168:171],a[156:159],v[248:251],v[168:171]
buffer_store_dwordx4 v[160:163], v1, s[52:55], s80 offen sc1
M v[168:171],a[160:163],v[252:255],v[168:171]
s_waitcnt lgkmcnt(6)
M v[44:47],a[164:167],v[184:187],v[44:47]
s_waitcnt lgkmcnt(4)
T a[152:153],v5,0
M v[44:47],a[168:171],v[188:191],v[44:47]
M v[76:79],a[168:171],v[204:207],v[76:79]
s_waitcnt vmcnt(13)
T a[154:155],v5,2048
v_cmp_ne_u32 vcc, s89, v10
M v[76:79],a[164:167],v[200:203],v[76:79]
s_cmp_eq_u64 vcc, 0
s_cbranch_scc0 L_pl1002
L_pd1002:
s_add_u32 s71, s81, 54272
buffer_load_dwordx4 v11, s[48:51], s71 offen offset:3072 lds
M v[108:111],a[164:167],v[216:219],v[108:111]
T a[156:157],v5,4096
M v[108:111],a[168:171],v[220:223],v[108:111]
s_add_u32 s96, s27, 7
M v[140:143],a[168:171],v[236:239],v[140:143]
s_and_b32 s96, s96, 7
T a[158:159],v5,6144
M v[140:143],a[164:167],v[232:235],v[140:143]
s_mul_i32 s96, s96, 20480
M v[172:175],a[164:167],v[248:251],v[172:175]
s_add_u32 s96, s96, s68
buffer_load_dwordx4 a[120:123], v1, s[52:55], s96 offen sc1
M v[172:175],a[168:171],v[252:255],v[172:175]
s_waitcnt lgkmcnt(6)
M v[40:43],a[172:175],a[176:179],v[40:43]
s_waitcnt lgkmcnt(4)
T a[160:161],v4,8192
M v[40:43],a[148:151],a[180:183],v[40:43]
M v[72:75],a[148:151],a[196:199],v[72:75]
T a[162:163],v4,10240
M v[72:75],a[172:175],a[192:195],v[72:75]
buffer_load_dwordx4 a[124:127], v1, s[52:55], s96 offen offset:1024 sc1
M v[104:107],a[172:175],a[208:211],v[104:107]
T a[164:165],v4,12288
M v[104:107],a[148:151],a[212:215],v[104:107]
M v[136:139],a[148:151],a[228:231],v[136:139]
T a[166:167],v4,14336
M v[136:139],a[172:175],a[224:227],v[136:139]
M v[168:171],a[172:175],a[240:243],v[168:171]
buffer_load_dwordx4 a[128:131], v1, s[52:55], s96 offen offset:2048 sc1
M v[168:171],a[148:151],a[244:247],v[168:171]
s_waitcnt lgkmcnt(6)
M v[44:47],a[152:155],a[176:179],v[44:47]
s_waitcnt lgkmcnt(4)
T a[168:169],v5,8192
M v[44:47],a[156:159],a[180:183],v[44:47]
M v[76:79],a[156:159],a[196:199],v[76:79]
T a[170:171],v5,10240
M v[76:79],a[152:155],a[192:195],v[76:79]
buffer_load_dwordx4 a[132:135], v1, s[52:55], s96 offen offset:3072 sc1
M v[108:111],a[152:155],a[208:211],v[108:111]
T a[172:173],v5,12288
M v[108:111],a[156:159],a[212:215],v[108:111]
M v[140:143],a[156:159],a[228:231],v[140:143]
T a[174:175],v5,14336
M v[140:143],a[152:155],a[224:227],v[140:143]
s_mov_b32 s64, s65
M v[172:175],a[152:155],a[240:243],v[172:175]
s_cmp_eq_u32 s65, 131072
s_cselect_b32 s66, s90, s76
s_add_u32 s96, s96, 4096
buffer_load_dwordx4 a[136:139], v1, s[52:55], s96 offen sc1
M v[172:175],a[156:159],a[244:247],v[172:175]
s_waitcnt lgkmcnt(6)
s_add_u32 s65, s65, s66
M v[40:43],a[160:163],a[184:187],v[40:43]
s_waitcnt lgkmcnt(4)
v_add_u32 v2, s66, v2
v_add_u32 v3, s66, v3
M v[40:43],a[164:167],a[188:191],v[40:43]
v_add_u32 v4, s66, v4
v_add_u32 v5, s66, v5
M v[72:75],a[164:167],a[204:207],v[72:75]
M v[72:75],a[160:163],a[200:203],v[72:75]
M v[104:107],a[160:163],a[216:219],v[104:107]
M v[104:107],a[164:167],a[220:223],v[104:107]
s_waitcnt lgkmcnt(0)
M v[136:139],a[164:167],a[236:239],v[136:139]
s_barrier
M v[136:139],a[160:163],a[232:235],v[136:139]
T a[148:149],v2,0
M v[168:171],a[160:163],a[248:251],v[168:171]
s_add_u32 s81, s81, 2097152
s_add_u32 s91, s91, 1
T a[150:151],v2,2048
M v[168:171],a[164:167],a[252:255],v[168:171]
s_and_b32 s91, s91, 7
T a[152:153],v2,4096
M v[44:47],a[168:171],a[184:187],v[44:47]
s_add_u32 m0, s73, s64
T a[154:155],v2,6144
M v[44:47],a[172:175],a[188:191],v[44:47]
buffer_load_dwordx4 v11, s[48:51], s81 offen lds
M v[76:79],a[172:175],a[204:207],v[76:79]
T a[156:157],v3,0
M v[76:79],a[168:171],a[200:203],v[76:79]
M v[108:111],a[168:171],a[216:219],v[108:111]
T a[158:159],v3,2048
M v[108:111],a[172:175],a[220:223],v[108:111]
M v[140:143],a[172:175],a[236:239],v[140:143]
T a[160:161],v3,4096
M v[140:143],a[168:171],a[232:235],v[140:143]
s_add_u32 s71, s81, 7168
buffer_load_dwordx4 v11, s[48:51], s71 offen offset:1024 lds
M v[172:175],a[168:171],a[248:251],v[172:175]
T a[162:163],v3,6144
M v[172:175],a[172:175],a[252:255],v[172:175]
s_waitcnt lgkmcnt(6)
M v[40:43],a[148:151],v[16:19],v[40:43]
s_waitcnt lgkmcnt(4)
T a[164:165],v2,8192
M v[40:43],a[152:155],v[20:23],v[40:43]
M v[72:75],a[148:151],v[48:51],v[72:75]
T a[166:167],v2,10240
M v[72:75],a[152:155],v[52:55],v[72:75]
s_add_u32 s72, s81, 14336
buffer_load_dwordx4 v11, s[48:51], s72 offen offset:2048 lds
M v[104:107],a[148:151],v[80:83],v[104:107]
T a[168:169],v2,12288
M v[104:107],a[152:155],v[84:87],v[104:107]
M v[136:139],a[148:151],v[112:115],v[136:139]
T a[170:171],v2,14336
M v[136:139],a[152:155],v[116:119],v[136:139]
M v[168:171],a[148:151],v[144:147],v[168:171]
M v[168:171],a[152:155],v[148:151],v[168:171]
s_waitcnt lgkmcnt(6)
s_add_u32 s71, s81, 21504
buffer_load_dwordx4 v11, s[48:51], s71 offen offset:3072 lds
M v[44:47],a[156:159],v[16:19],v[44:47]
s_waitcnt lgkmcnt(4)
T a[172:173],v3,8192
M v[44:47],a[160:163],v[20:23],v[44:47]
M v[76:79],a[156:159],v[48:51],v[76:79]
T a[174:175],v3,10240
M v[76:79],a[160:163],v[52:55],v[76:79]
s_add_u32 s72, s81, 32768
M v[108:111],a[156:159],v[80:83],v[108:111]
s_add_u32 m0, m0, 4096
T a[148:149],v3,12288
M v[108:111],a[160:163],v[84:87],v[108:111]
buffer_load_dwordx4 v11, s[48:51], s72 offen lds
M v[140:143],a[156:159],v[112:115],v[140:143]
T a[150:151],v3,14336
M v[140:143],a[160:163],v[116:119],v[140:143]
M v[172:175],a[156:159],v[144:147],v[172:175]
M v[172:175],a[160:163],v[148:151],v[172:175]
s_waitcnt lgkmcnt(6)
M v[40:43],a[164:167],v[24:27],v[40:43]
s_waitcnt lgkmcnt(4)
T a[152:153],v4,0
M v[40:43],a[168:171],v[28:31],v[40:43]
s_add_u32 s71, s81, 39936
buffer_load_dwordx4 v11, s[48:51], s71 offen offset:1024 lds
M v[72:75],a[168:171],v[60:63],v[72:75]
T a[154:155],v4,2048
M v[72:75],a[164:167],v[56:59],v[72:75]
M v[104:107],a[164:167],v[88:91],v[104:107]
T a[156:157],v4,4096
M v[104:107],a[168:171],v[92:95],v[104:107]
M v[136:139],a[168:171],v[124:127],v[136:139]
T a[158:159],v4,6144
M v[136:139],a[164:167],v[120:123],v[136:139]
s_add_u32 s72, s81, 47104
buffer_load_dwordx4 v11, s[48:51], s72 offen offset:2048 lds
M v[168:171],a[164:167],v[152:155],v[168:171]
M v[168:171],a[168:171],v[156:159],v[168:171]
s_waitcnt lgkmcnt(6)
M v[44:47],a[172:175],v[24:27],v[44:47]
s_waitcnt lgkmcnt(4)
T a[160:161],v5,0
M v[44:47],a[148:151],v[28:31],v[44:47]
M v[76:79],a[148:151],v[60:63],v[76:79]
T a[162:163],v5,2048
M v[76:79],a[172:175],v[56:59],v[76:79]
s_add_u32 s71, s81, 54272
buffer_load_dwordx4 v11, s[48:51], s71 offen offset:3072 lds
M v[108:111],a[172:175],v[88:91],v[108:111]
T a[164:165],v5,4096
M v[108:111],a[148:151],v[92:95],v[108:111]
M v[140:143],a[148:151],v[124:127],v[140:143]
T a[166:167],v5,6144
M v[140:143],a[172:175],v[120:123],v[140:143]
M v[172:175],a[172:175],v[152:155],v[172:175]
s_lshl_b32 s67, s30, 3
M v[172:175],a[148:151],v[156:159],v[172:175]
s_waitcnt lgkmcnt(6)
s_add_u32 s67, s67, s27
M v[40:43],a[152:155],a[0:3],v[40:43]
s_waitcnt lgkmcnt(4)
s_lshl_b32 s67, s67, 7
T a[168:169],v4,8192
M v[40:43],a[156:159],a[4:7],v[40:43]
s_add_u32 s69, s27, 7
M v[72:75],a[156:159],a[20:23],v[72:75]
T a[170:171],v4,10240
v_lshrrev_b32 v8, 4, v1
M v[72:75],a[152:155],a[16:19],v[72:75]
v_min_u32 v8, 1, v8
v_max_u32 v8, 1, v8
M v[104:107],a[152:155],a[32:35],v[104:107]
T a[172:173],v4,12288
v_sub_u32 v8, s69, v8
M v[104:107],a[156:159],a[36:39],v[104:107]
v_and_b32 v8, 7, v8
v_lshlrev_b32 v8, 2, v8
M v[136:139],a[156:159],a[52:55],v[136:139]
T a[174:175],v4,14336
buffer_load_dword v10, v8, s[60:63], s67 offen sc1
M v[136:139],a[152:155],a[48:51],v[136:139]
M v[168:171],a[152:155],a[64:67],v[168:171]
M v[168:171],a[156:159],a[68:71],v[168:171]
s_waitcnt lgkmcnt(6)
M v[44:47],a[160:163],a[0:3],v[44:47]
s_waitcnt lgkmcnt(4)
T a[148:149],v5,8192
M v[44:47],a[164:167],a[4:7],v[44:47]
M v[76:79],a[164:167],a[20:23],v[76:79]
T a[150:151],v5,10240
M v[76:79],a[160:163],a[16:19],v[76:79]
M v[108:111],a[160:163],a[32:35],v[108:111]
T a[152:153],v5,12288
M v[108:111],a[164:167],a[36:39],v[108:111]
M v[140:143],a[164:167],a[52:55],v[140:143]
T a[154:155],v5,14336
M v[140:143],a[160:163],a[48:51],v[140:143]
s_mov_b32 s64, s65
M v[172:175],a[160:163],a[64:67],v[172:175]
s_cmp_eq_u32 s65, 131072
s_cselect_b32 s66, s90, s76
M v[172:175],a[164:167],a[68:71],v[172:175]
s_add_u32 s65, s65, s66
s_waitcnt vmcnt(13)
M a[80:83],a[140:143],a[120:123],0
M a[84:87],a[144:147],a[120:123],0
s_waitcnt vmcnt(12)
M a[88:91],a[140:143],a[124:127],0
M a[92:95],a[144:147],a[124:127],0
s_waitcnt vmcnt(11)
M a[96:99],a[140:143],a[128:131],0
M a[100:103],a[144:147],a[128:131],0
s_waitcnt vmcnt(10)
M a[104:107],a[140:143],a[132:135],0
M a[108:111],a[144:147],a[132:135],0
s_waitcnt vmcnt(9)
M a[112:115],a[140:143],a[136:139],0
M a[116:119],a[144:147],a[136:139],0
s_waitcnt lgkmcnt(6)
M v[40:43],a[168:171],a[8:11],v[40:43]
s_waitcnt lgkmcnt(4)
v_add_u32 v2, s66, v2
v_add_u32 v3, s66, v3
M v[40:43],a[172:175],a[12:15],v[40:43]
s_add_u32 s31, s27, 3
v_add_u32 v4, s66, v4
v_add_u32 v5, s66, v5
M v[72:75],a[172:175],a[28:31],v[72:75]
s_and_b32 s31, s31, 7
v_mov_b32 v15, s84
M v[72:75],a[168:171],a[24:27],v[72:75]
s_add_u32 s31, s31, s77
M v[104:107],a[168:171],a[40:43],v[104:107]
s_lshl_b32 s31, s31, 7
M v[104:107],a[172:175],a[44:47],v[104:107]
s_waitcnt lgkmcnt(0)
M v[136:139],a[172:175],a[60:63],v[136:139]
s_barrier
M v[136:139],a[168:171],a[56:59],v[136:139]
T a[156:157],v2,0
buffer_store_dword v15, v6, s[60:63], s31 offen sc1
M v[168:171],a[168:171],a[72:75],v[168:171]
s_lshl_b32 s81, s91, 7
T a[158:159],v2,2048
M v[168:171],a[172:175],a[76:79],v[168:171]
s_add_u32 s81, s81, s93
T a[160:161],v2,4096
M v[44:47],a[148:151],a[8:11],v[44:47]
s_add_u32 m0, s73, s64
T a[162:163],v2,6144
M v[44:47],a[152:155],a[12:15],v[44:47]
buffer_load_dwordx4 v11, s[48:51], s81 offen lds
M v[76:79],a[152:155],a[28:31],v[76:79]
T a[164:165],v3,0
M v[76:79],a[148:151],a[24:27],v[76:79]
M v[108:111],a[148:151],a[40:43],v[108:111]
T a[166:167],v3,2048
M v[108:111],a[152:155],a[44:47],v[108:111]
M v[140:143],a[152:155],a[60:63],v[140:143]
T a[168:169],v3,4096
M v[140:143],a[148:151],a[56:59],v[140:143]
s_add_u32 s71, s81, 7168
buffer_load_dwordx4 v11, s[48:51], s71 offen offset:1024 lds
M v[172:175],a[148:151],a[72:75],v[172:175]
T a[170:171],v3,6144
M v[172:175],a[152:155],a[76:79],v[172:175]
s_waitcnt lgkmcnt(6)
M v[32:35],a[156:159],v[176:179],0
s_waitcnt lgkmcnt(4)
s_add_u32 s97, s27, 4
T a[172:173],v2,8192
M v[32:35],a[160:163],v[180:183],v[32:35]
s_and_b32 s97, s97, 7
M v[64:67],a[156:159],v[192:195],0
s_mul_i32 s97, s97, 163840
T a[174:175],v2,10240
M v[64:67],a[160:163],v[196:199],v[64:67]
s_add_u32 s72, s81, 14336
s_add_u32 s97, s97, s94
buffer_load_dwordx4 v11, s[48:51], s72 offen offset:2048 lds
M v[96:99],a[156:159],v[208:211],0
s_add_u32 s80, s97, 4096
T a[148:149],v2,12288
C v40,v40,v41
M v[96:99],a[160:163],v[212:215],v[96:99]
C v41,v42,v43
C v42,v44,v45
M v[128:131],a[156:159],v[224:227],0
T a[150:151],v2,14336
C v43,v46,v47
M v[128:131],a[160:163],v[228:231],v[128:131]
buffer_store_dwordx4 v[40:43], v1, s[52:55], s97 offen sc1
M v[160:163],a[156:159],v[240:243],0
C v72,v72,v73
C v73,v74,v75
M v[160:163],a[160:163],v[244:247],v[160:163]
s_waitcnt lgkmcnt(6)
s_add_u32 s71, s81, 21504
buffer_load_dwordx4 v11, s[48:51], s71 offen offset:3072 lds
C v74,v76,v77
M v[36:39],a[164:167],v[176:179],0
s_waitcnt lgkmcnt(4)
T a[152:153],v3,8192
)"
R"(C v75,v78,v79
M v[36:39],a[168:171],v[180:183],v[36:39]
C v104,v104,v105
C v105,v106,v107
M v[68:71],a[164:167],v[192:195],0
T a[154:155],v3,10240
C v106,v108,v109
M v[68:71],a[168:171],v[196:199],v[68:71]
s_add_u32 s72, s81, 32768
buffer_store_dwordx4 v[72:75], v1, s[52:55], s97 offen offset:1024 sc1
M v[100:103],a[164:167],v[208:211],0
s_add_u32 m0, m0, 4096
T a[156:157],v3,12288
C v107,v110,v111
M v[100:103],a[168:171],v[212:215],v[100:103]
buffer_load_dwordx4 v11, s[48:51], s72 offen lds
C v136,v136,v137
M v[132:135],a[164:167],v[224:227],0
T a[158:159],v3,14336
C v137,v138,v139
M v[132:135],a[168:171],v[228:231],v[132:135]
C v138,v140,v141
C v139,v142,v143
M v[164:167],a[164:167],v[240:243],0
buffer_store_dwordx4 v[104:107], v1, s[52:55], s97 offen offset:2048 sc1
M v[164:167],a[168:171],v[244:247],v[164:167]
s_waitcnt lgkmcnt(6)
C v168,v168,v169
C v169,v170,v171
M v[32:35],a[172:175],v[184:187],v[32:35]
s_waitcnt lgkmcnt(4)
T a[160:161],v4,0
C v170,v172,v173
M v[32:35],a[148:151],v[188:191],v[32:35]
s_add_u32 s71, s81, 39936
buffer_load_dwordx4 v11, s[48:51], s71 offen offset:1024 lds
C v171,v174,v175
M v[64:67],a[148:151],v[204:207],v[64:67]
T a[162:163],v4,2048
M v[64:67],a[172:175],v[200:203],v[64:67]
buffer_store_dwordx4 v[136:139], v1, s[52:55], s97 offen offset:3072 sc1
M v[96:99],a[172:175],v[216:219],v[96:99]
T a[164:165],v4,4096
M v[96:99],a[148:151],v[220:223],v[96:99]
M v[128:131],a[148:151],v[236:239],v[128:131]
T a[166:167],v4,6144
M v[128:131],a[172:175],v[232:235],v[128:131]
s_add_u32 s72, s81, 47104
buffer_load_dwordx4 v11, s[48:51], s72 offen offset:2048 lds
M v[160:163],a[172:175],v[248:251],v[160:163]
buffer_store_dwordx4 v[168:171], v1, s[52:55], s80 offen sc1
M v[160:163],a[148:151],v[252:255],v[160:163]
s_waitcnt lgkmcnt(6)
M v[36:39],a[152:155],v[184:187],v[36:39]
s_waitcnt lgkmcnt(4)
T a[168:169],v5,0
M v[36:39],a[156:159],v[188:191],v[36:39]
M v[68:71],a[156:159],v[204:207],v[68:71]
s_waitcnt vmcnt(13)
T a[170:171],v5,2048
v_cmp_ne_u32 vcc, s89, v10
M v[68:71],a[152:155],v[200:203],v[68:71]
s_cmp_eq_u64 vcc, 0
s_cbranch_scc0 L_pl1003
L_pd1003:
s_add_u32 s71, s81, 54272
buffer_load_dwordx4 v11, s[48:51], s71 offen offset:3072 lds
M v[100:103],a[152:155],v[216:219],v[100:103]
T a[172:173],v5,4096
M v[100:103],a[156:159],v[220:223],v[100:103]
s_add_u32 s96, s27, 6
M v[132:135],a[156:159],v[236:239],v[132:135]
s_and_b32 s96, s96, 7
T a[174:175],v5,6144
M v[132:135],a[152:155],v[232:235],v[132:135]
s_mul_i32 s96, s96, 20480
M v[164:167],a[152:155],v[248:251],v[164:167]
s_add_u32 s96, s96, s68
buffer_load_dwordx4 a[120:123], v1, s[52:55], s96 offen sc1
M v[164:167],a[156:159],v[252:255],v[164:167]
s_waitcnt lgkmcnt(6)
M v[32:35],a[160:163],a[176:179],v[32:35]
s_waitcnt lgkmcnt(4)
T a[148:149],v4,8192
M v[32:35],a[164:167],a[180:183],v[32:35]
M v[64:67],a[164:167],a[196:199],v[64:67]
T a[150:151],v4,10240
M v[64:67],a[160:163],a[192:195],v[64:67]
buffer_load_dwordx4 a[124:127], v1, s[52:55], s96 offen offset:1024 sc1
M v[96:99],a[160:163],a[208:211],v[96:99]
T a[152:153],v4,12288
M v[96:99],a[164:167],a[212:215],v[96:99]
M v[128:131],a[164:167],a[228:231],v[128:131]
T a[154:155],v4,14336
M v[128:131],a[160:163],a[224:227],v[128:131]
M v[160:163],a[160:163],a[240:243],v[160:163]
buffer_load_dwordx4 a[128:131], v1, s[52:55], s96 offen offset:2048 sc1
M v[160:163],a[164:167],a[244:247],v[160:163]
s_waitcnt lgkmcnt(6)
M v[36:39],a[168:171],a[176:179],v[36:39]
s_waitcnt lgkmcnt(4)
T a[156:157],v5,8192
M v[36:39],a[172:175],a[180:183],v[36:39]
M v[68:71],a[172:175],a[196:199],v[68:71]
T a[158:159],v5,10240
M v[68:71],a[168:171],a[192:195],v[68:71]
buffer_load_dwordx4 a[132:135], v1, s[52:55], s96 offen offset:3072 sc1
M v[100:103],a[168:171],a[208:211],v[100:103]
T a[160:161],v5,12288
M v[100:103],a[172:175],a[212:215],v[100:103]
M v[132:135],a[172:175],a[228:231],v[132:135]
T a[162:163],v5,14336
M v[132:135],a[168:171],a[224:227],v[132:135]
s_mov_b32 s64, s65
M v[164:167],a[168:171],a[240:243],v[164:167]
s_cmp_eq_u32 s65, 131072
s_cselect_b32 s66, s90, s76
s_add_u32 s96, s96, 4096
buffer_load_dwordx4 a[136:139], v1, s[52:55], s96 offen sc1
M v[164:167],a[172:175],a[244:247],v[164:167]
s_waitcnt lgkmcnt(6)
s_add_u32 s65, s65, s66
M v[32:35],a[148:151],a[184:187],v[32:35]
s_waitcnt lgkmcnt(4)
v_add_u32 v2, s66, v2
v_add_u32 v3, s66, v3
M v[32:35],a[152:155],a[188:191],v[32:35]
v_add_u32 v4, s66, v4
v_add_u32 v5, s66, v5
M v[64:67],a[152:155],a[204:207],v[64:67]
M v[64:67],a[148:151],a[200:203],v[64:67]
M v[96:99],a[148:151],a[216:219],v[96:99]
M v[96:99],a[152:155],a[220:223],v[96:99]
s_waitcnt lgkmcnt(0)
M v[128:131],a[152:155],a[236:239],v[128:131]
s_barrier
M v[128:131],a[148:151],a[232:235],v[128:131]
T a[164:165],v2,0
M v[160:163],a[148:151],a[248:251],v[160:163]
s_add_u32 s81, s81, 2097152
s_add_u32 s91, s91, 1
T a[166:167],v2,2048
M v[160:163],a[152:155],a[252:255],v[160:163]
s_and_b32 s91, s91, 7
T a[168:169],v2,4096
M v[36:39],a[156:159],a[184:187],v[36:39]
s_add_u32 m0, s73, s64
T a[170:171],v2,6144
M v[36:39],a[160:163],a[188:191],v[36:39]
buffer_load_dwordx4 v11, s[48:51], s81 offen lds
M v[68:71],a[160:163],a[204:207],v[68:71]
T a[172:173],v3,0
M v[68:71],a[156:159],a[200:203],v[68:71]
M v[100:103],a[156:159],a[216:219],v[100:103]
T a[174:175],v3,2048
M v[100:103],a[160:163],a[220:223],v[100:103]
M v[132:135],a[160:163],a[236:239],v[132:135]
T a[148:149],v3,4096
M v[132:135],a[156:159],a[232:235],v[132:135]
s_add_u32 s71, s81, 7168
buffer_load_dwordx4 v11, s[48:51], s71 offen offset:1024 lds
M v[164:167],a[156:159],a[248:251],v[164:167]
T a[150:151],v3,6144
M v[164:167],a[160:163],a[252:255],v[164:167]
s_waitcnt lgkmcnt(6)
M v[32:35],a[164:167],v[16:19],v[32:35]
s_waitcnt lgkmcnt(4)
T a[152:153],v2,8192
M v[32:35],a[168:171],v[20:23],v[32:35]
M v[64:67],a[164:167],v[48:51],v[64:67]
T a[154:155],v2,10240
M v[64:67],a[168:171],v[52:55],v[64:67]
s_add_u32 s72, s81, 14336
buffer_load_dwordx4 v11, s[48:51], s72 offen offset:2048 lds
M v[96:99],a[164:167],v[80:83],v[96:99]
T a[156:157],v2,12288
M v[96:99],a[168:171],v[84:87],v[96:99]
M v[128:131],a[164:167],v[112:115],v[128:131]
T a[158:159],v2,14336
M v[128:131],a[168:171],v[116:119],v[128:131]
M v[160:163],a[164:167],v[144:147],v[160:163]
M v[160:163],a[168:171],v[148:151],v[160:163]
s_waitcnt lgkmcnt(6)
s_add_u32 s71, s81, 21504
buffer_load_dwordx4 v11, s[48:51], s71 offen offset:3072 lds
M v[36:39],a[172:175],v[16:19],v[36:39]
s_waitcnt lgkmcnt(4)
T a[160:161],v3,8192
M v[36:39],a[148:151],v[20:23],v[36:39]
M v[68:71],a[172:175],v[48:51],v[68:71]
T a[162:163],v3,10240
M v[68:71],a[148:151],v[52:55],v[68:71]
s_add_u32 s72, s81, 32768
M v[100:103],a[172:175],v[80:83],v[100:103]
s_add_u32 m0, m0, 4096
T a[164:165],v3,12288
M v[100:103],a[148:151],v[84:87],v[100:103]
buffer_load_dwordx4 v11, s[48:51], s72 offen lds
M v[132:135],a[172:175],v[112:115],v[132:135]
T a[166:167],v3,14336
M v[132:135],a[148:151],v[116:119],v[132:135]
M v[164:167],a[172:175],v[144:147],v[164:167]
M v[164:167],a[148:151],v[148:151],v[164:167]
s_waitcnt lgkmcnt(6)
M v[32:35],a[152:155],v[24:27],v[32:35]
s_waitcnt lgkmcnt(4)
T a[168:169],v4,0
M v[32:35],a[156:159],v[28:31],v[32:35]
s_add_u32 s71, s81, 39936
buffer_load_dwordx4 v11, s[48:51], s71 offen offset:1024 lds
M v[64:67],a[156:159],v[60:63],v[64:67]
T a[170:171],v4,2048
M v[64:67],a[152:155],v[56:59],v[64:67]
M v[96:99],a[152:155],v[88:91],v[96:99]
T a[172:173],v4,4096
M v[96:99],a[156:159],v[92:95],v[96:99]
M v[128:131],a[156:159],v[124:127],v[128:131]
T a[174:175],v4,6144
M v[128:131],a[152:155],v[120:123],v[128:131]
s_add_u32 s72, s81, 47104
buffer_load_dwordx4 v11, s[48:51], s72 offen offset:2048 lds
M v[160:163],a[152:155],v[152:155],v[160:163]
M v[160:163],a[156:159],v[156:159],v[160:163]
s_waitcnt lgkmcnt(6)
M v[36:39],a[160:163],v[24:27],v[36:39]
s_waitcnt lgkmcnt(4)
T a[148:149],v5,0
M v[36:39],a[164:167],v[28:31],v[36:39]
M v[68:71],a[164:167],v[60:63],v[68:71]
T a[150:151],v5,2048
M v[68:71],a[160:163],v[56:59],v[68:71]
s_add_u32 s71, s81, 54272
buffer_load_dwordx4 v11, s[48:51], s71 offen offset:3072 lds
M v[100:103],a[160:163],v[88:91],v[100:103]
T a[152:153],v5,4096
M v[100:103],a[164:167],v[92:95],v[100:103]
M v[132:135],a[164:167],v[124:127],v[132:135]
T a[154:155],v5,6144
M v[132:135],a[160:163],v[120:123],v[132:135]
M v[164:167],a[160:163],v[152:155],v[164:167]
s_lshl_b32 s67, s30, 3
M v[164:167],a[164:167],v[156:159],v[164:167]
s_waitcnt lgkmcnt(6)
s_add_u32 s67, s67, s27
M v[32:35],a[168:171],a[0:3],v[32:35]
s_waitcnt lgkmcnt(4)
s_lshl_b32 s67, s67, 7
T a[156:157],v4,8192
M v[32:35],a[172:175],a[4:7],v[32:35]
s_add_u32 s69, s27, 7
M v[64:67],a[172:175],a[20:23],v[64:67]
T a[158:159],v4,10240
v_lshrrev_b32 v8, 4, v1
M v[64:67],a[168:171],a[16:19],v[64:67]
v_min_u32 v8, 2, v8
v_max_u32 v8, 2, v8
M v[96:99],a[168:171],a[32:35],v[96:99]
T a[160:161],v4,12288
v_sub_u32 v8, s69, v8
M v[96:99],a[172:175],a[36:39],v[96:99]
v_and_b32 v8, 7, v8
v_lshlrev_b32 v8, 2, v8
M v[128:131],a[172:175],a[52:55],v[128:131]
T a[162:163],v4,14336
buffer_load_dword v10, v8, s[60:63], s67 offen sc1
M v[128:131],a[168:171],a[48:51],v[128:131]
M v[160:163],a[168:171],a[64:67],v[160:163]
M v[160:163],a[172:175],a[68:71],v[160:163]
s_waitcnt lgkmcnt(6)
M v[36:39],a[148:151],a[0:3],v[36:39]
s_waitcnt lgkmcnt(4)
T a[164:165],v5,8192
M v[36:39],a[152:155],a[4:7],v[36:39]
M v[68:71],a[152:155],a[20:23],v[68:71]
T a[166:167],v5,10240
M v[68:71],a[148:151],a[16:19],v[68:71]
M v[100:103],a[148:151],a[32:35],v[100:103]
T a[168:169],v5,12288
M v[100:103],a[152:155],a[36:39],v[100:103]
M v[132:135],a[152:155],a[52:55],v[132:135]
T a[170:171],v5,14336
M v[132:135],a[148:151],a[48:51],v[132:135]
s_mov_b32 s64, s65
M v[164:167],a[148:151],a[64:67],v[164:167]
s_cmp_eq_u32 s65, 131072
s_cselect_b32 s66, s90, s76
M v[164:167],a[152:155],a[68:71],v[164:167]
s_add_u32 s65, s65, s66
s_waitcnt vmcnt(13)
M a[80:83],a[140:143],a[120:123],a[80:83]
M a[84:87],a[144:147],a[120:123],a[84:87]
s_waitcnt vmcnt(12)
M a[88:91],a[140:143],a[124:127],a[88:91]
M a[92:95],a[144:147],a[124:127],a[92:95]
s_waitcnt vmcnt(11)
M a[96:99],a[140:143],a[128:131],a[96:99]
M a[100:103],a[144:147],a[128:131],a[100:103]
s_waitcnt vmcnt(10)
M a[104:107],a[140:143],a[132:135],a[104:107]
M a[108:111],a[144:147],a[132:135],a[108:111]
s_waitcnt vmcnt(9)
M a[112:115],a[140:143],a[136:139],a[112:115]
M a[116:119],a[144:147],a[136:139],a[116:119]
s_waitcnt lgkmcnt(6)
M v[32:35],a[156:159],a[8:11],v[32:35]
s_waitcnt lgkmcnt(4)
v_add_u32 v2, s66, v2
v_add_u32 v3, s66, v3
M v[32:35],a[160:163],a[12:15],v[32:35]
s_add_u32 s31, s27, 4
v_add_u32 v4, s66, v4
v_add_u32 v5, s66, v5
M v[64:67],a[160:163],a[28:31],v[64:67]
s_and_b32 s31, s31, 7
v_mov_b32 v15, s85
M v[64:67],a[156:159],a[24:27],v[64:67]
s_add_u32 s31, s31, s77
M v[96:99],a[156:159],a[40:43],v[96:99]
s_lshl_b32 s31, s31, 7
M v[96:99],a[160:163],a[44:47],v[96:99]
s_waitcnt lgkmcnt(0)
M v[128:131],a[160:163],a[60:63],v[128:131]
s_barrier
M v[128:131],a[156:159],a[56:59],v[128:131]
T a[172:173],v2,0
buffer_store_dword v15, v6, s[60:63], s31 offen sc1
M v[160:163],a[156:159],a[72:75],v[160:163]
s_lshl_b32 s81, s91, 7
T a[174:175],v2,2048
M v[160:163],a[160:163],a[76:79],v[160:163]
s_add_u32 s81, s81, s93
T a[148:149],v2,4096
M v[36:39],a[164:167],a[8:11],v[36:39]
s_add_u32 m0, s73, s64
T a[150:151],v2,6144
M v[36:39],a[168:171],a[12:15],v[36:39]
buffer_load_dwordx4 v11, s[48:51], s81 offen lds
M v[68:71],a[168:171],a[28:31],v[68:71]
T a[152:153],v3,0
M v[68:71],a[164:167],a[24:27],v[68:71]
M v[100:103],a[164:167],a[40:43],v[100:103]
T a[154:155],v3,2048
M v[100:103],a[168:171],a[44:47],v[100:103]
M v[132:135],a[168:171],a[60:63],v[132:135]
T a[156:157],v3,4096
M v[132:135],a[164:167],a[56:59],v[132:135]
s_add_u32 s71, s81, 7168
buffer_load_dwordx4 v11, s[48:51], s71 offen offset:1024 lds
M v[164:167],a[164:167],a[72:75],v[164:167]
T a[158:159],v3,6144
M v[164:167],a[168:171],a[76:79],v[164:167]
s_waitcnt lgkmcnt(6)
M v[40:43],a[172:175],v[176:179],0
s_waitcnt lgkmcnt(4)
s_add_u32 s97, s27, 5
T a[160:161],v2,8192
M v[40:43],a[148:151],v[180:183],v[40:43]
s_and_b32 s97, s97, 7
M v[72:75],a[172:175],v[192:195],0
s_mul_i32 s97, s97, 163840
T a[162:163],v2,10240
M v[72:75],a[148:151],v[196:199],v[72:75]
s_add_u32 s72, s81, 14336
s_add_u32 s97, s97, s94
buffer_load_dwordx4 v11, s[48:51], s72 offen offset:2048 lds
M v[104:107],a[172:175],v[208:211],0
s_add_u32 s80, s97, 4096
T a[164:165],v2,12288
C v32,v32,v33
M v[104:107],a[148:151],v[212:215],v[104:107]
C v33,v34,v35
C v34,v36,v37
M v[136:139],a[172:175],v[224:227],0
T a[166:167],v2,14336
C v35,v38,v39
M v[136:139],a[148:151],v[228:231],v[136:139]
buffer_store_dwordx4 v[32:35], v1, s[52:55], s97 offen sc1
M v[168:171],a[172:175],v[240:243],0
C v64,v64,v65
C v65,v66,v67
M v[168:171],a[148:151],v[244:247],v[168:171]
s_waitcnt lgkmcnt(6)
s_add_u32 s71, s81, 21504
buffer_load_dwordx4 v11, s[48:51], s71 offen offset:3072 lds
C v66,v68,v69
M v[44:47],a[152:155],v[176:179],0
s_waitcnt lgkmcnt(4)
T a[168:169],v3,8192
C v67,v70,v71
M v[44:47],a[156:159],v[180:183],v[44:47]
C v96,v96,v97
C v97,v98,v99
M v[76:79],a[152:155],v[192:195],0
T a[170:171],v3,10240
C v98,v100,v101
M v[76:79],a[156:159],v[196:199],v[76:79]
s_add_u32 s72, s81, 32768
buffer_store_dwordx4 v[64:67], v1, s[52:55], s97 offen offset:1024 sc1
M v[108:111],a[152:155],v[208:211],0
s_add_u32 m0, m0, 4096
T a[172:173],v3,12288
C v99,v102,v103
M v[108:111],a[156:159],v[212:215],v[108:111]
buffer_load_dwordx4 v11, s[48:51], s72 offen lds
C v128,v128,v129
M v[140:143],a[152:155],v[224:227],0
T a[174:175],v3,14336
C v129,v130,v131
M v[140:143],a[156:159],v[228:231],v[140:143]
C v130,v132,v133
C v131,v134,v135
M v[172:175],a[152:155],v[240:243],0
buffer_store_dwordx4 v[96:99], v1, s[52:55], s97 offen offset:2048 sc1
M v[172:175],a[156:159],v[244:247],v[172:175]
s_waitcnt lgkmcnt(6)
C v160,v160,v161
C v161,v162,v163
M v[40:43],a[160:163],v[184:187],v[40:43]
s_waitcnt lgkmcnt(4)
T a[148:149],v4,0
C v162,v164,v165
M v[40:43],a[164:167],v[188:191],v[40:43]
s_add_u32 s71, s81, 39936
buffer_load_dwordx4 v11, s[48:51], s71 offen offset:1024 lds
C v163,v166,v167
M v[72:75],a[164:167],v[204:207],v[72:75]
T a[150:151],v4,2048
M v[72:75],a[160:163],v[200:203],v[72:75]
buffer_store_dwordx4 v[128:131], v1, s[52:55], s97 offen offset:3072 sc1
M v[104:107],a[160:163],v[216:219],v[104:107]
T a[152:153],v4,4096
M v[104:107],a[164:167],v[220:223],v[104:107]
M v[136:139],a[164:167],v[236:239],v[136:139]
T a[154:155],v4,6144
M v[136:139],a[160:163],v[232:235],v[136:139]
s_add_u32 s72, s81, 47104
buffer_load_dwordx4 v11, s[48:51], s72 offen offset:2048 lds
M v[168:171],a[160:163],v[248:251],v[168:171]
buffer_store_dwordx4 v[160:163], v1, s[52:55], s80 offen sc1
M v[168:171],a[164:167],v[252:255],v[168:171]
s_waitcnt lgkmcnt(6)
M v[44:47],a[168:171],v[184:187],v[44:47]
s_waitcnt lgkmcnt(4)
T a[156:157],v5,0
M v[44:47],a[172:175],v[188:191],v[44:47]
M v[76:79],a[172:175],v[204:207],v[76:79]
s_waitcnt vmcnt(13)
T a[158:159],v5,2048
v_cmp_ne_u32 vcc, s89, v10
M v[76:79],a[168:171],v[200:203],v[76:79]
s_cmp_eq_u64 vcc, 0
s_cbranch_scc0 L_pl1004
L_pd1004:
s_add_u32 s71, s81, 54272
buffer_load_dwordx4 v11, s[48:51], s71 offen offset:3072 lds
M v[108:111],a[168:171],v[216:219],v[108:111]
T a[160:161],v5,4096
M v[108:111],a[172:175],v[220:223],v[108:111]
s_add_u32 s96, s27, 5
M v[140:143],a[172:175],v[236:239],v[140:143]
s_and_b32 s96, s96, 7
T a[162:163],v5,6144
M v[140:143],a[168:171],v[232:235],v[140:143]
s_mul_i32 s96, s96, 20480
M v[172:175],a[168:171],v[248:251],v[172:175]
s_add_u32 s96, s96, s68
buffer_load_dwordx4 a[120:123], v1, s[52:55], s96 offen sc1
M v[172:175],a[172:175],v[252:255],v[172:175]
s_waitcnt lgkmcnt(6)
M v[40:43],a[148:151],a[176:179],v[40:43]
s_waitcnt lgkmcnt(4)
T a[164:165],v4,8192
M v[40:43],a[152:155],a[180:183],v[40:43]
M v[72:75],a[152:155],a[196:199],v[72:75]
T a[166:167],v4,10240
M v[72:75],a[148:151],a[192:195],v[72:75]
buffer_load_dwordx4 a[124:127], v1, s[52:55], s96 offen offset:1024 sc1
M v[104:107],a[148:151],a[208:211],v[104:107]
T a[168:169],v4,12288
M v[104:107],a[152:155],a[212:215],v[104:107]
M v[136:139],a[152:155],a[228:231],v[136:139]
T a[170:171],v4,14336
M v[136:139],a[148:151],a[224:227],v[136:139]
M v[168:171],a[148:151],a[240:243],v[168:171]
buffer_load_dwordx4 a[128:131], v1, s[52:55], s96 offen offset:2048 sc1
M v[168:171],a[152:155],a[244:247],v[168:171]
s_waitcnt lgkmcnt(6)
M v[44:47],a[156:159],a[176:179],v[44:47]
s_waitcnt lgkmcnt(4)
T a[172:173],v5,8192
M v[44:47],a[160:163],a[180:183],v[44:47]
M v[76:79],a[160:163],a[196:199],v[76:79]
T a[174:175],v5,10240
M v[76:79],a[156:159],a[192:195],v[76:79]
buffer_load_dwordx4 a[132:135], v1, s[52:55], s96 offen offset:3072 sc1
M v[108:111],a[156:159],a[208:211],v[108:111]
T a[148:149],v5,12288
M v[108:111],a[160:163],a[212:215],v[108:111]
M v[140:143],a[160:163],a[228:231],v[140:143]
T a[150:151],v5,14336
)"
R"(M v[140:143],a[156:159],a[224:227],v[140:143]
s_mov_b32 s64, s65
M v[172:175],a[156:159],a[240:243],v[172:175]
s_cmp_eq_u32 s65, 131072
s_cselect_b32 s66, s90, s76
s_add_u32 s96, s96, 4096
buffer_load_dwordx4 a[136:139], v1, s[52:55], s96 offen sc1
M v[172:175],a[160:163],a[244:247],v[172:175]
s_waitcnt lgkmcnt(6)
s_add_u32 s65, s65, s66
M v[40:43],a[164:167],a[184:187],v[40:43]
s_waitcnt lgkmcnt(4)
v_add_u32 v2, s66, v2
v_add_u32 v3, s66, v3
M v[40:43],a[168:171],a[188:191],v[40:43]
v_add_u32 v4, s66, v4
v_add_u32 v5, s66, v5
M v[72:75],a[168:171],a[204:207],v[72:75]
M v[72:75],a[164:167],a[200:203],v[72:75]
M v[104:107],a[164:167],a[216:219],v[104:107]
M v[104:107],a[168:171],a[220:223],v[104:107]
s_waitcnt lgkmcnt(0)
M v[136:139],a[168:171],a[236:239],v[136:139]
s_barrier
M v[136:139],a[164:167],a[232:235],v[136:139]
T a[152:153],v2,0
M v[168:171],a[164:167],a[248:251],v[168:171]
s_add_u32 s81, s81, 2097152
s_add_u32 s91, s91, 1
T a[154:155],v2,2048
M v[168:171],a[168:171],a[252:255],v[168:171]
s_and_b32 s91, s91, 7
T a[156:157],v2,4096
M v[44:47],a[172:175],a[184:187],v[44:47]
s_add_u32 m0, s73, s64
T a[158:159],v2,6144
M v[44:47],a[148:151],a[188:191],v[44:47]
buffer_load_dwordx4 v11, s[48:51], s81 offen lds
M v[76:79],a[148:151],a[204:207],v[76:79]
T a[160:161],v3,0
M v[76:79],a[172:175],a[200:203],v[76:79]
M v[108:111],a[172:175],a[216:219],v[108:111]
T a[162:163],v3,2048
M v[108:111],a[148:151],a[220:223],v[108:111]
M v[140:143],a[148:151],a[236:239],v[140:143]
T a[164:165],v3,4096
M v[140:143],a[172:175],a[232:235],v[140:143]
s_add_u32 s71, s81, 7168
buffer_load_dwordx4 v11, s[48:51], s71 offen offset:1024 lds
M v[172:175],a[172:175],a[248:251],v[172:175]
T a[166:167],v3,6144
M v[172:175],a[148:151],a[252:255],v[172:175]
s_waitcnt lgkmcnt(6)
M v[40:43],a[152:155],v[16:19],v[40:43]
s_waitcnt lgkmcnt(4)
T a[168:169],v2,8192
M v[40:43],a[156:159],v[20:23],v[40:43]
M v[72:75],a[152:155],v[48:51],v[72:75]
T a[170:171],v2,10240
M v[72:75],a[156:159],v[52:55],v[72:75]
s_add_u32 s72, s81, 14336
buffer_load_dwordx4 v11, s[48:51], s72 offen offset:2048 lds
M v[104:107],a[152:155],v[80:83],v[104:107]
T a[172:173],v2,12288
M v[104:107],a[156:159],v[84:87],v[104:107]
M v[136:139],a[152:155],v[112:115],v[136:139]
T a[174:175],v2,14336
M v[136:139],a[156:159],v[116:119],v[136:139]
M v[168:171],a[152:155],v[144:147],v[168:171]
M v[168:171],a[156:159],v[148:151],v[168:171]
s_waitcnt lgkmcnt(6)
s_add_u32 s71, s81, 21504
buffer_load_dwordx4 v11, s[48:51], s71 offen offset:3072 lds
M v[44:47],a[160:163],v[16:19],v[44:47]
s_waitcnt lgkmcnt(4)
T a[148:149],v3,8192
M v[44:47],a[164:167],v[20:23],v[44:47]
M v[76:79],a[160:163],v[48:51],v[76:79]
T a[150:151],v3,10240
M v[76:79],a[164:167],v[52:55],v[76:79]
s_add_u32 s72, s81, 32768
M v[108:111],a[160:163],v[80:83],v[108:111]
s_add_u32 m0, m0, 4096
T a[152:153],v3,12288
M v[108:111],a[164:167],v[84:87],v[108:111]
buffer_load_dwordx4 v11, s[48:51], s72 offen lds
M v[140:143],a[160:163],v[112:115],v[140:143]
T a[154:155],v3,14336
M v[140:143],a[164:167],v[116:119],v[140:143]
M v[172:175],a[160:163],v[144:147],v[172:175]
M v[172:175],a[164:167],v[148:151],v[172:175]
s_waitcnt lgkmcnt(6)
M v[40:43],a[168:171],v[24:27],v[40:43]
s_waitcnt lgkmcnt(4)
T a[156:157],v4,0
M v[40:43],a[172:175],v[28:31],v[40:43]
s_add_u32 s71, s81, 39936
buffer_load_dwordx4 v11, s[48:51], s71 offen offset:1024 lds
M v[72:75],a[172:175],v[60:63],v[72:75]
T a[158:159],v4,2048
M v[72:75],a[168:171],v[56:59],v[72:75]
M v[104:107],a[168:171],v[88:91],v[104:107]
T a[160:161],v4,4096
M v[104:107],a[172:175],v[92:95],v[104:107]
M v[136:139],a[172:175],v[124:127],v[136:139]
T a[162:163],v4,6144
M v[136:139],a[168:171],v[120:123],v[136:139]
s_add_u32 s72, s81, 47104
buffer_load_dwordx4 v11, s[48:51], s72 offen offset:2048 lds
M v[168:171],a[168:171],v[152:155],v[168:171]
M v[168:171],a[172:175],v[156:159],v[168:171]
s_waitcnt lgkmcnt(6)
M v[44:47],a[148:151],v[24:27],v[44:47]
s_waitcnt lgkmcnt(4)
T a[164:165],v5,0
M v[44:47],a[152:155],v[28:31],v[44:47]
M v[76:79],a[152:155],v[60:63],v[76:79]
T a[166:167],v5,2048
M v[76:79],a[148:151],v[56:59],v[76:79]
s_add_u32 s71, s81, 54272
buffer_load_dwordx4 v11, s[48:51], s71 offen offset:3072 lds
M v[108:111],a[148:151],v[88:91],v[108:111]
T a[168:169],v5,4096
M v[108:111],a[152:155],v[92:95],v[108:111]
M v[140:143],a[152:155],v[124:127],v[140:143]
T a[170:171],v5,6144
M v[140:143],a[148:151],v[120:123],v[140:143]
M v[172:175],a[148:151],v[152:155],v[172:175]
s_lshl_b32 s67, s30, 3
M v[172:175],a[152:155],v[156:159],v[172:175]
s_waitcnt lgkmcnt(6)
s_add_u32 s67, s67, s27
M v[40:43],a[156:159],a[0:3],v[40:43]
s_waitcnt lgkmcnt(4)
s_lshl_b32 s67, s67, 7
T a[172:173],v4,8192
M v[40:43],a[160:163],a[4:7],v[40:43]
s_add_u32 s69, s27, 7
M v[72:75],a[160:163],a[20:23],v[72:75]
T a[174:175],v4,10240
v_lshrrev_b32 v8, 4, v1
M v[72:75],a[156:159],a[16:19],v[72:75]
v_min_u32 v8, 3, v8
v_max_u32 v8, 3, v8
M v[104:107],a[156:159],a[32:35],v[104:107]
T a[148:149],v4,12288
v_sub_u32 v8, s69, v8
M v[104:107],a[160:163],a[36:39],v[104:107]
v_and_b32 v8, 7, v8
v_lshlrev_b32 v8, 2, v8
M v[136:139],a[160:163],a[52:55],v[136:139]
T a[150:151],v4,14336
buffer_load_dword v10, v8, s[60:63], s67 offen sc1
M v[136:139],a[156:159],a[48:51],v[136:139]
M v[168:171],a[156:159],a[64:67],v[168:171]
M v[168:171],a[160:163],a[68:71],v[168:171]
s_waitcnt lgkmcnt(6)
M v[44:47],a[164:167],a[0:3],v[44:47]
s_waitcnt lgkmcnt(4)
T a[152:153],v5,8192
M v[44:47],a[168:171],a[4:7],v[44:47]
M v[76:79],a[168:171],a[20:23],v[76:79]
T a[154:155],v5,10240
M v[76:79],a[164:167],a[16:19],v[76:79]
M v[108:111],a[164:167],a[32:35],v[108:111]
T a[156:157],v5,12288
M v[108:111],a[168:171],a[36:39],v[108:111]
M v[140:143],a[168:171],a[52:55],v[140:143]
T a[158:159],v5,14336
M v[140:143],a[164:167],a[48:51],v[140:143]
s_mov_b32 s64, s65
M v[172:175],a[164:167],a[64:67],v[172:175]
s_cmp_eq_u32 s65, 131072
s_cselect_b32 s66, s90, s76
M v[172:175],a[168:171],a[68:71],v[172:175]
s_add_u32 s65, s65, s66
s_waitcnt vmcnt(13)
M a[80:83],a[140:143],a[120:123],a[80:83]
M a[84:87],a[144:147],a[120:123],a[84:87]
s_waitcnt vmcnt(12)
M a[88:91],a[140:143],a[124:127],a[88:91]
M a[92:95],a[144:147],a[124:127],a[92:95]
s_waitcnt vmcnt(11)
M a[96:99],a[140:143],a[128:131],a[96:99]
M a[100:103],a[144:147],a[128:131],a[100:103]
s_waitcnt vmcnt(10)
M a[104:107],a[140:143],a[132:135],a[104:107]
M a[108:111],a[144:147],a[132:135],a[108:111]
s_waitcnt vmcnt(9)
M a[112:115],a[140:143],a[136:139],a[112:115]
M a[116:119],a[144:147],a[136:139],a[116:119]
s_waitcnt lgkmcnt(6)
M v[40:43],a[172:175],a[8:11],v[40:43]
s_waitcnt lgkmcnt(4)
v_add_u32 v2, s66, v2
v_add_u32 v3, s66, v3
M v[40:43],a[148:151],a[12:15],v[40:43]
s_add_u32 s31, s27, 5
v_add_u32 v4, s66, v4
v_add_u32 v5, s66, v5
M v[72:75],a[148:151],a[28:31],v[72:75]
s_and_b32 s31, s31, 7
v_mov_b32 v15, s86
M v[72:75],a[172:175],a[24:27],v[72:75]
s_add_u32 s31, s31, s77
M v[104:107],a[172:175],a[40:43],v[104:107]
s_lshl_b32 s31, s31, 7
M v[104:107],a[148:151],a[44:47],v[104:107]
s_waitcnt lgkmcnt(0)
M v[136:139],a[148:151],a[60:63],v[136:139]
s_barrier
M v[136:139],a[172:175],a[56:59],v[136:139]
T a[160:161],v2,0
buffer_store_dword v15, v6, s[60:63], s31 offen sc1
M v[168:171],a[172:175],a[72:75],v[168:171]
T a[162:163],v2,2048
M v[168:171],a[148:151],a[76:79],v[168:171]
T a[164:165],v2,4096
M v[44:47],a[152:155],a[8:11],v[44:47]
T a[166:167],v2,6144
M v[44:47],a[156:159],a[12:15],v[44:47]
M v[76:79],a[156:159],a[28:31],v[76:79]
T a[168:169],v3,0
M v[76:79],a[152:155],a[24:27],v[76:79]
M v[108:111],a[152:155],a[40:43],v[108:111]
T a[170:171],v3,2048
M v[108:111],a[156:159],a[44:47],v[108:111]
M v[140:143],a[156:159],a[60:63],v[140:143]
T a[172:173],v3,4096
M v[140:143],a[152:155],a[56:59],v[140:143]
M v[172:175],a[152:155],a[72:75],v[172:175]
T a[174:175],v3,6144
M v[172:175],a[156:159],a[76:79],v[172:175]
s_waitcnt lgkmcnt(6)
M v[32:35],a[160:163],v[176:179],0
s_waitcnt lgkmcnt(4)
s_add_u32 s97, s27, 6
T a[148:149],v2,8192
M v[32:35],a[164:167],v[180:183],v[32:35]
s_and_b32 s97, s97, 7
M v[64:67],a[160:163],v[192:195],0
s_mul_i32 s97, s97, 163840
T a[150:151],v2,10240
M v[64:67],a[164:167],v[196:199],v[64:67]
s_add_u32 s97, s97, s94
C v40,v40,v41
C v41,v42,v43
M v[96:99],a[160:163],v[208:211],0
s_add_u32 s80, s97, 4096
T a[152:153],v2,12288
C v42,v44,v45
M v[96:99],a[164:167],v[212:215],v[96:99]
C v43,v46,v47
C v72,v72,v73
M v[128:131],a[160:163],v[224:227],0
T a[154:155],v2,14336
C v73,v74,v75
M v[128:131],a[164:167],v[228:231],v[128:131]
buffer_store_dwordx4 v[40:43], v1, s[52:55], s97 offen sc1
M v[160:163],a[160:163],v[240:243],0
C v74,v76,v77
C v75,v78,v79
M v[160:163],a[164:167],v[244:247],v[160:163]
s_waitcnt lgkmcnt(6)
C v104,v104,v105
C v105,v106,v107
M v[36:39],a[168:171],v[176:179],0
s_waitcnt lgkmcnt(4)
T a[156:157],v3,8192
C v106,v108,v109
M v[36:39],a[172:175],v[180:183],v[36:39]
C v107,v110,v111
C v136,v136,v137
M v[68:71],a[168:171],v[192:195],0
T a[158:159],v3,10240
C v137,v138,v139
M v[68:71],a[172:175],v[196:199],v[68:71]
buffer_store_dwordx4 v[72:75], v1, s[52:55], s97 offen offset:1024 sc1
M v[100:103],a[168:171],v[208:211],0
T a[160:161],v3,12288
C v138,v140,v141
M v[100:103],a[172:175],v[212:215],v[100:103]
C v139,v142,v143
C v168,v168,v169
M v[132:135],a[168:171],v[224:227],0
T a[162:163],v3,14336
C v169,v170,v171
M v[132:135],a[172:175],v[228:231],v[132:135]
buffer_store_dwordx4 v[104:107], v1, s[52:55], s97 offen offset:2048 sc1
M v[164:167],a[168:171],v[240:243],0
C v170,v172,v173
C v171,v174,v175
M v[164:167],a[172:175],v[244:247],v[164:167]
s_waitcnt lgkmcnt(6)
M v[32:35],a[148:151],v[184:187],v[32:35]
s_waitcnt lgkmcnt(4)
T a[164:165],v4,0
M v[32:35],a[152:155],v[188:191],v[32:35]
M v[64:67],a[152:155],v[204:207],v[64:67]
T a[166:167],v4,2048
M v[64:67],a[148:151],v[200:203],v[64:67]
buffer_store_dwordx4 v[136:139], v1, s[52:55], s97 offen offset:3072 sc1
M v[96:99],a[148:151],v[216:219],v[96:99]
T a[168:169],v4,4096
M v[96:99],a[152:155],v[220:223],v[96:99]
M v[128:131],a[152:155],v[236:239],v[128:131]
T a[170:171],v4,6144
M v[128:131],a[148:151],v[232:235],v[128:131]
buffer_store_dwordx4 v[168:171], v1, s[52:55], s80 offen sc1
M v[160:163],a[148:151],v[248:251],v[160:163]
M v[160:163],a[152:155],v[252:255],v[160:163]
s_waitcnt lgkmcnt(6)
M v[36:39],a[156:159],v[184:187],v[36:39]
s_waitcnt lgkmcnt(4)
T a[172:173],v5,0
M v[36:39],a[160:163],v[188:191],v[36:39]
M v[68:71],a[160:163],v[204:207],v[68:71]
s_waitcnt vmcnt(6)
T a[174:175],v5,2048
v_cmp_ne_u32 vcc, s89, v10
M v[68:71],a[156:159],v[200:203],v[68:71]
s_cmp_eq_u64 vcc, 0
s_cbranch_scc0 L_pl1005
L_pd1005:
M v[100:103],a[156:159],v[216:219],v[100:103]
T a[148:149],v5,4096
M v[100:103],a[160:163],v[220:223],v[100:103]
s_add_u32 s96, s27, 4
M v[132:135],a[160:163],v[236:239],v[132:135]
s_and_b32 s96, s96, 7
T a[150:151],v5,6144
M v[132:135],a[156:159],v[232:235],v[132:135]
s_mul_i32 s96, s96, 20480
M v[164:167],a[156:159],v[248:251],v[164:167]
s_add_u32 s96, s96, s68
buffer_load_dwordx4 a[120:123], v1, s[52:55], s96 offen sc1
M v[164:167],a[160:163],v[252:255],v[164:167]
s_waitcnt lgkmcnt(6)
M v[32:35],a[164:167],a[176:179],v[32:35]
s_waitcnt lgkmcnt(4)
T a[152:153],v4,8192
M v[32:35],a[168:171],a[180:183],v[32:35]
M v[64:67],a[168:171],a[196:199],v[64:67]
T a[154:155],v4,10240
M v[64:67],a[164:167],a[192:195],v[64:67]
buffer_load_dwordx4 a[124:127], v1, s[52:55], s96 offen offset:1024 sc1
M v[96:99],a[164:167],a[208:211],v[96:99]
T a[156:157],v4,12288
M v[96:99],a[168:171],a[212:215],v[96:99]
M v[128:131],a[168:171],a[228:231],v[128:131]
T a[158:159],v4,14336
M v[128:131],a[164:167],a[224:227],v[128:131]
M v[160:163],a[164:167],a[240:243],v[160:163]
buffer_load_dwordx4 a[128:131], v1, s[52:55], s96 offen offset:2048 sc1
M v[160:163],a[168:171],a[244:247],v[160:163]
s_waitcnt lgkmcnt(6)
M v[36:39],a[172:175],a[176:179],v[36:39]
s_waitcnt lgkmcnt(4)
T a[160:161],v5,8192
M v[36:39],a[148:151],a[180:183],v[36:39]
M v[68:71],a[148:151],a[196:199],v[68:71]
T a[162:163],v5,10240
M v[68:71],a[172:175],a[192:195],v[68:71]
buffer_load_dwordx4 a[132:135], v1, s[52:55], s96 offen offset:3072 sc1
M v[100:103],a[172:175],a[208:211],v[100:103]
T a[164:165],v5,12288
M v[100:103],a[148:151],a[212:215],v[100:103]
M v[132:135],a[148:151],a[228:231],v[132:135]
T a[166:167],v5,14336
M v[132:135],a[172:175],a[224:227],v[132:135]
s_mov_b32 s64, s65
M v[164:167],a[172:175],a[240:243],v[164:167]
s_cmp_eq_u32 s65, 131072
s_cselect_b32 s66, s90, s76
s_add_u32 s96, s96, 4096
buffer_load_dwordx4 a[136:139], v1, s[52:55], s96 offen sc1
M v[164:167],a[148:151],a[244:247],v[164:167]
s_waitcnt lgkmcnt(6)
s_add_u32 s65, s65, s66
M v[32:35],a[152:155],a[184:187],v[32:35]
s_waitcnt lgkmcnt(4)
v_add_u32 v2, s66, v2
v_add_u32 v3, s66, v3
M v[32:35],a[156:159],a[188:191],v[32:35]
v_add_u32 v4, s66, v4
v_add_u32 v5, s66, v5
M v[64:67],a[156:159],a[204:207],v[64:67]
M v[64:67],a[152:155],a[200:203],v[64:67]
M v[96:99],a[152:155],a[216:219],v[96:99]
M v[96:99],a[156:159],a[220:223],v[96:99]
s_waitcnt lgkmcnt(0)
M v[128:131],a[156:159],a[236:239],v[128:131]
s_barrier
M v[128:131],a[152:155],a[232:235],v[128:131]
T a[168:169],v2,0
M v[160:163],a[152:155],a[248:251],v[160:163]
T a[170:171],v2,2048
M v[160:163],a[156:159],a[252:255],v[160:163]
T a[172:173],v2,4096
M v[36:39],a[160:163],a[184:187],v[36:39]
T a[174:175],v2,6144
M v[36:39],a[164:167],a[188:191],v[36:39]
M v[68:71],a[164:167],a[204:207],v[68:71]
T a[148:149],v3,0
M v[68:71],a[160:163],a[200:203],v[68:71]
M v[100:103],a[160:163],a[216:219],v[100:103]
T a[150:151],v3,2048
M v[100:103],a[164:167],a[220:223],v[100:103]
M v[132:135],a[164:167],a[236:239],v[132:135]
T a[152:153],v3,4096
M v[132:135],a[160:163],a[232:235],v[132:135]
M v[164:167],a[160:163],a[248:251],v[164:167]
T a[154:155],v3,6144
M v[164:167],a[164:167],a[252:255],v[164:167]
s_waitcnt lgkmcnt(6)
M v[32:35],a[168:171],v[16:19],v[32:35]
s_waitcnt lgkmcnt(4)
T a[156:157],v2,8192
M v[32:35],a[172:175],v[20:23],v[32:35]
M v[64:67],a[168:171],v[48:51],v[64:67]
T a[158:159],v2,10240
M v[64:67],a[172:175],v[52:55],v[64:67]
M v[96:99],a[168:171],v[80:83],v[96:99]
T a[160:161],v2,12288
M v[96:99],a[172:175],v[84:87],v[96:99]
M v[128:131],a[168:171],v[112:115],v[128:131]
T a[162:163],v2,14336
M v[128:131],a[172:175],v[116:119],v[128:131]
M v[160:163],a[168:171],v[144:147],v[160:163]
M v[160:163],a[172:175],v[148:151],v[160:163]
s_waitcnt lgkmcnt(6)
M v[36:39],a[148:151],v[16:19],v[36:39]
s_waitcnt lgkmcnt(4)
T a[164:165],v3,8192
M v[36:39],a[152:155],v[20:23],v[36:39]
M v[68:71],a[148:151],v[48:51],v[68:71]
T a[166:167],v3,10240
M v[68:71],a[152:155],v[52:55],v[68:71]
M v[100:103],a[148:151],v[80:83],v[100:103]
T a[168:169],v3,12288
M v[100:103],a[152:155],v[84:87],v[100:103]
M v[132:135],a[148:151],v[112:115],v[132:135]
T a[170:171],v3,14336
M v[132:135],a[152:155],v[116:119],v[132:135]
M v[164:167],a[148:151],v[144:147],v[164:167]
M v[164:167],a[152:155],v[148:151],v[164:167]
s_waitcnt lgkmcnt(6)
M v[32:35],a[156:159],v[24:27],v[32:35]
s_waitcnt lgkmcnt(4)
T a[172:173],v4,0
M v[32:35],a[160:163],v[28:31],v[32:35]
M v[64:67],a[160:163],v[60:63],v[64:67]
T a[174:175],v4,2048
M v[64:67],a[156:159],v[56:59],v[64:67]
M v[96:99],a[156:159],v[88:91],v[96:99]
T a[148:149],v4,4096
M v[96:99],a[160:163],v[92:95],v[96:99]
M v[128:131],a[160:163],v[124:127],v[128:131]
T a[150:151],v4,6144
M v[128:131],a[156:159],v[120:123],v[128:131]
M v[160:163],a[156:159],v[152:155],v[160:163]
M v[160:163],a[160:163],v[156:159],v[160:163]
s_waitcnt lgkmcnt(6)
M v[36:39],a[164:167],v[24:27],v[36:39]
s_waitcnt lgkmcnt(4)
T a[152:153],v5,0
M v[36:39],a[168:171],v[28:31],v[36:39]
M v[68:71],a[168:171],v[60:63],v[68:71]
T a[154:155],v5,2048
M v[68:71],a[164:167],v[56:59],v[68:71]
M v[100:103],a[164:167],v[88:91],v[100:103]
T a[156:157],v5,4096
M v[100:103],a[168:171],v[92:95],v[100:103]
M v[132:135],a[168:171],v[124:127],v[132:135]
T a[158:159],v5,6144
M v[132:135],a[164:167],v[120:123],v[132:135]
M v[164:167],a[164:167],v[152:155],v[164:167]
s_lshl_b32 s67, s30, 3
M v[164:167],a[168:171],v[156:159],v[164:167]
s_waitcnt lgkmcnt(6)
s_add_u32 s67, s67, s27
M v[32:35],a[172:175],a[0:3],v[32:35]
s_waitcnt lgkmcnt(4)
s_lshl_b32 s67, s67, 7
T a[160:161],v4,8192
M v[32:35],a[148:151],a[4:7],v[32:35]
s_add_u32 s69, s27, 7
M v[64:67],a[148:151],a[20:23],v[64:67]
T a[162:163],v4,10240
v_lshrrev_b32 v8, 4, v1
M v[64:67],a[172:175],a[16:19],v[64:67]
v_min_u32 v8, 4, v8
v_max_u32 v8, 4, v8
M v[96:99],a[172:175],a[32:35],v[96:99]
T a[164:165],v4,12288
v_sub_u32 v8, s69, v8
M v[96:99],a[148:151],a[36:39],v[96:99]
v_and_b32 v8, 7, v8
v_lshlrev_b32 v8, 2, v8
M v[128:131],a[148:151],a[52:55],v[128:131]
)"
R"(T a[166:167],v4,14336
buffer_load_dword v10, v8, s[60:63], s67 offen sc1
M v[128:131],a[172:175],a[48:51],v[128:131]
M v[160:163],a[172:175],a[64:67],v[160:163]
M v[160:163],a[148:151],a[68:71],v[160:163]
s_waitcnt lgkmcnt(6)
M v[36:39],a[152:155],a[0:3],v[36:39]
s_waitcnt lgkmcnt(4)
T a[168:169],v5,8192
M v[36:39],a[156:159],a[4:7],v[36:39]
M v[68:71],a[156:159],a[20:23],v[68:71]
T a[170:171],v5,10240
M v[68:71],a[152:155],a[16:19],v[68:71]
M v[100:103],a[152:155],a[32:35],v[100:103]
T a[172:173],v5,12288
M v[100:103],a[156:159],a[36:39],v[100:103]
M v[132:135],a[156:159],a[52:55],v[132:135]
T a[174:175],v5,14336
M v[132:135],a[152:155],a[48:51],v[132:135]
s_mov_b32 s64, s65
M v[164:167],a[152:155],a[64:67],v[164:167]
s_cmp_eq_u32 s65, 131072
s_cselect_b32 s66, s90, s76
M v[164:167],a[156:159],a[68:71],v[164:167]
s_add_u32 s65, s65, s66
s_waitcnt vmcnt(5)
M a[80:83],a[140:143],a[120:123],a[80:83]
M a[84:87],a[144:147],a[120:123],a[84:87]
s_waitcnt vmcnt(4)
M a[88:91],a[140:143],a[124:127],a[88:91]
M a[92:95],a[144:147],a[124:127],a[92:95]
s_waitcnt vmcnt(3)
M a[96:99],a[140:143],a[128:131],a[96:99]
M a[100:103],a[144:147],a[128:131],a[100:103]
s_waitcnt vmcnt(2)
M a[104:107],a[140:143],a[132:135],a[104:107]
M a[108:111],a[144:147],a[132:135],a[108:111]
s_waitcnt vmcnt(1)
M a[112:115],a[140:143],a[136:139],a[112:115]
M a[116:119],a[144:147],a[136:139],a[116:119]
s_waitcnt lgkmcnt(6)
M v[32:35],a[160:163],a[8:11],v[32:35]
s_waitcnt lgkmcnt(4)
v_add_u32 v2, s66, v2
v_add_u32 v3, s66, v3
M v[32:35],a[164:167],a[12:15],v[32:35]
s_add_u32 s31, s27, 6
v_add_u32 v4, s66, v4
v_add_u32 v5, s66, v5
M v[64:67],a[164:167],a[28:31],v[64:67]
s_and_b32 s31, s31, 7
v_mov_b32 v15, s87
M v[64:67],a[160:163],a[24:27],v[64:67]
s_add_u32 s31, s31, s77
M v[96:99],a[160:163],a[40:43],v[96:99]
s_lshl_b32 s31, s31, 7
M v[96:99],a[164:167],a[44:47],v[96:99]
s_waitcnt lgkmcnt(0)
M v[128:131],a[164:167],a[60:63],v[128:131]
s_barrier
M v[128:131],a[160:163],a[56:59],v[128:131]
T a[148:149],v2,0
buffer_store_dword v15, v6, s[60:63], s31 offen sc1
M v[160:163],a[160:163],a[72:75],v[160:163]
T a[150:151],v2,2048
M v[160:163],a[164:167],a[76:79],v[160:163]
T a[152:153],v2,4096
M v[36:39],a[168:171],a[8:11],v[36:39]
T a[154:155],v2,6144
M v[36:39],a[172:175],a[12:15],v[36:39]
M v[68:71],a[172:175],a[28:31],v[68:71]
T a[156:157],v3,0
M v[68:71],a[168:171],a[24:27],v[68:71]
M v[100:103],a[168:171],a[40:43],v[100:103]
T a[158:159],v3,2048
M v[100:103],a[172:175],a[44:47],v[100:103]
M v[132:135],a[172:175],a[60:63],v[132:135]
T a[160:161],v3,4096
M v[132:135],a[168:171],a[56:59],v[132:135]
M v[164:167],a[168:171],a[72:75],v[164:167]
T a[162:163],v3,6144
M v[164:167],a[172:175],a[76:79],v[164:167]
s_waitcnt lgkmcnt(6)
M a[80:83],a[148:151],v[176:179],a[80:83]
s_waitcnt lgkmcnt(4)
s_add_u32 s97, s27, 7
T a[164:165],v2,8192
M a[80:83],a[152:155],v[180:183],a[80:83]
s_and_b32 s97, s97, 7
M a[88:91],a[148:151],v[192:195],a[88:91]
s_mul_i32 s97, s97, 163840
T a[166:167],v2,10240
M a[88:91],a[152:155],v[196:199],a[88:91]
s_add_u32 s97, s97, s94
C v32,v32,v33
C v33,v34,v35
M a[96:99],a[148:151],v[208:211],a[96:99]
s_add_u32 s80, s97, 4096
T a[168:169],v2,12288
C v34,v36,v37
M a[96:99],a[152:155],v[212:215],a[96:99]
C v35,v38,v39
C v64,v64,v65
M a[104:107],a[148:151],v[224:227],a[104:107]
T a[170:171],v2,14336
C v65,v66,v67
M a[104:107],a[152:155],v[228:231],a[104:107]
buffer_store_dwordx4 v[32:35], v1, s[52:55], s97 offen sc1
M a[112:115],a[148:151],v[240:243],a[112:115]
C v66,v68,v69
C v67,v70,v71
M a[112:115],a[152:155],v[244:247],a[112:115]
s_waitcnt lgkmcnt(6)
C v96,v96,v97
C v97,v98,v99
M a[84:87],a[156:159],v[176:179],a[84:87]
s_waitcnt lgkmcnt(4)
T a[172:173],v3,8192
C v98,v100,v101
M a[84:87],a[160:163],v[180:183],a[84:87]
buffer_store_dwordx4 v[64:67], v1, s[52:55], s97 offen offset:1024 sc1
M a[92:95],a[156:159],v[192:195],a[92:95]
T a[174:175],v3,10240
C v99,v102,v103
M a[92:95],a[160:163],v[196:199],a[92:95]
C v128,v128,v129
C v129,v130,v131
M a[100:103],a[156:159],v[208:211],a[100:103]
T a[148:149],v3,12288
C v130,v132,v133
M a[100:103],a[160:163],v[212:215],a[100:103]
buffer_store_dwordx4 v[96:99], v1, s[52:55], s97 offen offset:2048 sc1
M a[108:111],a[156:159],v[224:227],a[108:111]
T a[150:151],v3,14336
C v131,v134,v135
M a[108:111],a[160:163],v[228:231],a[108:111]
C v160,v160,v161
C v161,v162,v163
M a[116:119],a[156:159],v[240:243],a[116:119]
C v162,v164,v165
C v163,v166,v167
M a[116:119],a[160:163],v[244:247],a[116:119]
s_waitcnt lgkmcnt(6)
buffer_store_dwordx4 v[128:131], v1, s[52:55], s97 offen offset:3072 sc1
M a[80:83],a[164:167],v[184:187],a[80:83]
s_waitcnt lgkmcnt(4)
T a[152:153],v4,0
M a[80:83],a[168:171],v[188:191],a[80:83]
M a[88:91],a[168:171],v[204:207],a[88:91]
T a[154:155],v4,2048
M a[88:91],a[164:167],v[200:203],a[88:91]
buffer_store_dwordx4 v[160:163], v1, s[52:55], s80 offen sc1
M a[96:99],a[164:167],v[216:219],a[96:99]
T a[156:157],v4,4096
M a[96:99],a[168:171],v[220:223],a[96:99]
M a[104:107],a[168:171],v[236:239],a[104:107]
T a[158:159],v4,6144
M a[104:107],a[164:167],v[232:235],a[104:107]
M a[112:115],a[164:167],v[248:251],a[112:115]
M a[112:115],a[168:171],v[252:255],a[112:115]
s_waitcnt lgkmcnt(6)
M a[84:87],a[172:175],v[184:187],a[84:87]
s_waitcnt lgkmcnt(4)
T a[160:161],v5,0
M a[84:87],a[148:151],v[188:191],a[84:87]
M a[92:95],a[148:151],v[204:207],a[92:95]
s_waitcnt vmcnt(6)
T a[162:163],v5,2048
v_cmp_ne_u32 vcc, s89, v10
M a[92:95],a[172:175],v[200:203],a[92:95]
s_cmp_eq_u64 vcc, 0
s_cbranch_scc0 L_pl1006
L_pd1006:
M a[100:103],a[172:175],v[216:219],a[100:103]
T a[164:165],v5,4096
M a[100:103],a[148:151],v[220:223],a[100:103]
s_add_u32 s96, s27, 3
M a[108:111],a[148:151],v[236:239],a[108:111]
s_and_b32 s96, s96, 7
T a[166:167],v5,6144
M a[108:111],a[172:175],v[232:235],a[108:111]
s_mul_i32 s96, s96, 20480
M a[116:119],a[172:175],v[248:251],a[116:119]
s_add_u32 s96, s96, s68
buffer_load_dwordx4 a[120:123], v1, s[52:55], s96 offen sc1
M a[116:119],a[148:151],v[252:255],a[116:119]
s_waitcnt lgkmcnt(6)
M a[80:83],a[152:155],a[176:179],a[80:83]
s_waitcnt lgkmcnt(4)
T a[168:169],v4,8192
M a[80:83],a[156:159],a[180:183],a[80:83]
M a[88:91],a[156:159],a[196:199],a[88:91]
T a[170:171],v4,10240
buffer_load_dwordx4 a[124:127], v1, s[52:55], s96 offen offset:1024 sc1
M a[88:91],a[152:155],a[192:195],a[88:91]
M a[96:99],a[152:155],a[208:211],a[96:99]
T a[172:173],v4,12288
M a[96:99],a[156:159],a[212:215],a[96:99]
M a[104:107],a[156:159],a[228:231],a[104:107]
T a[174:175],v4,14336
buffer_load_dwordx4 a[128:131], v1, s[52:55], s96 offen offset:2048 sc1
M a[104:107],a[152:155],a[224:227],a[104:107]
M a[112:115],a[152:155],a[240:243],a[112:115]
M a[112:115],a[156:159],a[244:247],a[112:115]
s_waitcnt lgkmcnt(6)
M a[84:87],a[160:163],a[176:179],a[84:87]
s_waitcnt lgkmcnt(4)
s_lshl_b32 s67, s30, 3
T a[148:149],v5,8192
buffer_load_dwordx4 a[132:135], v1, s[52:55], s96 offen offset:3072 sc1
M a[84:87],a[164:167],a[180:183],a[84:87]
s_add_u32 s67, s67, s27
M a[92:95],a[164:167],a[196:199],a[92:95]
s_lshl_b32 s67, s67, 7
T a[150:151],v5,10240
M a[92:95],a[160:163],a[192:195],a[92:95]
s_add_u32 s69, s27, 7
M a[100:103],a[160:163],a[208:211],a[100:103]
s_add_u32 s96, s96, 4096
T a[152:153],v5,12288
buffer_load_dwordx4 a[136:139], v1, s[52:55], s96 offen sc1
M a[100:103],a[164:167],a[212:215],a[100:103]
v_lshrrev_b32 v8, 4, v1
v_min_u32 v8, 5, v8
M a[108:111],a[164:167],a[228:231],a[108:111]
T a[154:155],v5,14336
v_max_u32 v8, 5, v8
M a[108:111],a[160:163],a[224:227],a[108:111]
s_mov_b32 s64, s65
v_sub_u32 v8, s69, v8
v_and_b32 v8, 7, v8
M a[116:119],a[160:163],a[240:243],a[116:119]
s_cmp_eq_u32 s65, 131072
s_cselect_b32 s66, s90, s76
v_lshlrev_b32 v8, 2, v8
M a[116:119],a[164:167],a[244:247],a[116:119]
s_waitcnt lgkmcnt(6)
s_add_u32 s65, s65, s66
buffer_load_dword v10, v8, s[60:63], s67 offen sc1
M a[80:83],a[168:171],a[184:187],a[80:83]
s_waitcnt lgkmcnt(4)
v_add_u32 v2, s66, v2
v_add_u32 v3, s66, v3
M a[80:83],a[172:175],a[188:191],a[80:83]
v_add_u32 v4, s66, v4
v_add_u32 v5, s66, v5
M a[88:91],a[172:175],a[204:207],a[88:91]
M a[88:91],a[168:171],a[200:203],a[88:91]
M a[96:99],a[168:171],a[216:219],a[96:99]
M a[96:99],a[172:175],a[220:223],a[96:99]
s_waitcnt lgkmcnt(0)
M a[104:107],a[172:175],a[236:239],a[104:107]
s_barrier
M a[104:107],a[168:171],a[232:235],a[104:107]
T a[156:157],v2,0
M a[112:115],a[168:171],a[248:251],a[112:115]
T a[158:159],v2,2048
M a[112:115],a[172:175],a[252:255],a[112:115]
T a[160:161],v2,4096
M a[84:87],a[148:151],a[184:187],a[84:87]
T a[162:163],v2,6144
M a[84:87],a[152:155],a[188:191],a[84:87]
M a[92:95],a[152:155],a[204:207],a[92:95]
T a[164:165],v3,0
M a[92:95],a[148:151],a[200:203],a[92:95]
M a[100:103],a[148:151],a[216:219],a[100:103]
T a[166:167],v3,2048
M a[100:103],a[152:155],a[220:223],a[100:103]
M a[108:111],a[152:155],a[236:239],a[108:111]
T a[168:169],v3,4096
M a[108:111],a[148:151],a[232:235],a[108:111]
M a[116:119],a[148:151],a[248:251],a[116:119]
T a[170:171],v3,6144
M a[116:119],a[152:155],a[252:255],a[116:119]
s_waitcnt lgkmcnt(6)
M a[80:83],a[156:159],v[16:19],a[80:83]
s_waitcnt lgkmcnt(4)
T a[172:173],v2,8192
M a[80:83],a[160:163],v[20:23],a[80:83]
M a[88:91],a[156:159],v[48:51],a[88:91]
T a[174:175],v2,10240
v_mov_b32 v15, s88
M a[88:91],a[160:163],v[52:55],a[88:91]
s_add_u32 s31, s27, 7
M a[96:99],a[156:159],v[80:83],a[96:99]
s_and_b32 s31, s31, 7
T a[148:149],v2,12288
M a[96:99],a[160:163],v[84:87],a[96:99]
s_add_u32 s31, s31, s77
M a[104:107],a[156:159],v[112:115],a[104:107]
s_lshl_b32 s31, s31, 7
T a[150:151],v2,14336
M a[104:107],a[160:163],v[116:119],a[104:107]
M a[112:115],a[156:159],v[144:147],a[112:115]
s_waitcnt vmcnt(6)
s_barrier
M a[112:115],a[160:163],v[148:151],a[112:115]
s_waitcnt lgkmcnt(6)
buffer_store_dword v15, v6, s[60:63], s31 offen sc1
M a[84:87],a[164:167],v[16:19],a[84:87]
s_waitcnt lgkmcnt(4)
T a[152:153],v3,8192
M a[84:87],a[168:171],v[20:23],a[84:87]
M a[92:95],a[164:167],v[48:51],a[92:95]
T a[154:155],v3,10240
M a[92:95],a[168:171],v[52:55],a[92:95]
M a[100:103],a[164:167],v[80:83],a[100:103]
T a[156:157],v3,12288
M a[100:103],a[168:171],v[84:87],a[100:103]
M a[108:111],a[164:167],v[112:115],a[108:111]
T a[158:159],v3,14336
M a[108:111],a[168:171],v[116:119],a[108:111]
M a[116:119],a[164:167],v[144:147],a[116:119]
M a[116:119],a[168:171],v[148:151],a[116:119]
s_waitcnt lgkmcnt(6)
M a[80:83],a[172:175],v[24:27],a[80:83]
s_waitcnt lgkmcnt(4)
T a[160:161],v4,0
M a[80:83],a[148:151],v[28:31],a[80:83]
M a[88:91],a[148:151],v[60:63],a[88:91]
T a[162:163],v4,2048
M a[88:91],a[172:175],v[56:59],a[88:91]
M a[96:99],a[172:175],v[88:91],a[96:99]
T a[164:165],v4,4096
M a[96:99],a[148:151],v[92:95],a[96:99]
M a[104:107],a[148:151],v[124:127],a[104:107]
T a[166:167],v4,6144
M a[104:107],a[172:175],v[120:123],a[104:107]
s_waitcnt vmcnt(1)
v_cmp_ne_u32 vcc, s89, v10
M a[112:115],a[172:175],v[152:155],a[112:115]
s_cmp_eq_u64 vcc, 0
s_cbranch_scc0 L_pl1007
L_pd1007:
M a[112:115],a[148:151],v[156:159],a[112:115]
s_waitcnt lgkmcnt(6)
M a[84:87],a[152:155],v[24:27],a[84:87]
s_waitcnt lgkmcnt(4)
s_add_u32 s96, s27, 2
T a[168:169],v5,0
M a[84:87],a[156:159],v[28:31],a[84:87]
s_and_b32 s96, s96, 7
M a[92:95],a[156:159],v[60:63],a[92:95]
s_mul_i32 s96, s96, 20480
T a[170:171],v5,2048
M a[92:95],a[152:155],v[56:59],a[92:95]
s_add_u32 s96, s96, s68
buffer_load_dwordx4 v[176:179], v1, s[52:55], s96 offen sc1
M a[100:103],a[152:155],v[88:91],a[100:103]
T a[172:173],v5,4096
M a[100:103],a[156:159],v[92:95],a[100:103]
M a[108:111],a[156:159],v[124:127],a[108:111]
s_lshl_b32 s67, s30, 3
T a[174:175],v5,6144
M a[108:111],a[152:155],v[120:123],a[108:111]
s_add_u32 s67, s67, s27
buffer_load_dwordx4 v[192:195], v1, s[52:55], s96 offen offset:1024 sc1
M a[116:119],a[152:155],v[152:155],a[116:119]
s_lshl_b32 s67, s67, 7
M a[116:119],a[156:159],v[156:159],a[116:119]
s_waitcnt lgkmcnt(6)
s_add_u32 s69, s27, 7
M a[80:83],a[160:163],a[0:3],a[80:83]
s_waitcnt lgkmcnt(4)
T a[148:149],v4,8192
v_lshrrev_b32 v8, 4, v1
M a[80:83],a[164:167],a[4:7],a[80:83]
buffer_load_dwordx4 v[208:211], v1, s[52:55], s96 offen offset:2048 sc1
v_min_u32 v8, 6, v8
M a[88:91],a[164:167],a[20:23],a[88:91]
T a[150:151],v4,10240
v_max_u32 v8, 6, v8
M a[88:91],a[160:163],a[16:19],a[88:91]
v_sub_u32 v8, s69, v8
v_and_b32 v8, 7, v8
M a[96:99],a[160:163],a[32:35],a[96:99]
T a[152:153],v4,12288
v_lshlrev_b32 v8, 2, v8
M a[96:99],a[164:167],a[36:39],a[96:99]
buffer_load_dwordx4 v[224:227], v1, s[52:55], s96 offen offset:3072 sc1
buffer_load_dword v10, v8, s[60:63], s67 offen sc1
M a[104:107],a[164:167],a[52:55],a[104:107]
T a[154:155],v4,14336
M a[104:107],a[160:163],a[48:51],a[104:107]
M a[112:115],a[160:163],a[64:67],a[112:115]
M a[112:115],a[164:167],a[68:71],a[112:115]
s_waitcnt lgkmcnt(6)
s_add_u32 s96, s96, 4096
buffer_load_dwordx4 v[240:243], v1, s[52:55], s96 offen sc1
M a[84:87],a[168:171],a[0:3],a[84:87]
s_waitcnt lgkmcnt(4)
T a[156:157],v5,8192
M a[84:87],a[172:175],a[4:7],a[84:87]
M a[92:95],a[172:175],a[20:23],a[92:95]
T a[158:159],v5,10240
M a[92:95],a[168:171],a[16:19],a[92:95]
M a[100:103],a[168:171],a[32:35],a[100:103]
T a[160:161],v5,12288
M a[100:103],a[172:175],a[36:39],a[100:103]
M a[108:111],a[172:175],a[52:55],a[108:111]
T a[162:163],v5,14336
M a[108:111],a[168:171],a[48:51],a[108:111]
M a[116:119],a[168:171],a[64:67],a[116:119]
M a[116:119],a[172:175],a[68:71],a[116:119]
M a[80:83],a[140:143],a[120:123],a[80:83]
M a[84:87],a[144:147],a[120:123],a[84:87]
M a[88:91],a[140:143],a[124:127],a[88:91]
M a[92:95],a[144:147],a[124:127],a[92:95]
M a[96:99],a[140:143],a[128:131],a[96:99]
M a[100:103],a[144:147],a[128:131],a[100:103]
M a[104:107],a[140:143],a[132:135],a[104:107]
M a[108:111],a[144:147],a[132:135],a[108:111]
M a[112:115],a[140:143],a[136:139],a[112:115]
M a[116:119],a[144:147],a[136:139],a[116:119]
s_waitcnt lgkmcnt(6)
M a[80:83],a[148:151],a[8:11],a[80:83]
s_waitcnt lgkmcnt(4)
M a[80:83],a[152:155],a[12:15],a[80:83]
M a[88:91],a[152:155],a[28:31],a[88:91]
M a[88:91],a[148:151],a[24:27],a[88:91]
s_waitcnt vmcnt(1)
v_cmp_ne_u32 vcc, s89, v10
M a[96:99],a[148:151],a[40:43],a[96:99]
s_cmp_eq_u64 vcc, 0
s_cbranch_scc0 L_pl1008
L_pd1008:
M a[96:99],a[152:155],a[44:47],a[96:99]
M a[104:107],a[152:155],a[60:63],a[104:107]
s_add_u32 s96, s27, 1
M a[104:107],a[148:151],a[56:59],a[104:107]
s_and_b32 s96, s96, 7
M a[112:115],a[148:151],a[72:75],a[112:115]
s_mul_i32 s96, s96, 20480
M a[112:115],a[152:155],a[76:79],a[112:115]
s_waitcnt lgkmcnt(2)
s_add_u32 s96, s96, s68
buffer_load_dwordx4 v[180:183], v1, s[52:55], s96 offen sc1
M a[84:87],a[156:159],a[8:11],a[84:87]
s_waitcnt lgkmcnt(0)
M a[84:87],a[160:163],a[12:15],a[84:87]
buffer_load_dwordx4 v[196:199], v1, s[52:55], s96 offen offset:1024 sc1
M a[92:95],a[160:163],a[28:31],a[92:95]
M a[92:95],a[156:159],a[24:27],a[92:95]
buffer_load_dwordx4 v[212:215], v1, s[52:55], s96 offen offset:2048 sc1
M a[80:83],a[140:143],v[176:179],a[80:83]
M a[84:87],a[144:147],v[176:179],a[84:87]
M a[88:91],a[140:143],v[192:195],a[88:91]
M a[92:95],a[144:147],v[192:195],a[92:95]
M a[96:99],a[140:143],v[208:211],a[96:99]
M a[100:103],a[144:147],v[208:211],a[100:103]
M a[104:107],a[140:143],v[224:227],a[104:107]
M a[108:111],a[144:147],v[224:227],a[108:111]
s_waitcnt vmcnt(3)
M a[112:115],a[140:143],v[240:243],a[112:115]
M a[116:119],a[144:147],v[240:243],a[116:119]
M a[100:103],a[156:159],a[40:43],a[100:103]
M a[100:103],a[160:163],a[44:47],a[100:103]
buffer_load_dwordx4 v[228:231], v1, s[52:55], s96 offen offset:3072 sc1
M a[108:111],a[160:163],a[60:63],a[108:111]
M a[108:111],a[156:159],a[56:59],a[108:111]
s_add_u32 s96, s96, 4096
buffer_load_dwordx4 v[244:247], v1, s[52:55], s96 offen sc1
M a[116:119],a[156:159],a[72:75],a[116:119]
M a[116:119],a[160:163],a[76:79],a[116:119]
v_lshrrev_b32 v9, 8, v1
v_lshlrev_b32 v14, 4, v9
v_bfe_u32 v9, v1, 4, 4
v_lshl_add_u32 v14, v9, 11, v14
s_mul_i32 s31, s26, 163840
s_waitcnt vmcnt(4)
M a[80:83],a[140:143],v[180:183],a[80:83]
M a[84:87],a[144:147],v[180:183],a[84:87]
s_waitcnt vmcnt(3)
M a[88:91],a[140:143],v[196:199],a[88:91]
M a[92:95],a[144:147],v[196:199],a[92:95]
s_waitcnt vmcnt(2)
M a[96:99],a[140:143],v[212:215],a[96:99]
M a[100:103],a[144:147],v[212:215],a[100:103]
s_nop 2
R v32,a80
R v33,a81
R v34,a82
R v35,a83
R v36,a84
R v37,a85
R v38,a86
R v39,a87
C v32,v32,v33
C v33,v34,v35
C v34,v36,v37
C v35,v38,v39
s_nop 0
v_permlane32_swap_b32 v32, v34
v_permlane32_swap_b32 v33, v35
s_nop 0
v_permlane16_swap_b32 v32, v34
v_permlane16_swap_b32 v33, v35
buffer_store_dwordx4 v[32:35], v14, s[56:59], s31 offen sc1
s_add_u32 s31, s31, 32768
s_waitcnt vmcnt(2)
)"
R"(M a[104:107],a[140:143],v[228:231],a[104:107]
M a[108:111],a[144:147],v[228:231],a[108:111]
R v64,a88
R v65,a89
R v66,a90
R v67,a91
R v68,a92
R v69,a93
R v70,a94
R v71,a95
C v64,v64,v65
C v65,v66,v67
C v66,v68,v69
C v67,v70,v71
s_nop 0
v_permlane32_swap_b32 v64, v66
v_permlane32_swap_b32 v65, v67
s_nop 0
v_permlane16_swap_b32 v64, v66
v_permlane16_swap_b32 v65, v67
buffer_store_dwordx4 v[64:67], v14, s[56:59], s31 offen sc1
s_add_u32 s31, s31, 32768
s_waitcnt vmcnt(2)
M a[112:115],a[140:143],v[244:247],a[112:115]
M a[116:119],a[144:147],v[244:247],a[116:119]
R v96,a96
R v97,a97
R v98,a98
R v99,a99
R v100,a100
R v101,a101
R v102,a102
R v103,a103
C v96,v96,v97
C v97,v98,v99
C v98,v100,v101
C v99,v102,v103
s_nop 0
v_permlane32_swap_b32 v96, v98
v_permlane32_swap_b32 v97, v99
s_nop 0
v_permlane16_swap_b32 v96, v98
v_permlane16_swap_b32 v97, v99
buffer_store_dwordx4 v[96:99], v14, s[56:59], s31 offen sc1
s_add_u32 s31, s31, 32768
R v128,a104
R v129,a105
R v130,a106
R v131,a107
R v132,a108
R v133,a109
R v134,a110
R v135,a111
C v128,v128,v129
C v129,v130,v131
C v130,v132,v133
C v131,v134,v135
s_nop 0
v_permlane32_swap_b32 v128, v130
v_permlane32_swap_b32 v129, v131
s_nop 0
v_permlane16_swap_b32 v128, v130
v_permlane16_swap_b32 v129, v131
buffer_store_dwordx4 v[128:131], v14, s[56:59], s31 offen sc1
s_add_u32 s31, s31, 32768
R v160,a112
R v161,a113
R v162,a114
R v163,a115
R v164,a116
R v165,a117
R v166,a118
R v167,a119
C v160,v160,v161
C v161,v162,v163
C v162,v164,v165
C v163,v166,v167
s_nop 0
v_permlane32_swap_b32 v160, v162
v_permlane32_swap_b32 v161, v163
s_nop 0
v_permlane16_swap_b32 v160, v162
v_permlane16_swap_b32 v161, v163
buffer_store_dwordx4 v[160:163], v14, s[56:59], s31 offen sc1
s_add_u32 s31, s31, 32768
s_cmp_lg_u32 s24, 0
s_cbranch_scc1 L_ep1009
s_lshl_b32 s31, s30, 7
s_lshl_b32 s23, s27, 2
s_add_u32 s31, s31, s23
s_add_u32 s31, s31, 32768
s_mov_b64 exec, 1
v_mov_b32 v15, s89
buffer_store_dword v15, off, s[60:63], s31 sc1
s_mov_b64 exec, -1
L_ep1009:
s_endpgm
L_pl1002:
s_mov_b32 s69, 262144
L_pm1002:
s_sleep 1
buffer_load_dword v10, v8, s[60:63], s67 offen sc1
s_waitcnt vmcnt(0)
v_cmp_ne_u32 vcc, s89, v10
s_sub_u32 s69, s69, 1
s_cmp_eq_u32 s69, 0
s_cbranch_scc1 L_pd1002
s_cmp_eq_u64 vcc, 0
s_cbranch_scc0 L_pm1002
s_branch L_pd1002
L_pl1003:
s_mov_b32 s69, 262144
L_pm1003:
s_sleep 1
buffer_load_dword v10, v8, s[60:63], s67 offen sc1
s_waitcnt vmcnt(0)
v_cmp_ne_u32 vcc, s89, v10
s_sub_u32 s69, s69, 1
s_cmp_eq_u32 s69, 0
s_cbranch_scc1 L_pd1003
s_cmp_eq_u64 vcc, 0
s_cbranch_scc0 L_pm1003
s_branch L_pd1003
L_pl1004:
s_mov_b32 s69, 262144
L_pm1004:
s_sleep 1
buffer_load_dword v10, v8, s[60:63], s67 offen sc1
s_waitcnt vmcnt(0)
v_cmp_ne_u32 vcc, s89, v10
s_sub_u32 s69, s69, 1
s_cmp_eq_u32 s69, 0
s_cbranch_scc1 L_pd1004
s_cmp_eq_u64 vcc, 0
s_cbranch_scc0 L_pm1004
s_branch L_pd1004
L_pl1005:
s_mov_b32 s69, 262144
L_pm1005:
s_sleep 1
buffer_load_dword v10, v8, s[60:63], s67 offen sc1
s_waitcnt vmcnt(0)
v_cmp_ne_u32 vcc, s89, v10
s_sub_u32 s69, s69, 1
s_cmp_eq_u32 s69, 0
s_cbranch_scc1 L_pd1005
s_cmp_eq_u64 vcc, 0
s_cbranch_scc0 L_pm1005
s_branch L_pd1005
L_pl1006:
s_mov_b32 s69, 262144
L_pm1006:
s_sleep 1
buffer_load_dword v10, v8, s[60:63], s67 offen sc1
s_waitcnt vmcnt(0)
v_cmp_ne_u32 vcc, s89, v10
s_sub_u32 s69, s69, 1
s_cmp_eq_u32 s69, 0
s_cbranch_scc1 L_pd1006
s_cmp_eq_u64 vcc, 0
s_cbranch_scc0 L_pm1006
s_branch L_pd1006
L_pl1007:
s_mov_b32 s69, 262144
L_pm1007:
s_sleep 1
buffer_load_dword v10, v8, s[60:63], s67 offen sc1
s_waitcnt vmcnt(0)
v_cmp_ne_u32 vcc, s89, v10
s_sub_u32 s69, s69, 1
s_cmp_eq_u32 s69, 0
s_cbranch_scc1 L_pd1007
s_cmp_eq_u64 vcc, 0
s_cbranch_scc0 L_pm1007
s_branch L_pd1007
L_pl1008:
s_mov_b32 s69, 262144
L_pm1008:
s_sleep 1
buffer_load_dword v10, v8, s[60:63], s67 offen sc1
s_waitcnt vmcnt(0)
v_cmp_ne_u32 vcc, s89, v10
s_sub_u32 s69, s69, 1
s_cmp_eq_u32 s69, 0
s_cbranch_scc1 L_pd1008
s_cmp_eq_u64 vcc, 0
s_cbranch_scc0 L_pm1008
s_branch L_pd1008
L_slow0:
s_waitcnt vmcnt(0) lgkmcnt(0)
s_mov_b32 s18, 131072
s_mov_b32 s19, 131072
v_lshrrev_b32 v166, 4, v1
v_and_b32 v165, 15, v166
v_lshrrev_b32 v166, 4, v166
v_lshlrev_b32 v164, 8, v165
v_lshl_add_u32 v164, v166, 4, v164
v_lshlrev_b32 v165, 1, v165
v_lshl_add_u32 v165, v166, 10, v165
v_mov_b32 v32, 0xff800000
v_mov_b32 v33, 0xff800000
v_mov_b32 v34, 0xff800000
v_mov_b32 v35, 0xff800000
v_mov_b32 v64, 0xff800000
v_mov_b32 v65, 0xff800000
v_mov_b32 v66, 0xff800000
v_mov_b32 v67, 0xff800000
v_mov_b32 v96, 0xff800000
v_mov_b32 v97, 0xff800000
v_mov_b32 v98, 0xff800000
v_mov_b32 v99, 0xff800000
v_mov_b32 v128, 0xff800000
v_mov_b32 v129, 0xff800000
v_mov_b32 v130, 0xff800000
v_mov_b32 v131, 0xff800000
v_mov_b32 v160, 0xff800000
v_mov_b32 v161, 0xff800000
v_mov_b32 v162, 0xff800000
v_mov_b32 v163, 0xff800000
s_mov_b32 s31, 0
L_sl1_0:
s_add_u32 s16, s12, s97
s_addc_u32 s17, s13, 0
s_add_u32 s23, s31, 4096
buffer_load_dwordx4 a[160:163], v164, s[16:19], s31 offen offset:0
buffer_load_dwordx4 a[164:167], v164, s[16:19], s31 offen offset:64
buffer_load_dwordx4 a[168:171], v164, s[16:19], s31 offen offset:128
buffer_load_dwordx4 a[172:175], v164, s[16:19], s31 offen offset:192
buffer_load_dwordx4 a[176:179], v164, s[16:19], s23 offen offset:0
buffer_load_dwordx4 a[180:183], v164, s[16:19], s23 offen offset:64
buffer_load_dwordx4 a[184:187], v164, s[16:19], s23 offen offset:128
buffer_load_dwordx4 a[188:191], v164, s[16:19], s23 offen offset:192
s_waitcnt vmcnt(0)
s_add_u32 s16, s14, s97
s_addc_u32 s17, s15, 0
M v[36:39],a[160:163],v[16:19],0
M v[36:39],a[164:167],v[20:23],v[36:39]
M v[36:39],a[168:171],v[24:27],v[36:39]
M v[36:39],a[172:175],v[28:31],v[36:39]
M v[40:43],a[176:179],v[16:19],0
M v[40:43],a[180:183],v[20:23],v[40:43]
M v[40:43],a[184:187],v[24:27],v[40:43]
M v[40:43],a[188:191],v[28:31],v[40:43]
s_nop 7
s_nop 7
v_max3_f32 v32, v32, v36, v37
v_max3_f32 v32, v32, v38, v39
v_max3_f32 v32, v32, v40, v41
v_max3_f32 v32, v32, v42, v43
M v[68:71],a[160:163],v[48:51],0
M v[68:71],a[164:167],v[52:55],v[68:71]
M v[68:71],a[168:171],v[56:59],v[68:71]
M v[68:71],a[172:175],v[60:63],v[68:71]
M v[72:75],a[176:179],v[48:51],0
M v[72:75],a[180:183],v[52:55],v[72:75]
M v[72:75],a[184:187],v[56:59],v[72:75]
M v[72:75],a[188:191],v[60:63],v[72:75]
s_nop 7
s_nop 7
v_max3_f32 v64, v64, v68, v69
v_max3_f32 v64, v64, v70, v71
v_max3_f32 v64, v64, v72, v73
v_max3_f32 v64, v64, v74, v75
M v[36:39],a[160:163],v[80:83],0
M v[36:39],a[164:167],v[84:87],v[36:39]
M v[36:39],a[168:171],v[88:91],v[36:39]
M v[36:39],a[172:175],v[92:95],v[36:39]
M v[40:43],a[176:179],v[80:83],0
M v[40:43],a[180:183],v[84:87],v[40:43]
M v[40:43],a[184:187],v[88:91],v[40:43]
M v[40:43],a[188:191],v[92:95],v[40:43]
s_nop 7
s_nop 7
v_max3_f32 v96, v96, v36, v37
v_max3_f32 v96, v96, v38, v39
v_max3_f32 v96, v96, v40, v41
v_max3_f32 v96, v96, v42, v43
M v[68:71],a[160:163],v[112:115],0
M v[68:71],a[164:167],v[116:119],v[68:71]
M v[68:71],a[168:171],v[120:123],v[68:71]
M v[68:71],a[172:175],v[124:127],v[68:71]
M v[72:75],a[176:179],v[112:115],0
M v[72:75],a[180:183],v[116:119],v[72:75]
M v[72:75],a[184:187],v[120:123],v[72:75]
M v[72:75],a[188:191],v[124:127],v[72:75]
s_nop 7
s_nop 7
v_max3_f32 v128, v128, v68, v69
v_max3_f32 v128, v128, v70, v71
v_max3_f32 v128, v128, v72, v73
v_max3_f32 v128, v128, v74, v75
M v[100:103],a[160:163],v[144:147],0
M v[100:103],a[164:167],v[148:151],v[100:103]
M v[100:103],a[168:171],v[152:155],v[100:103]
M v[100:103],a[172:175],v[156:159],v[100:103]
M v[104:107],a[176:179],v[144:147],0
M v[104:107],a[180:183],v[148:151],v[104:107]
M v[104:107],a[184:187],v[152:155],v[104:107]
M v[104:107],a[188:191],v[156:159],v[104:107]
s_nop 7
s_nop 7
v_max3_f32 v160, v160, v100, v101
v_max3_f32 v160, v160, v102, v103
v_max3_f32 v160, v160, v104, v105
v_max3_f32 v160, v160, v106, v107
s_add_u32 s31, s31, 8192
s_cmp_lt_u32 s31, 131072
s_cbranch_scc1 L_sl1_0
v_mov_b32 v33, v32
s_nop 1
v_permlane32_swap_b32 v32, v33
v_max_f32 v32, v32, v33
v_mov_b32 v33, v32
s_nop 1
v_permlane16_swap_b32 v32, v33
v_max_f32 v32, v32, v33
v_sub_f32 v33, 0, v32
v_sub_f32 v34, 0, v32
v_sub_f32 v35, 0, v32
v_sub_f32 v32, 0, v32
v_mov_b32 v132, 0
v_mov_b32 v65, v64
s_nop 1
v_permlane32_swap_b32 v64, v65
v_max_f32 v64, v64, v65
v_mov_b32 v65, v64
s_nop 1
v_permlane16_swap_b32 v64, v65
v_max_f32 v64, v64, v65
v_sub_f32 v65, 0, v64
v_sub_f32 v66, 0, v64
v_sub_f32 v67, 0, v64
v_sub_f32 v64, 0, v64
v_mov_b32 v133, 0
v_mov_b32 v97, v96
s_nop 1
v_permlane32_swap_b32 v96, v97
v_max_f32 v96, v96, v97
v_mov_b32 v97, v96
s_nop 1
v_permlane16_swap_b32 v96, v97
v_max_f32 v96, v96, v97
v_sub_f32 v97, 0, v96
v_sub_f32 v98, 0, v96
v_sub_f32 v99, 0, v96
v_sub_f32 v96, 0, v96
v_mov_b32 v134, 0
v_mov_b32 v129, v128
s_nop 1
v_permlane32_swap_b32 v128, v129
v_max_f32 v128, v128, v129
v_mov_b32 v129, v128
s_nop 1
v_permlane16_swap_b32 v128, v129
v_max_f32 v128, v128, v129
v_sub_f32 v129, 0, v128
v_sub_f32 v130, 0, v128
v_sub_f32 v131, 0, v128
v_sub_f32 v128, 0, v128
v_mov_b32 v135, 0
v_mov_b32 v161, v160
s_nop 1
v_permlane32_swap_b32 v160, v161
v_max_f32 v160, v160, v161
v_mov_b32 v161, v160
s_nop 1
v_permlane16_swap_b32 v160, v161
v_max_f32 v160, v160, v161
v_sub_f32 v161, 0, v160
v_sub_f32 v162, 0, v160
v_sub_f32 v163, 0, v160
v_sub_f32 v160, 0, v160
v_mov_b32 v136, 0
v_accvgpr_write_b32 a0, 0
v_accvgpr_write_b32 a1, 0
v_accvgpr_write_b32 a2, 0
v_accvgpr_write_b32 a3, 0
v_accvgpr_write_b32 a4, 0
v_accvgpr_write_b32 a5, 0
v_accvgpr_write_b32 a6, 0
v_accvgpr_write_b32 a7, 0
v_accvgpr_write_b32 a8, 0
v_accvgpr_write_b32 a9, 0
v_accvgpr_write_b32 a10, 0
v_accvgpr_write_b32 a11, 0
v_accvgpr_write_b32 a12, 0
v_accvgpr_write_b32 a13, 0
v_accvgpr_write_b32 a14, 0
v_accvgpr_write_b32 a15, 0
v_accvgpr_write_b32 a16, 0
v_accvgpr_write_b32 a17, 0
v_accvgpr_write_b32 a18, 0
v_accvgpr_write_b32 a19, 0
v_accvgpr_write_b32 a20, 0
v_accvgpr_write_b32 a21, 0
v_accvgpr_write_b32 a22, 0
v_accvgpr_write_b32 a23, 0
v_accvgpr_write_b32 a24, 0
v_accvgpr_write_b32 a25, 0
v_accvgpr_write_b32 a26, 0
v_accvgpr_write_b32 a27, 0
v_accvgpr_write_b32 a28, 0
v_accvgpr_write_b32 a29, 0
v_accvgpr_write_b32 a30, 0
v_accvgpr_write_b32 a31, 0
v_accvgpr_write_b32 a32, 0
v_accvgpr_write_b32 a33, 0
v_accvgpr_write_b32 a34, 0
v_accvgpr_write_b32 a35, 0
v_accvgpr_write_b32 a36, 0
v_accvgpr_write_b32 a37, 0
v_accvgpr_write_b32 a38, 0
v_accvgpr_write_b32 a39, 0
v_accvgpr_write_b32 a40, 0
v_accvgpr_write_b32 a41, 0
v_accvgpr_write_b32 a42, 0
v_accvgpr_write_b32 a43, 0
v_accvgpr_write_b32 a44, 0
v_accvgpr_write_b32 a45, 0
v_accvgpr_write_b32 a46, 0
v_accvgpr_write_b32 a47, 0
v_accvgpr_write_b32 a48, 0
v_accvgpr_write_b32 a49, 0
v_accvgpr_write_b32 a50, 0
v_accvgpr_write_b32 a51, 0
v_accvgpr_write_b32 a52, 0
v_accvgpr_write_b32 a53, 0
v_accvgpr_write_b32 a54, 0
v_accvgpr_write_b32 a55, 0
v_accvgpr_write_b32 a56, 0
v_accvgpr_write_b32 a57, 0
v_accvgpr_write_b32 a58, 0
v_accvgpr_write_b32 a59, 0
v_accvgpr_write_b32 a60, 0
v_accvgpr_write_b32 a61, 0
v_accvgpr_write_b32 a62, 0
v_accvgpr_write_b32 a63, 0
v_accvgpr_write_b32 a64, 0
v_accvgpr_write_b32 a65, 0
v_accvgpr_write_b32 a66, 0
v_accvgpr_write_b32 a67, 0
v_accvgpr_write_b32 a68, 0
v_accvgpr_write_b32 a69, 0
v_accvgpr_write_b32 a70, 0
v_accvgpr_write_b32 a71, 0
v_accvgpr_write_b32 a72, 0
v_accvgpr_write_b32 a73, 0
v_accvgpr_write_b32 a74, 0
v_accvgpr_write_b32 a75, 0
v_accvgpr_write_b32 a76, 0
v_accvgpr_write_b32 a77, 0
v_accvgpr_write_b32 a78, 0
v_accvgpr_write_b32 a79, 0
v_accvgpr_write_b32 a80, 0
v_accvgpr_write_b32 a81, 0
v_accvgpr_write_b32 a82, 0
v_accvgpr_write_b32 a83, 0
v_accvgpr_write_b32 a84, 0
v_accvgpr_write_b32 a85, 0
v_accvgpr_write_b32 a86, 0
v_accvgpr_write_b32 a87, 0
v_accvgpr_write_b32 a88, 0
v_accvgpr_write_b32 a89, 0
v_accvgpr_write_b32 a90, 0
v_accvgpr_write_b32 a91, 0
v_accvgpr_write_b32 a92, 0
v_accvgpr_write_b32 a93, 0
v_accvgpr_write_b32 a94, 0
v_accvgpr_write_b32 a95, 0
v_accvgpr_write_b32 a96, 0
v_accvgpr_write_b32 a97, 0
v_accvgpr_write_b32 a98, 0
v_accvgpr_write_b32 a99, 0
v_accvgpr_write_b32 a100, 0
v_accvgpr_write_b32 a101, 0
v_accvgpr_write_b32 a102, 0
v_accvgpr_write_b32 a103, 0
v_accvgpr_write_b32 a104, 0
v_accvgpr_write_b32 a105, 0
v_accvgpr_write_b32 a106, 0
v_accvgpr_write_b32 a107, 0
v_accvgpr_write_b32 a108, 0
v_accvgpr_write_b32 a109, 0
v_accvgpr_write_b32 a110, 0
v_accvgpr_write_b32 a111, 0
v_accvgpr_write_b32 a112, 0
v_accvgpr_write_b32 a113, 0
v_accvgpr_write_b32 a114, 0
v_accvgpr_write_b32 a115, 0
v_accvgpr_write_b32 a116, 0
v_accvgpr_write_b32 a117, 0
v_accvgpr_write_b32 a118, 0
v_accvgpr_write_b32 a119, 0
v_accvgpr_write_b32 a120, 0
v_accvgpr_write_b32 a121, 0
v_accvgpr_write_b32 a122, 0
v_accvgpr_write_b32 a123, 0
v_accvgpr_write_b32 a124, 0
v_accvgpr_write_b32 a125, 0
v_accvgpr_write_b32 a126, 0
)"
R"(v_accvgpr_write_b32 a127, 0
v_accvgpr_write_b32 a128, 0
v_accvgpr_write_b32 a129, 0
v_accvgpr_write_b32 a130, 0
v_accvgpr_write_b32 a131, 0
v_accvgpr_write_b32 a132, 0
v_accvgpr_write_b32 a133, 0
v_accvgpr_write_b32 a134, 0
v_accvgpr_write_b32 a135, 0
v_accvgpr_write_b32 a136, 0
v_accvgpr_write_b32 a137, 0
v_accvgpr_write_b32 a138, 0
v_accvgpr_write_b32 a139, 0
v_accvgpr_write_b32 a140, 0
v_accvgpr_write_b32 a141, 0
v_accvgpr_write_b32 a142, 0
v_accvgpr_write_b32 a143, 0
v_accvgpr_write_b32 a144, 0
v_accvgpr_write_b32 a145, 0
v_accvgpr_write_b32 a146, 0
v_accvgpr_write_b32 a147, 0
v_accvgpr_write_b32 a148, 0
v_accvgpr_write_b32 a149, 0
v_accvgpr_write_b32 a150, 0
v_accvgpr_write_b32 a151, 0
v_accvgpr_write_b32 a152, 0
v_accvgpr_write_b32 a153, 0
v_accvgpr_write_b32 a154, 0
v_accvgpr_write_b32 a155, 0
v_accvgpr_write_b32 a156, 0
v_accvgpr_write_b32 a157, 0
v_accvgpr_write_b32 a158, 0
v_accvgpr_write_b32 a159, 0
s_mov_b32 s31, 0
L_sl2_0:
s_add_u32 s16, s12, s97
s_addc_u32 s17, s13, 0
s_add_u32 s23, s31, 4096
buffer_load_dwordx4 a[160:163], v164, s[16:19], s31 offen offset:0
buffer_load_dwordx4 a[164:167], v164, s[16:19], s31 offen offset:64
buffer_load_dwordx4 a[168:171], v164, s[16:19], s31 offen offset:128
buffer_load_dwordx4 a[172:175], v164, s[16:19], s31 offen offset:192
buffer_load_dwordx4 a[176:179], v164, s[16:19], s23 offen offset:0
buffer_load_dwordx4 a[180:183], v164, s[16:19], s23 offen offset:64
buffer_load_dwordx4 a[184:187], v164, s[16:19], s23 offen offset:128
buffer_load_dwordx4 a[188:191], v164, s[16:19], s23 offen offset:192
s_waitcnt vmcnt(0)
s_add_u32 s16, s14, s97
s_addc_u32 s17, s15, 0
buffer_load_ushort v168, v165, s[16:19], s31 offen offset:0
buffer_load_ushort v169, v165, s[16:19], s31 offen offset:256
buffer_load_ushort v170, v165, s[16:19], s31 offen offset:512
buffer_load_ushort v171, v165, s[16:19], s31 offen offset:768
buffer_load_ushort v172, v165, s[16:19], s23 offen offset:0
buffer_load_ushort v173, v165, s[16:19], s23 offen offset:256
buffer_load_ushort v174, v165, s[16:19], s23 offen offset:512
buffer_load_ushort v175, v165, s[16:19], s23 offen offset:768
s_waitcnt vmcnt(0)
v_lshl_or_b32 v168, v169, 16, v168
v_accvgpr_write_b32 a192, v168
v_lshl_or_b32 v170, v171, 16, v170
v_accvgpr_write_b32 a193, v170
v_lshl_or_b32 v172, v173, 16, v172
v_accvgpr_write_b32 a194, v172
v_lshl_or_b32 v174, v175, 16, v174
v_accvgpr_write_b32 a195, v174
buffer_load_ushort v168, v165, s[16:19], s31 offen offset:32
buffer_load_ushort v169, v165, s[16:19], s31 offen offset:288
buffer_load_ushort v170, v165, s[16:19], s31 offen offset:544
buffer_load_ushort v171, v165, s[16:19], s31 offen offset:800
buffer_load_ushort v172, v165, s[16:19], s23 offen offset:32
buffer_load_ushort v173, v165, s[16:19], s23 offen offset:288
buffer_load_ushort v174, v165, s[16:19], s23 offen offset:544
buffer_load_ushort v175, v165, s[16:19], s23 offen offset:800
s_waitcnt vmcnt(0)
v_lshl_or_b32 v168, v169, 16, v168
v_accvgpr_write_b32 a196, v168
v_lshl_or_b32 v170, v171, 16, v170
v_accvgpr_write_b32 a197, v170
v_lshl_or_b32 v172, v173, 16, v172
v_accvgpr_write_b32 a198, v172
v_lshl_or_b32 v174, v175, 16, v174
v_accvgpr_write_b32 a199, v174
buffer_load_ushort v168, v165, s[16:19], s31 offen offset:64
buffer_load_ushort v169, v165, s[16:19], s31 offen offset:320
buffer_load_ushort v170, v165, s[16:19], s31 offen offset:576
buffer_load_ushort v171, v165, s[16:19], s31 offen offset:832
buffer_load_ushort v172, v165, s[16:19], s23 offen offset:64
buffer_load_ushort v173, v165, s[16:19], s23 offen offset:320
buffer_load_ushort v174, v165, s[16:19], s23 offen offset:576
buffer_load_ushort v175, v165, s[16:19], s23 offen offset:832
s_waitcnt vmcnt(0)
v_lshl_or_b32 v168, v169, 16, v168
v_accvgpr_write_b32 a200, v168
v_lshl_or_b32 v170, v171, 16, v170
v_accvgpr_write_b32 a201, v170
v_lshl_or_b32 v172, v173, 16, v172
v_accvgpr_write_b32 a202, v172
v_lshl_or_b32 v174, v175, 16, v174
v_accvgpr_write_b32 a203, v174
buffer_load_ushort v168, v165, s[16:19], s31 offen offset:96
buffer_load_ushort v169, v165, s[16:19], s31 offen offset:352
buffer_load_ushort v170, v165, s[16:19], s31 offen offset:608
buffer_load_ushort v171, v165, s[16:19], s31 offen offset:864
buffer_load_ushort v172, v165, s[16:19], s23 offen offset:96
buffer_load_ushort v173, v165, s[16:19], s23 offen offset:352
buffer_load_ushort v174, v165, s[16:19], s23 offen offset:608
buffer_load_ushort v175, v165, s[16:19], s23 offen offset:864
s_waitcnt vmcnt(0)
v_lshl_or_b32 v168, v169, 16, v168
v_accvgpr_write_b32 a204, v168
v_lshl_or_b32 v170, v171, 16, v170
v_accvgpr_write_b32 a205, v170
v_lshl_or_b32 v172, v173, 16, v172
v_accvgpr_write_b32 a206, v172
v_lshl_or_b32 v174, v175, 16, v174
v_accvgpr_write_b32 a207, v174
buffer_load_ushort v168, v165, s[16:19], s31 offen offset:128
buffer_load_ushort v169, v165, s[16:19], s31 offen offset:384
buffer_load_ushort v170, v165, s[16:19], s31 offen offset:640
buffer_load_ushort v171, v165, s[16:19], s31 offen offset:896
buffer_load_ushort v172, v165, s[16:19], s23 offen offset:128
buffer_load_ushort v173, v165, s[16:19], s23 offen offset:384
buffer_load_ushort v174, v165, s[16:19], s23 offen offset:640
buffer_load_ushort v175, v165, s[16:19], s23 offen offset:896
s_waitcnt vmcnt(0)
v_lshl_or_b32 v168, v169, 16, v168
v_accvgpr_write_b32 a208, v168
v_lshl_or_b32 v170, v171, 16, v170
v_accvgpr_write_b32 a209, v170
v_lshl_or_b32 v172, v173, 16, v172
v_accvgpr_write_b32 a210, v172
v_lshl_or_b32 v174, v175, 16, v174
v_accvgpr_write_b32 a211, v174
buffer_load_ushort v168, v165, s[16:19], s31 offen offset:160
buffer_load_ushort v169, v165, s[16:19], s31 offen offset:416
buffer_load_ushort v170, v165, s[16:19], s31 offen offset:672
buffer_load_ushort v171, v165, s[16:19], s31 offen offset:928
buffer_load_ushort v172, v165, s[16:19], s23 offen offset:160
buffer_load_ushort v173, v165, s[16:19], s23 offen offset:416
buffer_load_ushort v174, v165, s[16:19], s23 offen offset:672
buffer_load_ushort v175, v165, s[16:19], s23 offen offset:928
s_waitcnt vmcnt(0)
v_lshl_or_b32 v168, v169, 16, v168
v_accvgpr_write_b32 a212, v168
v_lshl_or_b32 v170, v171, 16, v170
v_accvgpr_write_b32 a213, v170
v_lshl_or_b32 v172, v173, 16, v172
v_accvgpr_write_b32 a214, v172
v_lshl_or_b32 v174, v175, 16, v174
v_accvgpr_write_b32 a215, v174
buffer_load_ushort v168, v165, s[16:19], s31 offen offset:192
buffer_load_ushort v169, v165, s[16:19], s31 offen offset:448
buffer_load_ushort v170, v165, s[16:19], s31 offen offset:704
buffer_load_ushort v171, v165, s[16:19], s31 offen offset:960
buffer_load_ushort v172, v165, s[16:19], s23 offen offset:192
buffer_load_ushort v173, v165, s[16:19], s23 offen offset:448
buffer_load_ushort v174, v165, s[16:19], s23 offen offset:704
buffer_load_ushort v175, v165, s[16:19], s23 offen offset:960
s_waitcnt vmcnt(0)
v_lshl_or_b32 v168, v169, 16, v168
v_accvgpr_write_b32 a216, v168
v_lshl_or_b32 v170, v171, 16, v170
v_accvgpr_write_b32 a217, v170
v_lshl_or_b32 v172, v173, 16, v172
v_accvgpr_write_b32 a218, v172
v_lshl_or_b32 v174, v175, 16, v174
v_accvgpr_write_b32 a219, v174
buffer_load_ushort v168, v165, s[16:19], s31 offen offset:224
buffer_load_ushort v169, v165, s[16:19], s31 offen offset:480
buffer_load_ushort v170, v165, s[16:19], s31 offen offset:736
buffer_load_ushort v171, v165, s[16:19], s31 offen offset:992
buffer_load_ushort v172, v165, s[16:19], s23 offen offset:224
buffer_load_ushort v173, v165, s[16:19], s23 offen offset:480
buffer_load_ushort v174, v165, s[16:19], s23 offen offset:736
buffer_load_ushort v175, v165, s[16:19], s23 offen offset:992
s_waitcnt vmcnt(0)
v_lshl_or_b32 v168, v169, 16, v168
v_accvgpr_write_b32 a220, v168
v_lshl_or_b32 v170, v171, 16, v170
v_accvgpr_write_b32 a221, v170
v_lshl_or_b32 v172, v173, 16, v172
v_accvgpr_write_b32 a222, v172
v_lshl_or_b32 v174, v175, 16, v174
v_accvgpr_write_b32 a223, v174
s_nop 1
M v[36:39],a[160:163],v[16:19],v[32:35]
M v[36:39],a[164:167],v[20:23],v[36:39]
M v[36:39],a[168:171],v[24:27],v[36:39]
M v[36:39],a[172:175],v[28:31],v[36:39]
M v[40:43],a[176:179],v[16:19],v[32:35]
M v[40:43],a[180:183],v[20:23],v[40:43]
M v[40:43],a[184:187],v[24:27],v[40:43]
M v[40:43],a[188:191],v[28:31],v[40:43]
s_nop 7
s_nop 7
E v36,v36
E v37,v37
E v38,v38
E v39,v39
E v40,v40
E v41,v41
E v42,v42
E v43,v43
s_nop 0
v_add_f32 v132, v132, v36
v_add_f32 v132, v132, v37
v_add_f32 v132, v132, v38
v_add_f32 v132, v132, v39
v_add_f32 v132, v132, v40
v_add_f32 v132, v132, v41
v_add_f32 v132, v132, v42
v_add_f32 v132, v132, v43
C v44,v36,v37
C v45,v38,v39
C v46,v40,v41
C v47,v42,v43
s_nop 1
M a[0:3],a[192:195],v[44:47],a[0:3]
M a[4:7],a[196:199],v[44:47],a[4:7]
M a[8:11],a[200:203],v[44:47],a[8:11]
M a[12:15],a[204:207],v[44:47],a[12:15]
M a[16:19],a[208:211],v[44:47],a[16:19]
M a[20:23],a[212:215],v[44:47],a[20:23]
M a[24:27],a[216:219],v[44:47],a[24:27]
M a[28:31],a[220:223],v[44:47],a[28:31]
s_nop 1
M v[68:71],a[160:163],v[48:51],v[64:67]
M v[68:71],a[164:167],v[52:55],v[68:71]
M v[68:71],a[168:171],v[56:59],v[68:71]
M v[68:71],a[172:175],v[60:63],v[68:71]
M v[72:75],a[176:179],v[48:51],v[64:67]
M v[72:75],a[180:183],v[52:55],v[72:75]
M v[72:75],a[184:187],v[56:59],v[72:75]
M v[72:75],a[188:191],v[60:63],v[72:75]
s_nop 7
s_nop 7
E v68,v68
E v69,v69
E v70,v70
E v71,v71
E v72,v72
E v73,v73
E v74,v74
E v75,v75
s_nop 0
v_add_f32 v133, v133, v68
v_add_f32 v133, v133, v69
v_add_f32 v133, v133, v70
v_add_f32 v133, v133, v71
v_add_f32 v133, v133, v72
v_add_f32 v133, v133, v73
v_add_f32 v133, v133, v74
v_add_f32 v133, v133, v75
C v76,v68,v69
C v77,v70,v71
C v78,v72,v73
C v79,v74,v75
s_nop 1
M a[32:35],a[192:195],v[76:79],a[32:35]
M a[36:39],a[196:199],v[76:79],a[36:39]
M a[40:43],a[200:203],v[76:79],a[40:43]
M a[44:47],a[204:207],v[76:79],a[44:47]
M a[48:51],a[208:211],v[76:79],a[48:51]
M a[52:55],a[212:215],v[76:79],a[52:55]
M a[56:59],a[216:219],v[76:79],a[56:59]
M a[60:63],a[220:223],v[76:79],a[60:63]
s_nop 1
M v[36:39],a[160:163],v[80:83],v[96:99]
M v[36:39],a[164:167],v[84:87],v[36:39]
M v[36:39],a[168:171],v[88:91],v[36:39]
M v[36:39],a[172:175],v[92:95],v[36:39]
M v[40:43],a[176:179],v[80:83],v[96:99]
M v[40:43],a[180:183],v[84:87],v[40:43]
M v[40:43],a[184:187],v[88:91],v[40:43]
M v[40:43],a[188:191],v[92:95],v[40:43]
s_nop 7
s_nop 7
E v36,v36
E v37,v37
E v38,v38
E v39,v39
E v40,v40
E v41,v41
E v42,v42
E v43,v43
s_nop 0
v_add_f32 v134, v134, v36
v_add_f32 v134, v134, v37
v_add_f32 v134, v134, v38
v_add_f32 v134, v134, v39
v_add_f32 v134, v134, v40
v_add_f32 v134, v134, v41
v_add_f32 v134, v134, v42
v_add_f32 v134, v134, v43
C v44,v36,v37
C v45,v38,v39
C v46,v40,v41
C v47,v42,v43
s_nop 1
M a[64:67],a[192:195],v[44:47],a[64:67]
M a[68:71],a[196:199],v[44:47],a[68:71]
M a[72:75],a[200:203],v[44:47],a[72:75]
M a[76:79],a[204:207],v[44:47],a[76:79]
M a[80:83],a[208:211],v[44:47],a[80:83]
M a[84:87],a[212:215],v[44:47],a[84:87]
M a[88:91],a[216:219],v[44:47],a[88:91]
M a[92:95],a[220:223],v[44:47],a[92:95]
s_nop 1
M v[68:71],a[160:163],v[112:115],v[128:131]
M v[68:71],a[164:167],v[116:119],v[68:71]
M v[68:71],a[168:171],v[120:123],v[68:71]
M v[68:71],a[172:175],v[124:127],v[68:71]
M v[72:75],a[176:179],v[112:115],v[128:131]
M v[72:75],a[180:183],v[116:119],v[72:75]
M v[72:75],a[184:187],v[120:123],v[72:75]
M v[72:75],a[188:191],v[124:127],v[72:75]
s_nop 7
s_nop 7
E v68,v68
E v69,v69
E v70,v70
E v71,v71
E v72,v72
E v73,v73
E v74,v74
E v75,v75
s_nop 0
v_add_f32 v135, v135, v68
v_add_f32 v135, v135, v69
v_add_f32 v135, v135, v70
v_add_f32 v135, v135, v71
v_add_f32 v135, v135, v72
v_add_f32 v135, v135, v73
v_add_f32 v135, v135, v74
v_add_f32 v135, v135, v75
C v76,v68,v69
C v77,v70,v71
C v78,v72,v73
C v79,v74,v75
s_nop 1
M a[96:99],a[192:195],v[76:79],a[96:99]
M a[100:103],a[196:199],v[76:79],a[100:103]
M a[104:107],a[200:203],v[76:79],a[104:107]
M a[108:111],a[204:207],v[76:79],a[108:111]
M a[112:115],a[208:211],v[76:79],a[112:115]
M a[116:119],a[212:215],v[76:79],a[116:119]
M a[120:123],a[216:219],v[76:79],a[120:123]
M a[124:127],a[220:223],v[76:79],a[124:127]
s_nop 1
M v[100:103],a[160:163],v[144:147],v[160:163]
M v[100:103],a[164:167],v[148:151],v[100:103]
M v[100:103],a[168:171],v[152:155],v[100:103]
M v[100:103],a[172:175],v[156:159],v[100:103]
M v[104:107],a[176:179],v[144:147],v[160:163]
M v[104:107],a[180:183],v[148:151],v[104:107]
M v[104:107],a[184:187],v[152:155],v[104:107]
M v[104:107],a[188:191],v[156:159],v[104:107]
s_nop 7
s_nop 7
E v100,v100
E v101,v101
E v102,v102
E v103,v103
E v104,v104
E v105,v105
E v106,v106
E v107,v107
s_nop 0
v_add_f32 v136, v136, v100
v_add_f32 v136, v136, v101
v_add_f32 v136, v136, v102
v_add_f32 v136, v136, v103
v_add_f32 v136, v136, v104
v_add_f32 v136, v136, v105
v_add_f32 v136, v136, v106
v_add_f32 v136, v136, v107
C v108,v100,v101
C v109,v102,v103
C v110,v104,v105
C v111,v106,v107
s_nop 1
M a[128:131],a[192:195],v[108:111],a[128:131]
M a[132:135],a[196:199],v[108:111],a[132:135]
M a[136:139],a[200:203],v[108:111],a[136:139]
M a[140:143],a[204:207],v[108:111],a[140:143]
M a[144:147],a[208:211],v[108:111],a[144:147]
M a[148:151],a[212:215],v[108:111],a[148:151]
M a[152:155],a[216:219],v[108:111],a[152:155]
M a[156:159],a[220:223],v[108:111],a[156:159]
s_add_u32 s31, s31, 8192
s_cmp_lt_u32 s31, 131072
s_cbranch_scc1 L_sl2_0
v_mov_b32 v164, v132
s_nop 1
v_permlane32_swap_b32 v132, v164
v_add_f32 v132, v132, v164
v_mov_b32 v164, v132
s_nop 1
v_permlane16_swap_b32 v132, v164
v_add_f32 v132, v132, v164
v_mov_b32 v165, v133
s_nop 1
v_permlane32_swap_b32 v133, v165
v_add_f32 v133, v133, v165
v_mov_b32 v165, v133
s_nop 1
v_permlane16_swap_b32 v133, v165
v_add_f32 v133, v133, v165
v_mov_b32 v166, v134
s_nop 1
v_permlane32_swap_b32 v134, v166
v_add_f32 v134, v134, v166
v_mov_b32 v166, v134
s_nop 1
v_permlane16_swap_b32 v134, v166
v_add_f32 v134, v134, v166
v_mov_b32 v167, v135
s_nop 1
v_permlane32_swap_b32 v135, v167
v_add_f32 v135, v135, v167
v_mov_b32 v167, v135
s_nop 1
v_permlane16_swap_b32 v135, v167
v_add_f32 v135, v135, v167
v_mov_b32 v168, v136
s_nop 1
v_permlane32_swap_b32 v136, v168
v_add_f32 v136, v136, v168
v_mov_b32 v168, v136
s_nop 1
v_permlane16_swap_b32 v136, v168
v_add_f32 v136, v136, v168
s_nop 7
s_nop 7
D a[160:163],v2,0
D a[164:167],v3,0
D a[168:171],v2,1024
D a[172:175],v3,1024
D a[176:179],v2,4096
D a[180:183],v3,4096
D a[184:187],v2,5120
D a[188:191],v3,5120
s_waitcnt lgkmcnt(0)
s_nop 7
s_nop 7
s_setpc_b64 s[98:99]
L_slow1:
s_waitcnt vmcnt(0) lgkmcnt(0)
s_mov_b32 s18, 131072
s_mov_b32 s19, 131072
v_lshrrev_b32 v166, 4, v1
v_and_b32 v165, 15, v166
v_lshrrev_b32 v166, 4, v166
v_lshlrev_b32 v164, 8, v165
v_lshl_add_u32 v164, v166, 4, v164
v_lshlrev_b32 v165, 1, v165
v_lshl_add_u32 v165, v166, 10, v165
v_mov_b32 v32, 0xff800000
v_mov_b32 v33, 0xff800000
v_mov_b32 v34, 0xff800000
v_mov_b32 v35, 0xff800000
v_mov_b32 v64, 0xff800000
v_mov_b32 v65, 0xff800000
v_mov_b32 v66, 0xff800000
v_mov_b32 v67, 0xff800000
v_mov_b32 v96, 0xff800000
v_mov_b32 v97, 0xff800000
v_mov_b32 v98, 0xff800000
v_mov_b32 v99, 0xff800000
v_mov_b32 v128, 0xff800000
v_mov_b32 v129, 0xff800000
v_mov_b32 v130, 0xff800000
v_mov_b32 v131, 0xff800000
v_mov_b32 v160, 0xff800000
v_mov_b32 v161, 0xff800000
v_mov_b32 v162, 0xff800000
v_mov_b32 v163, 0xff800000
s_mov_b32 s31, 0
L_sl1_1:
s_add_u32 s16, s12, s97
s_addc_u32 s17, s13, 0
s_add_u32 s23, s31, 4096
buffer_load_dwordx4 a[160:163], v164, s[16:19], s31 offen offset:0
buffer_load_dwordx4 a[164:167], v164, s[16:19], s31 offen offset:64
buffer_load_dwordx4 a[168:171], v164, s[16:19], s31 offen offset:128
buffer_load_dwordx4 a[172:175], v164, s[16:19], s31 offen offset:192
buffer_load_dwordx4 a[176:179], v164, s[16:19], s23 offen offset:0
buffer_load_dwordx4 a[180:183], v164, s[16:19], s23 offen offset:64
buffer_load_dwordx4 a[184:187], v164, s[16:19], s23 offen offset:128
buffer_load_dwordx4 a[188:191], v164, s[16:19], s23 offen offset:192
s_waitcnt vmcnt(0)
s_add_u32 s16, s14, s97
s_addc_u32 s17, s15, 0
M v[36:39],a[160:163],v[176:179],0
M v[36:39],a[164:167],v[180:183],v[36:39]
M v[36:39],a[168:171],v[184:187],v[36:39]
M v[36:39],a[172:175],v[188:191],v[36:39]
M v[40:43],a[176:179],v[176:179],0
M v[40:43],a[180:183],v[180:183],v[40:43]
)"
R"(M v[40:43],a[184:187],v[184:187],v[40:43]
M v[40:43],a[188:191],v[188:191],v[40:43]
s_nop 7
s_nop 7
v_max3_f32 v32, v32, v36, v37
v_max3_f32 v32, v32, v38, v39
v_max3_f32 v32, v32, v40, v41
v_max3_f32 v32, v32, v42, v43
M v[68:71],a[160:163],v[192:195],0
M v[68:71],a[164:167],v[196:199],v[68:71]
M v[68:71],a[168:171],v[200:203],v[68:71]
M v[68:71],a[172:175],v[204:207],v[68:71]
M v[72:75],a[176:179],v[192:195],0
M v[72:75],a[180:183],v[196:199],v[72:75]
M v[72:75],a[184:187],v[200:203],v[72:75]
M v[72:75],a[188:191],v[204:207],v[72:75]
s_nop 7
s_nop 7
v_max3_f32 v64, v64, v68, v69
v_max3_f32 v64, v64, v70, v71
v_max3_f32 v64, v64, v72, v73
v_max3_f32 v64, v64, v74, v75
M v[36:39],a[160:163],v[208:211],0
M v[36:39],a[164:167],v[212:215],v[36:39]
M v[36:39],a[168:171],v[216:219],v[36:39]
M v[36:39],a[172:175],v[220:223],v[36:39]
M v[40:43],a[176:179],v[208:211],0
M v[40:43],a[180:183],v[212:215],v[40:43]
M v[40:43],a[184:187],v[216:219],v[40:43]
M v[40:43],a[188:191],v[220:223],v[40:43]
s_nop 7
s_nop 7
v_max3_f32 v96, v96, v36, v37
v_max3_f32 v96, v96, v38, v39
v_max3_f32 v96, v96, v40, v41
v_max3_f32 v96, v96, v42, v43
M v[68:71],a[160:163],v[224:227],0
M v[68:71],a[164:167],v[228:231],v[68:71]
M v[68:71],a[168:171],v[232:235],v[68:71]
M v[68:71],a[172:175],v[236:239],v[68:71]
M v[72:75],a[176:179],v[224:227],0
M v[72:75],a[180:183],v[228:231],v[72:75]
M v[72:75],a[184:187],v[232:235],v[72:75]
M v[72:75],a[188:191],v[236:239],v[72:75]
s_nop 7
s_nop 7
v_max3_f32 v128, v128, v68, v69
v_max3_f32 v128, v128, v70, v71
v_max3_f32 v128, v128, v72, v73
v_max3_f32 v128, v128, v74, v75
M v[100:103],a[160:163],v[240:243],0
M v[100:103],a[164:167],v[244:247],v[100:103]
M v[100:103],a[168:171],v[248:251],v[100:103]
M v[100:103],a[172:175],v[252:255],v[100:103]
M v[104:107],a[176:179],v[240:243],0
M v[104:107],a[180:183],v[244:247],v[104:107]
M v[104:107],a[184:187],v[248:251],v[104:107]
M v[104:107],a[188:191],v[252:255],v[104:107]
s_nop 7
s_nop 7
v_max3_f32 v160, v160, v100, v101
v_max3_f32 v160, v160, v102, v103
v_max3_f32 v160, v160, v104, v105
v_max3_f32 v160, v160, v106, v107
s_add_u32 s31, s31, 8192
s_cmp_lt_u32 s31, 131072
s_cbranch_scc1 L_sl1_1
v_mov_b32 v33, v32
s_nop 1
v_permlane32_swap_b32 v32, v33
v_max_f32 v32, v32, v33
v_mov_b32 v33, v32
s_nop 1
v_permlane16_swap_b32 v32, v33
v_max_f32 v32, v32, v33
v_sub_f32 v33, 0, v32
v_sub_f32 v34, 0, v32
v_sub_f32 v35, 0, v32
v_sub_f32 v32, 0, v32
v_mov_b32 v132, 0
v_mov_b32 v65, v64
s_nop 1
v_permlane32_swap_b32 v64, v65
v_max_f32 v64, v64, v65
v_mov_b32 v65, v64
s_nop 1
v_permlane16_swap_b32 v64, v65
v_max_f32 v64, v64, v65
v_sub_f32 v65, 0, v64
v_sub_f32 v66, 0, v64
v_sub_f32 v67, 0, v64
v_sub_f32 v64, 0, v64
v_mov_b32 v133, 0
v_mov_b32 v97, v96
s_nop 1
v_permlane32_swap_b32 v96, v97
v_max_f32 v96, v96, v97
v_mov_b32 v97, v96
s_nop 1
v_permlane16_swap_b32 v96, v97
v_max_f32 v96, v96, v97
v_sub_f32 v97, 0, v96
v_sub_f32 v98, 0, v96
v_sub_f32 v99, 0, v96
v_sub_f32 v96, 0, v96
v_mov_b32 v134, 0
v_mov_b32 v129, v128
s_nop 1
v_permlane32_swap_b32 v128, v129
v_max_f32 v128, v128, v129
v_mov_b32 v129, v128
s_nop 1
v_permlane16_swap_b32 v128, v129
v_max_f32 v128, v128, v129
v_sub_f32 v129, 0, v128
v_sub_f32 v130, 0, v128
v_sub_f32 v131, 0, v128
v_sub_f32 v128, 0, v128
v_mov_b32 v135, 0
v_mov_b32 v161, v160
s_nop 1
v_permlane32_swap_b32 v160, v161
v_max_f32 v160, v160, v161
v_mov_b32 v161, v160
s_nop 1
v_permlane16_swap_b32 v160, v161
v_max_f32 v160, v160, v161
v_sub_f32 v161, 0, v160
v_sub_f32 v162, 0, v160
v_sub_f32 v163, 0, v160
v_sub_f32 v160, 0, v160
v_mov_b32 v136, 0
v_accvgpr_write_b32 a0, 0
v_accvgpr_write_b32 a1, 0
v_accvgpr_write_b32 a2, 0
v_accvgpr_write_b32 a3, 0
v_accvgpr_write_b32 a4, 0
v_accvgpr_write_b32 a5, 0
v_accvgpr_write_b32 a6, 0
v_accvgpr_write_b32 a7, 0
v_accvgpr_write_b32 a8, 0
v_accvgpr_write_b32 a9, 0
v_accvgpr_write_b32 a10, 0
v_accvgpr_write_b32 a11, 0
v_accvgpr_write_b32 a12, 0
v_accvgpr_write_b32 a13, 0
v_accvgpr_write_b32 a14, 0
v_accvgpr_write_b32 a15, 0
v_accvgpr_write_b32 a16, 0
v_accvgpr_write_b32 a17, 0
v_accvgpr_write_b32 a18, 0
v_accvgpr_write_b32 a19, 0
v_accvgpr_write_b32 a20, 0
v_accvgpr_write_b32 a21, 0
v_accvgpr_write_b32 a22, 0
v_accvgpr_write_b32 a23, 0
v_accvgpr_write_b32 a24, 0
v_accvgpr_write_b32 a25, 0
v_accvgpr_write_b32 a26, 0
v_accvgpr_write_b32 a27, 0
v_accvgpr_write_b32 a28, 0
v_accvgpr_write_b32 a29, 0
v_accvgpr_write_b32 a30, 0
v_accvgpr_write_b32 a31, 0
v_accvgpr_write_b32 a32, 0
v_accvgpr_write_b32 a33, 0
v_accvgpr_write_b32 a34, 0
v_accvgpr_write_b32 a35, 0
v_accvgpr_write_b32 a36, 0
v_accvgpr_write_b32 a37, 0
v_accvgpr_write_b32 a38, 0
v_accvgpr_write_b32 a39, 0
v_accvgpr_write_b32 a40, 0
v_accvgpr_write_b32 a41, 0
v_accvgpr_write_b32 a42, 0
v_accvgpr_write_b32 a43, 0
v_accvgpr_write_b32 a44, 0
v_accvgpr_write_b32 a45, 0
v_accvgpr_write_b32 a46, 0
v_accvgpr_write_b32 a47, 0
v_accvgpr_write_b32 a48, 0
v_accvgpr_write_b32 a49, 0
v_accvgpr_write_b32 a50, 0
v_accvgpr_write_b32 a51, 0
v_accvgpr_write_b32 a52, 0
v_accvgpr_write_b32 a53, 0
v_accvgpr_write_b32 a54, 0
v_accvgpr_write_b32 a55, 0
v_accvgpr_write_b32 a56, 0
v_accvgpr_write_b32 a57, 0
v_accvgpr_write_b32 a58, 0
v_accvgpr_write_b32 a59, 0
v_accvgpr_write_b32 a60, 0
v_accvgpr_write_b32 a61, 0
v_accvgpr_write_b32 a62, 0
v_accvgpr_write_b32 a63, 0
v_accvgpr_write_b32 a64, 0
v_accvgpr_write_b32 a65, 0
v_accvgpr_write_b32 a66, 0
v_accvgpr_write_b32 a67, 0
v_accvgpr_write_b32 a68, 0
v_accvgpr_write_b32 a69, 0
v_accvgpr_write_b32 a70, 0
v_accvgpr_write_b32 a71, 0
v_accvgpr_write_b32 a72, 0
v_accvgpr_write_b32 a73, 0
v_accvgpr_write_b32 a74, 0
v_accvgpr_write_b32 a75, 0
v_accvgpr_write_b32 a76, 0
v_accvgpr_write_b32 a77, 0
v_accvgpr_write_b32 a78, 0
v_accvgpr_write_b32 a79, 0
v_accvgpr_write_b32 a80, 0
v_accvgpr_write_b32 a81, 0
v_accvgpr_write_b32 a82, 0
v_accvgpr_write_b32 a83, 0
v_accvgpr_write_b32 a84, 0
v_accvgpr_write_b32 a85, 0
v_accvgpr_write_b32 a86, 0
v_accvgpr_write_b32 a87, 0
v_accvgpr_write_b32 a88, 0
v_accvgpr_write_b32 a89, 0
v_accvgpr_write_b32 a90, 0
v_accvgpr_write_b32 a91, 0
v_accvgpr_write_b32 a92, 0
v_accvgpr_write_b32 a93, 0
v_accvgpr_write_b32 a94, 0
v_accvgpr_write_b32 a95, 0
v_accvgpr_write_b32 a96, 0
v_accvgpr_write_b32 a97, 0
v_accvgpr_write_b32 a98, 0
v_accvgpr_write_b32 a99, 0
v_accvgpr_write_b32 a100, 0
v_accvgpr_write_b32 a101, 0
v_accvgpr_write_b32 a102, 0
v_accvgpr_write_b32 a103, 0
v_accvgpr_write_b32 a104, 0
v_accvgpr_write_b32 a105, 0
v_accvgpr_write_b32 a106, 0
v_accvgpr_write_b32 a107, 0
v_accvgpr_write_b32 a108, 0
v_accvgpr_write_b32 a109, 0
v_accvgpr_write_b32 a110, 0
v_accvgpr_write_b32 a111, 0
v_accvgpr_write_b32 a112, 0
v_accvgpr_write_b32 a113, 0
v_accvgpr_write_b32 a114, 0
v_accvgpr_write_b32 a115, 0
v_accvgpr_write_b32 a116, 0
v_accvgpr_write_b32 a117, 0
v_accvgpr_write_b32 a118, 0
v_accvgpr_write_b32 a119, 0
v_accvgpr_write_b32 a120, 0
v_accvgpr_write_b32 a121, 0
v_accvgpr_write_b32 a122, 0
v_accvgpr_write_b32 a123, 0
v_accvgpr_write_b32 a124, 0
v_accvgpr_write_b32 a125, 0
v_accvgpr_write_b32 a126, 0
v_accvgpr_write_b32 a127, 0
v_accvgpr_write_b32 a128, 0
v_accvgpr_write_b32 a129, 0
v_accvgpr_write_b32 a130, 0
v_accvgpr_write_b32 a131, 0
v_accvgpr_write_b32 a132, 0
v_accvgpr_write_b32 a133, 0
v_accvgpr_write_b32 a134, 0
v_accvgpr_write_b32 a135, 0
v_accvgpr_write_b32 a136, 0
v_accvgpr_write_b32 a137, 0
v_accvgpr_write_b32 a138, 0
v_accvgpr_write_b32 a139, 0
v_accvgpr_write_b32 a140, 0
v_accvgpr_write_b32 a141, 0
v_accvgpr_write_b32 a142, 0
v_accvgpr_write_b32 a143, 0
v_accvgpr_write_b32 a144, 0
v_accvgpr_write_b32 a145, 0
v_accvgpr_write_b32 a146, 0
v_accvgpr_write_b32 a147, 0
v_accvgpr_write_b32 a148, 0
v_accvgpr_write_b32 a149, 0
v_accvgpr_write_b32 a150, 0
v_accvgpr_write_b32 a151, 0
v_accvgpr_write_b32 a152, 0
v_accvgpr_write_b32 a153, 0
v_accvgpr_write_b32 a154, 0
v_accvgpr_write_b32 a155, 0
v_accvgpr_write_b32 a156, 0
v_accvgpr_write_b32 a157, 0
v_accvgpr_write_b32 a158, 0
v_accvgpr_write_b32 a159, 0
s_mov_b32 s31, 0
L_sl2_1:
s_add_u32 s16, s12, s97
s_addc_u32 s17, s13, 0
s_add_u32 s23, s31, 4096
buffer_load_dwordx4 a[160:163], v164, s[16:19], s31 offen offset:0
buffer_load_dwordx4 a[164:167], v164, s[16:19], s31 offen offset:64
buffer_load_dwordx4 a[168:171], v164, s[16:19], s31 offen offset:128
buffer_load_dwordx4 a[172:175], v164, s[16:19], s31 offen offset:192
buffer_load_dwordx4 a[176:179], v164, s[16:19], s23 offen offset:0
buffer_load_dwordx4 a[180:183], v164, s[16:19], s23 offen offset:64
buffer_load_dwordx4 a[184:187], v164, s[16:19], s23 offen offset:128
buffer_load_dwordx4 a[188:191], v164, s[16:19], s23 offen offset:192
s_waitcnt vmcnt(0)
s_add_u32 s16, s14, s97
s_addc_u32 s17, s15, 0
buffer_load_ushort v168, v165, s[16:19], s31 offen offset:0
buffer_load_ushort v169, v165, s[16:19], s31 offen offset:256
buffer_load_ushort v170, v165, s[16:19], s31 offen offset:512
buffer_load_ushort v171, v165, s[16:19], s31 offen offset:768
buffer_load_ushort v172, v165, s[16:19], s23 offen offset:0
buffer_load_ushort v173, v165, s[16:19], s23 offen offset:256
buffer_load_ushort v174, v165, s[16:19], s23 offen offset:512
buffer_load_ushort v175, v165, s[16:19], s23 offen offset:768
s_waitcnt vmcnt(0)
v_lshl_or_b32 v168, v169, 16, v168
v_accvgpr_write_b32 a192, v168
v_lshl_or_b32 v170, v171, 16, v170
v_accvgpr_write_b32 a193, v170
v_lshl_or_b32 v172, v173, 16, v172
v_accvgpr_write_b32 a194, v172
v_lshl_or_b32 v174, v175, 16, v174
v_accvgpr_write_b32 a195, v174
buffer_load_ushort v168, v165, s[16:19], s31 offen offset:32
buffer_load_ushort v169, v165, s[16:19], s31 offen offset:288
buffer_load_ushort v170, v165, s[16:19], s31 offen offset:544
buffer_load_ushort v171, v165, s[16:19], s31 offen offset:800
buffer_load_ushort v172, v165, s[16:19], s23 offen offset:32
buffer_load_ushort v173, v165, s[16:19], s23 offen offset:288
buffer_load_ushort v174, v165, s[16:19], s23 offen offset:544
buffer_load_ushort v175, v165, s[16:19], s23 offen offset:800
s_waitcnt vmcnt(0)
v_lshl_or_b32 v168, v169, 16, v168
v_accvgpr_write_b32 a196, v168
v_lshl_or_b32 v170, v171, 16, v170
v_accvgpr_write_b32 a197, v170
v_lshl_or_b32 v172, v173, 16, v172
v_accvgpr_write_b32 a198, v172
v_lshl_or_b32 v174, v175, 16, v174
v_accvgpr_write_b32 a199, v174
buffer_load_ushort v168, v165, s[16:19], s31 offen offset:64
buffer_load_ushort v169, v165, s[16:19], s31 offen offset:320
buffer_load_ushort v170, v165, s[16:19], s31 offen offset:576
buffer_load_ushort v171, v165, s[16:19], s31 offen offset:832
buffer_load_ushort v172, v165, s[16:19], s23 offen offset:64
buffer_load_ushort v173, v165, s[16:19], s23 offen offset:320
buffer_load_ushort v174, v165, s[16:19], s23 offen offset:576
buffer_load_ushort v175, v165, s[16:19], s23 offen offset:832
s_waitcnt vmcnt(0)
v_lshl_or_b32 v168, v169, 16, v168
v_accvgpr_write_b32 a200, v168
v_lshl_or_b32 v170, v171, 16, v170
v_accvgpr_write_b32 a201, v170
v_lshl_or_b32 v172, v173, 16, v172
v_accvgpr_write_b32 a202, v172
v_lshl_or_b32 v174, v175, 16, v174
v_accvgpr_write_b32 a203, v174
buffer_load_ushort v168, v165, s[16:19], s31 offen offset:96
buffer_load_ushort v169, v165, s[16:19], s31 offen offset:352
buffer_load_ushort v170, v165, s[16:19], s31 offen offset:608
buffer_load_ushort v171, v165, s[16:19], s31 offen offset:864
buffer_load_ushort v172, v165, s[16:19], s23 offen offset:96
buffer_load_ushort v173, v165, s[16:19], s23 offen offset:352
buffer_load_ushort v174, v165, s[16:19], s23 offen offset:608
buffer_load_ushort v175, v165, s[16:19], s23 offen offset:864
s_waitcnt vmcnt(0)
v_lshl_or_b32 v168, v169, 16, v168
v_accvgpr_write_b32 a204, v168
v_lshl_or_b32 v170, v171, 16, v170
v_accvgpr_write_b32 a205, v170
v_lshl_or_b32 v172, v173, 16, v172
v_accvgpr_write_b32 a206, v172
v_lshl_or_b32 v174, v175, 16, v174
v_accvgpr_write_b32 a207, v174
buffer_load_ushort v168, v165, s[16:19], s31 offen offset:128
buffer_load_ushort v169, v165, s[16:19], s31 offen offset:384
buffer_load_ushort v170, v165, s[16:19], s31 offen offset:640
buffer_load_ushort v171, v165, s[16:19], s31 offen offset:896
buffer_load_ushort v172, v165, s[16:19], s23 offen offset:128
buffer_load_ushort v173, v165, s[16:19], s23 offen offset:384
buffer_load_ushort v174, v165, s[16:19], s23 offen offset:640
buffer_load_ushort v175, v165, s[16:19], s23 offen offset:896
s_waitcnt vmcnt(0)
v_lshl_or_b32 v168, v169, 16, v168
v_accvgpr_write_b32 a208, v168
v_lshl_or_b32 v170, v171, 16, v170
v_accvgpr_write_b32 a209, v170
v_lshl_or_b32 v172, v173, 16, v172
v_accvgpr_write_b32 a210, v172
v_lshl_or_b32 v174, v175, 16, v174
v_accvgpr_write_b32 a211, v174
buffer_load_ushort v168, v165, s[16:19], s31 offen offset:160
buffer_load_ushort v169, v165, s[16:19], s31 offen offset:416
buffer_load_ushort v170, v165, s[16:19], s31 offen offset:672
buffer_load_ushort v171, v165, s[16:19], s31 offen offset:928
buffer_load_ushort v172, v165, s[16:19], s23 offen offset:160
buffer_load_ushort v173, v165, s[16:19], s23 offen offset:416
buffer_load_ushort v174, v165, s[16:19], s23 offen offset:672
buffer_load_ushort v175, v165, s[16:19], s23 offen offset:928
s_waitcnt vmcnt(0)
v_lshl_or_b32 v168, v169, 16, v168
v_accvgpr_write_b32 a212, v168
v_lshl_or_b32 v170, v171, 16, v170
v_accvgpr_write_b32 a213, v170
v_lshl_or_b32 v172, v173, 16, v172
v_accvgpr_write_b32 a214, v172
v_lshl_or_b32 v174, v175, 16, v174
v_accvgpr_write_b32 a215, v174
buffer_load_ushort v168, v165, s[16:19], s31 offen offset:192
buffer_load_ushort v169, v165, s[16:19], s31 offen offset:448
buffer_load_ushort v170, v165, s[16:19], s31 offen offset:704
buffer_load_ushort v171, v165, s[16:19], s31 offen offset:960
buffer_load_ushort v172, v165, s[16:19], s23 offen offset:192
buffer_load_ushort v173, v165, s[16:19], s23 offen offset:448
buffer_load_ushort v174, v165, s[16:19], s23 offen offset:704
buffer_load_ushort v175, v165, s[16:19], s23 offen offset:960
s_waitcnt vmcnt(0)
v_lshl_or_b32 v168, v169, 16, v168
v_accvgpr_write_b32 a216, v168
v_lshl_or_b32 v170, v171, 16, v170
v_accvgpr_write_b32 a217, v170
v_lshl_or_b32 v172, v173, 16, v172
v_accvgpr_write_b32 a218, v172
v_lshl_or_b32 v174, v175, 16, v174
v_accvgpr_write_b32 a219, v174
buffer_load_ushort v168, v165, s[16:19], s31 offen offset:224
buffer_load_ushort v169, v165, s[16:19], s31 offen offset:480
buffer_load_ushort v170, v165, s[16:19], s31 offen offset:736
buffer_load_ushort v171, v165, s[16:19], s31 offen offset:992
buffer_load_ushort v172, v165, s[16:19], s23 offen offset:224
buffer_load_ushort v173, v165, s[16:19], s23 offen offset:480
buffer_load_ushort v174, v165, s[16:19], s23 offen offset:736
buffer_load_ushort v175, v165, s[16:19], s23 offen offset:992
s_waitcnt vmcnt(0)
v_lshl_or_b32 v168, v169, 16, v168
v_accvgpr_write_b32 a220, v168
v_lshl_or_b32 v170, v171, 16, v170
v_accvgpr_write_b32 a221, v170
v_lshl_or_b32 v172, v173, 16, v172
v_accvgpr_write_b32 a222, v172
v_lshl_or_b32 v174, v175, 16, v174
v_accvgpr_write_b32 a223, v174
s_nop 1
M v[36:39],a[160:163],v[176:179],v[32:35]
M v[36:39],a[164:167],v[180:183],v[36:39]
M v[36:39],a[168:171],v[184:187],v[36:39]
M v[36:39],a[172:175],v[188:191],v[36:39]
M v[40:43],a[176:179],v[176:179],v[32:35]
M v[40:43],a[180:183],v[180:183],v[40:43]
M v[40:43],a[184:187],v[184:187],v[40:43]
M v[40:43],a[188:191],v[188:191],v[40:43]
s_nop 7
s_nop 7
E v36,v36
E v37,v37
E v38,v38
E v39,v39
E v40,v40
E v41,v41
E v42,v42
E v43,v43
s_nop 0
v_add_f32 v132, v132, v36
v_add_f32 v132, v132, v37
v_add_f32 v132, v132, v38
v_add_f32 v132, v132, v39
v_add_f32 v132, v132, v40
v_add_f32 v132, v132, v41
v_add_f32 v132, v132, v42
v_add_f32 v132, v132, v43
C v44,v36,v37
C v45,v38,v39
C v46,v40,v41
C v47,v42,v43
s_nop 1
M a[0:3],a[192:195],v[44:47],a[0:3]
M a[4:7],a[196:199],v[44:47],a[4:7]
M a[8:11],a[200:203],v[44:47],a[8:11]
M a[12:15],a[204:207],v[44:47],a[12:15]
M a[16:19],a[208:211],v[44:47],a[16:19]
M a[20:23],a[212:215],v[44:47],a[20:23]
M a[24:27],a[216:219],v[44:47],a[24:27]
M a[28:31],a[220:223],v[44:47],a[28:31]
s_nop 1
M v[68:71],a[160:163],v[192:195],v[64:67]
M v[68:71],a[164:167],v[196:199],v[68:71]
M v[68:71],a[168:171],v[200:203],v[68:71]
M v[68:71],a[172:175],v[204:207],v[68:71]
M v[72:75],a[176:179],v[192:195],v[64:67]
M v[72:75],a[180:183],v[196:199],v[72:75]
M v[72:75],a[184:187],v[200:203],v[72:75]
M v[72:75],a[188:191],v[204:207],v[72:75]
s_nop 7
s_nop 7
E v68,v68
E v69,v69
E v70,v70
E v71,v71
)"
R"(E v72,v72
E v73,v73
E v74,v74
E v75,v75
s_nop 0
v_add_f32 v133, v133, v68
v_add_f32 v133, v133, v69
v_add_f32 v133, v133, v70
v_add_f32 v133, v133, v71
v_add_f32 v133, v133, v72
v_add_f32 v133, v133, v73
v_add_f32 v133, v133, v74
v_add_f32 v133, v133, v75
C v76,v68,v69
C v77,v70,v71
C v78,v72,v73
C v79,v74,v75
s_nop 1
M a[32:35],a[192:195],v[76:79],a[32:35]
M a[36:39],a[196:199],v[76:79],a[36:39]
M a[40:43],a[200:203],v[76:79],a[40:43]
M a[44:47],a[204:207],v[76:79],a[44:47]
M a[48:51],a[208:211],v[76:79],a[48:51]
M a[52:55],a[212:215],v[76:79],a[52:55]
M a[56:59],a[216:219],v[76:79],a[56:59]
M a[60:63],a[220:223],v[76:79],a[60:63]
s_nop 1
M v[36:39],a[160:163],v[208:211],v[96:99]
M v[36:39],a[164:167],v[212:215],v[36:39]
M v[36:39],a[168:171],v[216:219],v[36:39]
M v[36:39],a[172:175],v[220:223],v[36:39]
M v[40:43],a[176:179],v[208:211],v[96:99]
M v[40:43],a[180:183],v[212:215],v[40:43]
M v[40:43],a[184:187],v[216:219],v[40:43]
M v[40:43],a[188:191],v[220:223],v[40:43]
s_nop 7
s_nop 7
E v36,v36
E v37,v37
E v38,v38
E v39,v39
E v40,v40
E v41,v41
E v42,v42
E v43,v43
s_nop 0
v_add_f32 v134, v134, v36
v_add_f32 v134, v134, v37
v_add_f32 v134, v134, v38
v_add_f32 v134, v134, v39
v_add_f32 v134, v134, v40
v_add_f32 v134, v134, v41
v_add_f32 v134, v134, v42
v_add_f32 v134, v134, v43
C v44,v36,v37
C v45,v38,v39
C v46,v40,v41
C v47,v42,v43
s_nop 1
M a[64:67],a[192:195],v[44:47],a[64:67]
M a[68:71],a[196:199],v[44:47],a[68:71]
M a[72:75],a[200:203],v[44:47],a[72:75]
M a[76:79],a[204:207],v[44:47],a[76:79]
M a[80:83],a[208:211],v[44:47],a[80:83]
M a[84:87],a[212:215],v[44:47],a[84:87]
M a[88:91],a[216:219],v[44:47],a[88:91]
M a[92:95],a[220:223],v[44:47],a[92:95]
s_nop 1
M v[68:71],a[160:163],v[224:227],v[128:131]
M v[68:71],a[164:167],v[228:231],v[68:71]
M v[68:71],a[168:171],v[232:235],v[68:71]
M v[68:71],a[172:175],v[236:239],v[68:71]
M v[72:75],a[176:179],v[224:227],v[128:131]
M v[72:75],a[180:183],v[228:231],v[72:75]
M v[72:75],a[184:187],v[232:235],v[72:75]
M v[72:75],a[188:191],v[236:239],v[72:75]
s_nop 7
s_nop 7
E v68,v68
E v69,v69
E v70,v70
E v71,v71
E v72,v72
E v73,v73
E v74,v74
E v75,v75
s_nop 0
v_add_f32 v135, v135, v68
v_add_f32 v135, v135, v69
v_add_f32 v135, v135, v70
v_add_f32 v135, v135, v71
v_add_f32 v135, v135, v72
v_add_f32 v135, v135, v73
v_add_f32 v135, v135, v74
v_add_f32 v135, v135, v75
C v76,v68,v69
C v77,v70,v71
C v78,v72,v73
C v79,v74,v75
s_nop 1
M a[96:99],a[192:195],v[76:79],a[96:99]
M a[100:103],a[196:199],v[76:79],a[100:103]
M a[104:107],a[200:203],v[76:79],a[104:107]
M a[108:111],a[204:207],v[76:79],a[108:111]
M a[112:115],a[208:211],v[76:79],a[112:115]
M a[116:119],a[212:215],v[76:79],a[116:119]
M a[120:123],a[216:219],v[76:79],a[120:123]
M a[124:127],a[220:223],v[76:79],a[124:127]
s_nop 1
M v[100:103],a[160:163],v[240:243],v[160:163]
M v[100:103],a[164:167],v[244:247],v[100:103]
M v[100:103],a[168:171],v[248:251],v[100:103]
M v[100:103],a[172:175],v[252:255],v[100:103]
M v[104:107],a[176:179],v[240:243],v[160:163]
M v[104:107],a[180:183],v[244:247],v[104:107]
M v[104:107],a[184:187],v[248:251],v[104:107]
M v[104:107],a[188:191],v[252:255],v[104:107]
s_nop 7
s_nop 7
E v100,v100
E v101,v101
E v102,v102
E v103,v103
E v104,v104
E v105,v105
E v106,v106
E v107,v107
s_nop 0
v_add_f32 v136, v136, v100
v_add_f32 v136, v136, v101
v_add_f32 v136, v136, v102
v_add_f32 v136, v136, v103
v_add_f32 v136, v136, v104
v_add_f32 v136, v136, v105
v_add_f32 v136, v136, v106
v_add_f32 v136, v136, v107
C v108,v100,v101
C v109,v102,v103
C v110,v104,v105
C v111,v106,v107
s_nop 1
M a[128:131],a[192:195],v[108:111],a[128:131]
M a[132:135],a[196:199],v[108:111],a[132:135]
M a[136:139],a[200:203],v[108:111],a[136:139]
M a[140:143],a[204:207],v[108:111],a[140:143]
M a[144:147],a[208:211],v[108:111],a[144:147]
M a[148:151],a[212:215],v[108:111],a[148:151]
M a[152:155],a[216:219],v[108:111],a[152:155]
M a[156:159],a[220:223],v[108:111],a[156:159]
s_add_u32 s31, s31, 8192
s_cmp_lt_u32 s31, 131072
s_cbranch_scc1 L_sl2_1
v_mov_b32 v164, v132
s_nop 1
v_permlane32_swap_b32 v132, v164
v_add_f32 v132, v132, v164
v_mov_b32 v164, v132
s_nop 1
v_permlane16_swap_b32 v132, v164
v_add_f32 v132, v132, v164
v_mov_b32 v165, v133
s_nop 1
v_permlane32_swap_b32 v133, v165
v_add_f32 v133, v133, v165
v_mov_b32 v165, v133
s_nop 1
v_permlane16_swap_b32 v133, v165
v_add_f32 v133, v133, v165
v_mov_b32 v166, v134
s_nop 1
v_permlane32_swap_b32 v134, v166
v_add_f32 v134, v134, v166
v_mov_b32 v166, v134
s_nop 1
v_permlane16_swap_b32 v134, v166
v_add_f32 v134, v134, v166
v_mov_b32 v167, v135
s_nop 1
v_permlane32_swap_b32 v135, v167
v_add_f32 v135, v135, v167
v_mov_b32 v167, v135
s_nop 1
v_permlane16_swap_b32 v135, v167
v_add_f32 v135, v135, v167
v_mov_b32 v168, v136
s_nop 1
v_permlane32_swap_b32 v136, v168
v_add_f32 v136, v136, v168
v_mov_b32 v168, v136
s_nop 1
v_permlane16_swap_b32 v136, v168
v_add_f32 v136, v136, v168
s_nop 7
s_nop 7
D a[160:163],v2,0
D a[164:167],v3,0
D a[168:171],v2,1024
D a[172:175],v3,1024
D a[176:179],v2,4096
D a[180:183],v3,4096
D a[184:187],v2,5120
D a[188:191],v3,5120
s_waitcnt lgkmcnt(0)
s_nop 7
s_nop 7
s_setpc_b64 s[98:99]
L_pc0X0:
s_sleep 1
s_memrealtime s[18:19]
s_waitcnt lgkmcnt(0)
s_sub_u32 s19, s3, s18
s_cmp_gt_i32 s19, 0
s_cbranch_scc0 L_pe0X0
s_cmp_lt_i32 s19, 220
s_cbranch_scc1 L_pc0X0
s_mov_b32 s19, 0
s_branch L_pe0X0
L_pc0X1:
s_sleep 1
s_memrealtime s[18:19]
s_waitcnt lgkmcnt(0)
s_sub_u32 s19, s3, s18
s_cmp_gt_i32 s19, 0
s_cbranch_scc0 L_pe0X1
s_cmp_lt_i32 s19, 220
s_cbranch_scc1 L_pc0X1
s_mov_b32 s19, 0
s_branch L_pe0X1
L_pc0XL:
s_sleep 1
s_memrealtime s[18:19]
s_waitcnt lgkmcnt(0)
s_sub_u32 s19, s3, s18
s_cmp_gt_i32 s19, 0
s_cbranch_scc0 L_pe0XL
s_cmp_lt_i32 s19, 220
s_cbranch_scc1 L_pc0XL
s_mov_b32 s19, 0
s_branch L_pe0XL
L_pc1S0:
s_sleep 1
s_memrealtime s[18:19]
s_waitcnt lgkmcnt(0)
s_sub_u32 s19, s3, s18
s_cmp_gt_i32 s19, 0
s_cbranch_scc0 L_pe1S0
s_cmp_lt_i32 s19, 220
s_cbranch_scc1 L_pc1S0
s_mov_b32 s19, 0
s_branch L_pe1S0
L_pc1S1:
s_sleep 1
s_memrealtime s[18:19]
s_waitcnt lgkmcnt(0)
s_sub_u32 s19, s3, s18
s_cmp_gt_i32 s19, 0
s_cbranch_scc0 L_pe1S1
s_cmp_lt_i32 s19, 220
s_cbranch_scc1 L_pc1S1
s_mov_b32 s19, 0
s_branch L_pe1S1
L_pc1SL:
s_sleep 1
s_memrealtime s[18:19]
s_waitcnt lgkmcnt(0)
s_sub_u32 s19, s3, s18
s_cmp_gt_i32 s19, 0
s_cbranch_scc0 L_pe1SL
s_cmp_lt_i32 s19, 220
s_cbranch_scc1 L_pc1SL
s_mov_b32 s19, 0
s_branch L_pe1SL
L_pc0Y0:
s_sleep 1
s_memrealtime s[18:19]
s_waitcnt lgkmcnt(0)
s_sub_u32 s19, s3, s18
s_cmp_gt_i32 s19, 0
s_cbranch_scc0 L_pe0Y0
s_cmp_lt_i32 s19, 220
s_cbranch_scc1 L_pc0Y0
s_mov_b32 s19, 0
s_branch L_pe0Y0
L_pc0Y1:
s_sleep 1
s_memrealtime s[18:19]
s_waitcnt lgkmcnt(0)
s_sub_u32 s19, s3, s18
s_cmp_gt_i32 s19, 0
s_cbranch_scc0 L_pe0Y1
s_cmp_lt_i32 s19, 220
s_cbranch_scc1 L_pc0Y1
s_mov_b32 s19, 0
s_branch L_pe0Y1
L_pc0YL:
s_sleep 1
s_memrealtime s[18:19]
s_waitcnt lgkmcnt(0)
s_sub_u32 s19, s3, s18
s_cmp_gt_i32 s19, 0
s_cbranch_scc0 L_pe0YL
s_cmp_lt_i32 s19, 220
s_cbranch_scc1 L_pc0YL
s_mov_b32 s19, 0
s_branch L_pe0YL
L_code_end:
.purgem M
.purgem T
.purgem D
.purgem C
.purgem R
.purgem E
)"
  :: "s"(__builtin_amdgcn_kernarg_segment_ptr()), "v"(threadIdx.x), "s"(blockIdx.x)
  : "v1", "v2", "v3", "v4", "v5", "v6", "v7", "v8", "v9", "v10", "v11", "v12", "v13", "v14", "v15", "v16", "v17", "v18", "v19", "v20", "v21", "v22", "v23", "v24", "v25", "v26", "v27", "v28", "v29", "v30", "v31", "v32", "v33", "v34", "v35", "v36", "v37", "v38", "v39", "v40", "v41", "v42", "v43", "v44", "v45", "v46", "v47", "v48", "v49", "v50", "v51", "v52", "v53", "v54", "v55", "v56", "v57", "v58", "v59", "v60", "v61", "v62", "v63", "v64", "v65", "v66", "v67", "v68", "v69", "v70", "v71", "v72", "v73", "v74", "v75", "v76", "v77", "v78", "v79", "v80", "v81", "v82", "v83", "v84", "v85", "v86", "v87", "v88", "v89", "v90", "v91", "v92", "v93", "v94", "v95", "v96", "v97", "v98", "v99", "v100", "v101", "v102", "v103", "v104", "v105", "v106", "v107", "v108", "v109", "v110", "v111", "v112", "v113", "v114", "v115", "v116", "v117", "v118", "v119", "v120", "v121", "v122", "v123", "v124", "v125", "v126", "v127", "v128", "v129", "v130", "v131", "v132", "v133", "v134", "v135", "v136", "v137", "v138", "v139", "v140", "v141", "v142", "v143", "v144", "v145", "v146", "v147", "v148", "v149", "v150", "v151", "v152", "v153", "v154", "v155", "v156", "v157", "v158", "v159", "v160", "v161", "v162", "v163", "v164", "v165", "v166", "v167", "v168", "v169", "v170", "v171", "v172", "v173", "v174", "v175", "v176", "v177", "v178", "v179", "v180", "v181", "v182", "v183", "v184", "v185", "v186", "v187", "v188", "v189", "v190", "v191", "v192", "v193", "v194", "v195", "v196", "v197", "v198", "v199", "v200", "v201", "v202", "v203", "v204", "v205", "v206", "v207", "v208", "v209", "v210", "v211", "v212", "v213", "v214", "v215", "v216", "v217", "v218", "v219", "v220", "v221", "v222", "v223", "v224", "v225", "v226", "v227", "v228", "v229", "v230", "v231", "v232", "v233", "v234", "v235", "v236", "v237", "v238", "v239", "v240", "v241", "v242", "v243", "v244", "v245", "v246", "v247", "v248", "v249", "v250", "v251", "v252", "v253", "v254", "v255", "a0", "a1", "a2", "a3", "a4", "a5", "a6", "a7", "a8", "a9", "a10", "a11", "a12", "a13", "a14", "a15", "a16", "a17", "a18", "a19", "a20", "a21", "a22", "a23", "a24", "a25", "a26", "a27", "a28", "a29", "a30", "a31", "a32", "a33", "a34", "a35", "a36", "a37", "a38", "a39", "a40", "a41", "a42", "a43", "a44", "a45", "a46", "a47", "a48", "a49", "a50", "a51", "a52", "a53", "a54", "a55", "a56", "a57", "a58", "a59", "a60", "a61", "a62", "a63", "a64", "a65", "a66", "a67", "a68", "a69", "a70", "a71", "a72", "a73", "a74", "a75", "a76", "a77", "a78", "a79", "a80", "a81", "a82", "a83", "a84", "a85", "a86", "a87", "a88", "a89", "a90", "a91", "a92", "a93", "a94", "a95", "a96", "a97", "a98", "a99", "a100", "a101", "a102", "a103", "a104", "a105", "a106", "a107", "a108", "a109", "a110", "a111", "a112", "a113", "a114", "a115", "a116", "a117", "a118", "a119", "a120", "a121", "a122", "a123", "a124", "a125", "a126", "a127", "a128", "a129", "a130", "a131", "a132", "a133", "a134", "a135", "a136", "a137", "a138", "a139", "a140", "a141", "a142", "a143", "a144", "a145", "a146", "a147", "a148", "a149", "a150", "a151", "a152", "a153", "a154", "a155", "a156", "a157", "a158", "a159", "a160", "a161", "a162", "a163", "a164", "a165", "a166", "a167", "a168", "a169", "a170", "a171", "a172", "a173", "a174", "a175", "a176", "a177", "a178", "a179", "a180", "a181", "a182", "a183", "a184", "a185", "a186", "a187", "a188", "a189", "a190", "a191", "a192", "a193", "a194", "a195", "a196", "a197", "a198", "a199", "a200", "a201", "a202", "a203", "a204", "a205", "a206", "a207", "a208", "a209", "a210", "a211", "a212", "a213", "a214", "a215", "a216", "a217", "a218", "a219", "a220", "a221", "a222", "a223", "a224", "a225", "a226", "a227", "a228", "a229", "a230", "a231", "a232", "a233", "a234", "a235", "a236", "a237", "a238", "a239", "a240", "a241", "a242", "a243", "a244", "a245", "a246", "a247", "a248", "a249", "a250", "a251", "a252", "a253", "a254", "a255", "s3", "s4", "s5", "s6", "s7", "s8", "s9", "s10", "s11", "s12", "s13", "s14", "s15", "s16", "s17", "s18", "s19", "s20", "s21", "s22", "s23", "s24", "s25", "s26", "s27", "s28", "s29", "s30", "s31", "s36", "s37", "s38", "s39", "s40", "s41", "s42", "s43", "s44", "s45", "s46", "s47", "s48", "s49", "s50", "s51", "s52", "s53", "s54", "s55", "s56", "s57", "s58", "s59", "s60", "s61", "s62", "s63", "s64", "s65", "s66", "s67", "s68", "s69", "s70", "s71", "s72", "s73", "s74", "s75", "s76", "s77", "s78", "s79", "s80", "s81", "s82", "s83", "s84", "s85", "s86", "s87", "s88", "s89", "s90", "s91", "s92", "s93", "s94", "s95", "s96", "s97", "s98", "s99", "memory", "scc", "vcc");
}

extern "C" void kernel_launch(void* const* d_in, const int* in_sizes, int n_in, void* d_out, int out_size, void* d_ws, size_t ws_size, hipStream_t stream) {
  MhaArgs a;
  for (int i = 0; i < 10; ++i) a.p[i] = d_in[i];
  a.p[10] = d_out; a.p[11] = d_ws;
  hipLaunchKernelGGL(mha_fused, dim3(256), dim3(256), 0, stream, a);
}
```
